```python
import torch

def _softmax(x, axis=-1):
    return torch.softmax(x, dim=axis)

def _gelu(x):
    return torch.nn.functional.gelu(x, approximate='tanh')

def _silu(x):
    return torch.nn.functional.silu(x)

def _sigmoid(x):
    return torch.sigmoid(x)

def _layernorm(x, axis=-1, eps=1e-05):
    m = x.mean(dim=axis, keepdim=True)
    v = x.var(dim=axis, keepdim=True, unbiased=False)
    return (x - m) / torch.sqrt(v + eps)

def _scatter_add(tgt, idx, src, axis=0):
    return tgt.clone().index_add_(axis, idx, src)

def _segment_sum(src, seg, n):
    out = torch.zeros((n,) + src.shape[1:], dtype=src.dtype)
    return out.index_add_(0, seg, src)

def _pad(x, pw):
    flat = tuple((v for pair in reversed(pw) for v in pair))
    return torch.nn.functional.pad(x, flat)
kernel_name = 'standalone__autoregressive_conv_wavernn__speech'

def setup_inputs():

    _w = lambda a: (lambda t: t.to(torch.bfloat16) if t.is_floating_point() else t)(torch.as_tensor(a))
    B, T, D, K = (2, 1000, 256, 31)
    return {'x': _w((lambda *s: torch.randn(*s, dtype=torch.bfloat16) * 0.05)(B, T, D)), 'w': _w((lambda *s: torch.randn(*s, dtype=torch.bfloat16) * 0.05)(K, D, 2 * D))}

def reference(x, w):
    B, T, D = x.shape
    K = w.shape[0]
    dil = 2
    pad = (K - 1) * dil
    xp = _pad(x, ((0, 0), (pad, 0), (0, 0)))
    h = torch.einsum('btd,de->bte', xp[:, 0:T, :], w[0])
    for i in range(1, K):
        h = h + torch.einsum('btd,de->bte', xp[:, i * dil:i * dil + T, :], w[i])
    f, g = (h[..., :D].clone(), h[..., D:].clone())
    return torch.tanh(f) * _sigmoid(g)
```

```cpp
#include <hip/hip_runtime.h>
#define LDS_BYTES 163840

__global__ __launch_bounds__(256, 1) void conv_gate_kernel(const void* x, const void* w, void* out, void* ws) {
  const unsigned bid = blockIdx.x, tid = threadIdx.x;
  asm volatile(
    "s_load_dwordx8 s[24:31], %[ka], 0x0\n"
    "s_xor_b32 s3, %[bid], 5\n"
    "v_readfirstlane_b32 s49, %[tid]\n"
    "v_and_b32 v1, 63, %[tid]\n"
    "s_and_b32 s33, s3, 3\n"
    "s_bfe_u32 s35, s3, 0x10003\n"
    "s_lshl1_add_u32 s35, s33, s35\n"
    "s_lshr_b32 s49, s49, 6\n"
    "s_getpc_b64 s[50:51]\n"
    "LS_%=:\n"
    "s_lshr_b32 s34, s33, 1\n"
    "s_mul_i32 s43, s35, 6144\n"
    "s_add_u32 s43, s43, LV0_%=-LS_%=\n"
    "s_add_u32 s52, s50, s43\n"
    "s_addc_u32 s53, s51, 0\n"
    "s_mul_i32 s44, s34, 12288\n"
    "s_add_u32 s44, s44, LP0_%=-LS_%=\n"
    "s_add_u32 s54, s50, s44\n"
    "s_addc_u32 s55, s51, 0\n"
    "s_lshl_b32 s36, s49, 12\n"
    "s_add_u32 s44, s44, s36\n"
    "s_mul_i32 s43, s33, 12288\n"
    "s_add_u32 s43, s43, LV0_%=-LS_%=\n"
    "s_cmp_eq_u32 s49, 3\n"
    "s_cselect_b32 s43, s43, s44\n"
    "s_cselect_b32 s36, 0x1000, 0\n"
    "s_lshr_b32 s44, s3, 3\n"
    "s_lshl_b32 s44, s44, 7\n"
    "s_add_u32 s43, s43, s44\n"
    "v_min_u32 v31, 2, v1\n"
    "v_mul_u32_u24 v31, s36, v31\n"
    "v_add_u32 v31, s43, v31\n"
    "v_mov_b32 v59, v31\n"
    "global_load_dword v31, v31, s[50:51]\n"
    "s_and_b32 s43, s3, 4\n"
    "s_lshl_b32 s43, s43, 6\n"
    "s_lshl_b32 s44, s33, 16\n"
    "s_or_b32 s43, s43, s44\n"
    "s_lshr_b32 s44, s3, 3\n"
    "s_lshl_b32 s44, s44, 10\n"
    "s_add_u32 s44, s44, s43\n"
    "s_lshl_b32 s36, s49, 1\n"
    "s_or_b32 s36, s36, s49\n"
    "s_and_b32 s36, s36, 5\n"
    "s_lshl_b32 s36, s36, 7\n"
    "s_add_u32 s44, s44, s36\n"
    "s_and_b32 s36, s3, 16\n"
    "s_lshl_b32 s36, s36, 3\n"
    "s_or_b32 s43, s43, s36\n"
    "s_and_b32 s36, s3, 8\n"
    "s_lshl_b32 s36, s36, 6\n"
    "s_or_b32 s43, s43, s36\n"
    "v_cmp_lt_u32 vcc, 30, v1\n"
    "v_lshl_add_u32 v4, v1, 18, s44\n"
    "v_mov_b32 v33, 4286873600\n"
    "v_bfrev_b32 v34, 1\n"
    "v_cndmask_b32 v33, 0, v33, vcc\n"
    "v_add_u32 v4, v4, v33\n"
    "v_subrev_u32 v33, 0, v1\n"
    "v_cmp_gt_u32 vcc, 16, v33\n"
    "s_nop 1\n"
    "v_cndmask_b32 v56, v34, v4, vcc\n"
    "v_subrev_u32 v33, 16, v1\n"
    "v_cmp_gt_u32 vcc, 22, v33\n"
    "s_nop 1\n"
    "v_cndmask_b32 v57, v34, v4, vcc\n"
    "v_subrev_u32 v33, 38, v1\n"
    "v_cmp_gt_u32 vcc, 24, v33\n"
    "s_nop 1\n"
    "v_cndmask_b32 v58, v34, v4, vcc\n"
    "v_lshrrev_b32 v32, 3, v1\n"
    "v_and_b32 v33, 7, v1\n"
    "v_lshlrev_b32 v33, 4, v33\n"
    "v_lshl_or_b32 v2, v32, 10, v33\n"
    "v_lshl_or_b32 v35, v32, 11, v33\n"
    "s_lshl_b32 s44, s49, 13\n"
    "s_add_u32 s43, s43, s44\n"
    "s_and_b32 s44, s49, 1\n"
    "s_lshl_b32 s44, s44, 6\n"
    "v_and_b32 v32, 16, v1\n"
    "v_lshl_or_b32 v32, v32, 1, s44\n"
    "v_xad_u32 v3, v2, v32, s43\n"
    "s_bfe_u32 s43, s3, 0x10005\n"
    "s_lshl4_add_u32 s43, s43, s49\n"
    "s_lshl_b32 s42, s43, 14\n"
    "s_bfe_u32 s43, s3, 0x10006\n"
    "s_lshl_b32 s43, s43, 9\n"
    "s_lshl_b32 s44, s33, 7\n"
    "s_or_b32 s43, s43, s44\n"
    "s_add_u32 s42, s42, s43\n"
    "s_sub_u32 s42, s42, 30720\n"
    "s_and_b32 s43, s49, 2\n"
    "s_lshl_b32 s44, s43, 4\n"
    "v_xor_b32 v32, s44, v35\n"
    "v_xor_b32 v33, 64, v32\n"
    "v_add_u32 v5, s42, v32\n"
    "s_add_u32 s44, s42, 65536\n"
    "v_add_u32 v6, s44, v33\n"
    "s_add_u32 s44, s42, 131072\n"
    "v_xad_u32 v7, v32, 16, s44\n"
    "s_add_u32 s44, s42, 196608\n"
    "v_xad_u32 v8, v33, 16, s44\n"
    "s_lshl_b32 s43, s43, 29\n"
    "s_add_u32 s43, s43, 262144\n"
    "v_add_u32 v9, s43, v5\n"
    "s_bfe_u32 s43, s3, 0x20003\n"
    "s_mul_i32 s43, s43, 36\n"
    "s_add_u32 s43, s43, s49\n"
    "s_lshl_b32 s43, s43, 11\n"
    "s_add_u32 s43, s43, s42\n"
    "s_sub_u32 s43, s43, 1024\n"
    "v_min_u32 v17, 9, v1\n"
    "v_lshl_add_u32 v17, v17, 11, s43\n"
    "s_mul_i32 s38, s49, 1152\n"
    "s_add_u32 s7, s38, 18432\n"
    "s_cmp_lt_u32 s49, 2\n"
    "s_cselect_b32 s7, s7, 0x30000\n"
    "s_lshl_b32 s37, s49, 10\n"
    "s_add_u32 s37, s37, 62208\n"
    "s_bfe_u32 s43, s3, 0x10007\n"
    "s_mul_i32 s43, s43, 512000\n"
    "s_mov_b32 s14, 8126464\n"
    "s_mov_b32 s15, 0x20000\n"
    "s_mov_b32 s10, 512000\n"
    "s_mov_b32 s11, 0x20000\n"
    "s_waitcnt lgkmcnt(0)\n"
    "s_mov_b32 s12, s26\n"
    "s_and_b32 s13, s27, 0xffff\n"
    "s_add_u32 s8, s24, s43\n"
    "s_addc_u32 s9, s25, 0\n"
    "s_and_b32 s9, s9, 0xffff\n"
    "s_lshl_b32 s36, s34, 11\n"
    "s_sub_u32 s36, 1024, s36\n"
    "s_add_u32 m0, s38, 0\n"
    "s_nop 0\n"
    "buffer_load_dwordx4 v5, s[8:11], 0 offen lds\n"
    "s_add_u32 m0, s38, 4608\n"
    "s_nop 0\n"
    "buffer_load_dwordx4 v6, s[8:11], 0 offen lds\n"
    "s_add_u32 m0, s38, 9216\n"
    "s_nop 0\n"
    "buffer_load_dwordx4 v7, s[8:11], 0 offen lds\n"
    "s_add_u32 m0, s38, 13824\n"
    "s_nop 0\n"
    "buffer_load_dwordx4 v8, s[8:11], 0 offen lds\n"
    "s_add_u32 s20, s28, s43\n"
    "s_addc_u32 s21, s29, 0\n"
    "s_and_b32 s21, s21, 0xffff\n"
    "s_mov_b32 s22, 512000\n"
    "s_mov_b32 s23, 0x20000\n"
    "s_mov_b32 s16, s30\n"
    "s_and_b32 s17, s31, 0xffff\n"
    "s_add_u32 m0, s7, 0\n"
    "s_nop 0\n"
    "buffer_load_dwordx4 v9, s[8:11], 0 offen lds\n"
    "s_mov_b32 s18, 8667136\n"
    "s_mov_b32 s19, 0x20000\n"
    "s_lshr_b32 s43, s3, 4\n"
    "s_bfe_u32 s44, s3, 0x10002\n"
    "s_lshl4_add_u32 s43, s44, s43\n"
    "s_lshl_b32 s39, s43, 18\n"
    "s_mul_i32 s44, s35, 36864\n"
    "buffer_load_dword v30, v56, s[12:15], 0 offen\n"
    "s_add_u32 s39, s39, s44\n"
    "s_lshl2_add_u32 s43, s43, s49\n"
    "s_lshl_b32 s40, s43, 11\n"
    "s_add_u32 s40, s40, 8388608\n"
    "s_lshl_b32 s41, s43, 7\n"
    "s_add_u32 s41, s41, 8650752\n"
    "v_lshlrev_b32 v22, 4, %[tid]\n"
    "buffer_load_dword v30, v17, s[8:11], 0 offen\n"
    "v_and_b32 v32, 7, v1\n"
    "v_lshlrev_b32 v23, 7, v32\n"
    "v_cmp_eq_u32 vcc, s35, v1\n"
    "v_bfrev_b32 v33, 1\n"
    "v_and_b32 v34, 15, v1\n"
    "v_cndmask_b32 v24, v33, v23, vcc\n"
    "v_lshrrev_b32 v35, 4, v1\n"
    "s_add_u32 m0, s37, 40960\n"
    "s_mov_b32 s24, 0\n"
    "buffer_load_dwordx4 v3, s[12:15], s24 offen lds\n"
    "v_lshrrev_b32 v60, 2, v34\n"
    "v_cmp_eq_u32 vcc, v60, v35\n"
    "v_and_b32 v60, 1, v34\n"
    "v_lshlrev_b32 v60, 4, v60\n"
    "v_mov_b32 v61, 0x3f80\n"
    "v_lshlrev_b32 v61, v60, v61\n"
    "v_cndmask_b32 v61, 0, v61, vcc\n"
    "s_add_u32 m0, s37, 45056\n"
    "s_mov_b32 s24, 262144\n"
    "buffer_load_dwordx4 v3, s[12:15], s24 offen lds\n"
    "v_and_b32 v60, 2, v34\n"
    "v_cmp_eq_u32 vcc, 0, v60\n"
    "v_and_b32 v62, 8, v1\n"
    "v_xor_b32 v62, 8, v62\n"
    "v_cndmask_b32 v36, 0, v61, vcc\n"
    "v_cndmask_b32 v37, v61, 0, vcc\n"
    "v_mov_b32 v38, v36\n"
    "s_add_u32 m0, s37, 49152\n"
    "s_mov_b32 s24, 524288\n"
    "buffer_load_dwordx4 v3, s[12:15], s24 offen lds\n"
    "v_mov_b32 v39, v37\n"
    "v_lshrrev_b32 v62, 1, v62\n"
    "v_xor_b32 v34, v34, v62\n"
    "s_movk_i32 s43, 1152\n"
    "v_add_u32 v60, 0, v34\n"
    "v_bfe_u32 v61, v60, 3, 1\n"
    "v_and_or_b32 v61, v60, 6, v61\n"
    "s_add_u32 m0, s37, 53248\n"
    "s_mov_b32 s24, 786432\n"
    "buffer_load_dwordx4 v3, s[12:15], s24 offen lds\n"
    "v_xor_b32 v61, v61, v35\n"
    "v_lshlrev_b32 v61, 4, v61\n"
    "v_mad_u32_u24 v10, v60, s43, v61\n"
    "v_xor_b32 v13, 64, v10\n"
    "v_add_u32 v60, 1, v34\n"
    "v_bfe_u32 v61, v60, 3, 1\n"
    "v_and_or_b32 v61, v60, 6, v61\n"
    "s_add_u32 m0, s37, 57344\n"
    "s_mov_b32 s24, 1048576\n"
    "buffer_load_dwordx4 v3, s[12:15], s24 offen lds\n"
    "v_xor_b32 v61, v61, v35\n"
    "v_lshlrev_b32 v61, 4, v61\n"
    "v_mad_u32_u24 v11, v60, s43, v61\n"
    "v_xor_b32 v14, 64, v11\n"
    "v_add_u32 v60, 2, v34\n"
    "v_bfe_u32 v61, v60, 3, 1\n"
    "v_and_or_b32 v61, v60, 6, v61\n"
    "s_add_u32 m0, s38, 20736\n"
    "v_add_u32 v5, s36, v5\n"
    "buffer_load_dwordx4 v5, s[8:11], 0 offen lds\n"
    "v_xor_b32 v61, v61, v35\n"
    "v_lshlrev_b32 v61, 4, v61\n"
    "v_mad_u32_u24 v12, v60, s43, v61\n"
    "v_xor_b32 v15, 64, v12\n"
    "v_bfe_u32 v60, v1, 2, 2\n"
    "v_lshl_add_u32 v60, v35, 3, v60\n"
    "v_bfe_u32 v61, v1, 3, 2\n"
    "s_add_u32 m0, s38, 25344\n"
    "v_add_u32 v6, s36, v6\n"
    "buffer_load_dwordx4 v6, s[8:11], 0 offen lds\n"
    "v_xor_b32 v61, s49, v61\n"
    "v_lshlrev_b32 v61, 5, v61\n"
    "v_lshl_add_u32 v60, v60, 7, v61\n"
    "v_and_b32 v61, 3, v1\n"
    "v_lshl_add_u32 v60, v61, 3, v60\n"
    "v_add_u32 v20, 62208, v60\n"
    "v_add_u32 v21, 127744, v60\n"
    "s_add_u32 m0, s38, 29952\n"
    "v_add_u32 v7, s36, v7\n"
    "buffer_load_dwordx4 v7, s[8:11], 0 offen lds\n"
    "s_bfe_u32 s43, s3, 0x10005\n"
    "s_lshl_b32 s43, s43, 6\n"
    "s_and_b32 s44, s35, 3\n"
    "s_add_u32 s43, s43, s44\n"
    "s_lshl2_add_u32 s43, s43, s34\n"
    "s_lshl_b32 s43, s43, 10\n"
    "s_bfe_u32 s44, s3, 0x10006\n"
    "s_add_u32 m0, s38, 34560\n"
    "v_add_u32 v8, s36, v8\n"
    "buffer_load_dwordx4 v8, s[8:11], 0 offen lds\n"
    "s_lshl_b32 s44, s44, 9\n"
    "s_add_u32 s43, s43, s44\n"
    "s_and_b32 s44, s3, 4\n"
    "s_lshl_b32 s44, s44, 6\n"
    "s_add_u32 s43, s43, s44\n"
    "s_and_b32 s44, s3, 16\n"
    "s_lshl_b32 s44, s44, 3\n"
    "s_add_u32 m0, s7, 20736\n"
    "v_add_u32 v9, s36, v9\n"
    "buffer_load_dwordx4 v9, s[8:11], 0 offen lds\n"
    "s_add_u32 s43, s43, s44\n"
    "s_lshl_b32 s44, s49, 5\n"
    "s_add_u32 s43, s43, s44\n"
    "v_lshl_add_u32 v25, v34, 14, s43\n"
    "v_lshl_add_u32 v25, v35, 3, v25\n"
    "v_mov_b32 v196, v36\n"
    "v_mov_b32 v197, v37\n"
    "s_add_u32 m0, s37, 61440\n"
    "s_mov_b32 s24, 1310720\n"
    "buffer_load_dwordx4 v3, s[12:15], s24 offen lds\n"
    "v_lshl_or_b32 v198, v36, 2, v36\n"
    "v_lshl_or_b32 v199, v37, 2, v37\n"
    "s_mov_b32 s43, 0xbfffbfff\n"
    "v_and_b32 v198, s43, v198\n"
    "v_and_b32 v199, s43, v199\n"
    "s_and_b32 s44, s49, 1\n"
    "s_lshl_b32 s44, s44, 1\n"
    "s_add_u32 m0, s37, 65536\n"
    "s_mov_b32 s24, 1572864\n"
    "buffer_load_dwordx4 v3, s[12:15], s24 offen lds\n"
    "s_movk_i32 s43, 1152\n"
    "s_and_b32 s42, s49, 2\n"
    "s_lshl_b32 s42, s42, 8\n"
    "v_lshrrev_b32 v240, 2, v1\n"
    "v_bfe_u32 v241, v240, 3, 1\n"
    "v_and_or_b32 v241, v240, 6, v241\n"
    "v_bfe_u32 v242, v1, 1, 1\n"
    "s_add_u32 m0, s37, 69632\n"
    "s_mov_b32 s24, 1835008\n"
    "buffer_load_dwordx4 v3, s[12:15], s24 offen lds\n"
    "v_or_b32 v242, s44, v242\n"
    "v_and_b32 v243, 1, v1\n"
    "v_lshlrev_b32 v243, 3, v243\n"
    "v_xor_b32 v241, v241, v242\n"
    "v_lshl_or_b32 v241, v241, 4, v243\n"
    "v_mad_u32_u24 v241, v240, s43, v241\n"
    "v_add_u32 v18, s42, v241\n"
    "s_add_u32 m0, s37, 73728\n"
    "s_mov_b32 s24, 2097152\n"
    "buffer_load_dwordx4 v3, s[12:15], s24 offen lds\n"
    "v_xor_b32 v19, 64, v18\n"
    "s_lshr_b32 s25, s49, 1\n"
    "v_add_u32 v240, s25, v240\n"
    "v_bfe_u32 v241, v240, 3, 1\n"
    "v_and_or_b32 v241, v240, 6, v241\n"
    "v_xor_b32 v241, v241, v242\n"
    "v_lshl_or_b32 v241, v241, 4, v243\n"
    "s_add_u32 m0, s37, 77824\n"
    "s_mov_b32 s24, 2359296\n"
    "buffer_load_dwordx4 v3, s[12:15], s24 offen lds\n"
    "s_sub_u32 s26, 512, s42\n"
    "v_mad_u32_u24 v241, v240, s43, v241\n"
    "v_add_u32 v2, s26, v241\n"
    "v_xor_b32 v4, 64, v2\n"
    "s_lshr_b32 s25, s49, 1\n"
    "s_mul_i32 s25, s25, 1152\n"
    "s_lshl_b32 s26, s44, 8\n"
    "s_add_u32 m0, s37, 81920\n"
    "s_mov_b32 s24, 2621440\n"
    "buffer_load_dwordx4 v3, s[12:15], s24 offen lds\n"
    "s_add_u32 s25, s25, s26\n"
    "s_add_u32 s25, s25, 18432\n"
    "v_lshl_add_u32 v62, v1, 3, s25\n"
    "v_and_b32 v244, 16, v1\n"
    "v_lshlrev_b32 v244, 3, v244\n"
    "v_sub_u32 v63, 128, v244\n"
    "v_add_u32 v63, v63, v62\n"
    "s_add_u32 m0, s37, 86016\n"
    "s_mov_b32 s24, 2883584\n"
    "buffer_load_dwordx4 v3, s[12:15], s24 offen lds\n"
    "v_add_u32 v16, v244, v62\n"
    "s_lshl_b32 s26, s44, 6\n"
    "v_cmp_lt_u32 vcc, 47, v1\n"
    "v_mov_b32 v244, s26\n"
    "s_nop 0\n"
    "v_cndmask_b32 v244, 0, v244, vcc\n"
    "v_add_u32 v16, v16, v244\n"
    "s_add_u32 m0, s37, 90112\n"
    "s_mov_b32 s24, 3145728\n"
    "buffer_load_dwordx4 v3, s[12:15], s24 offen lds\n"
    "v_mov_b32 v252, v198\n"
    "v_mov_b32 v253, v199\n"
    "v_mov_b32 v254, v36\n"
    "v_mov_b32 v255, v37\n"
    "v_mov_b32 v234, 0\n"
    "v_mov_b32 v235, 0\n"
    "v_mov_b32 v236, v36\n"
    "s_add_u32 m0, s37, 94208\n"
    "s_mov_b32 s24, 3407872\n"
    "buffer_load_dwordx4 v3, s[12:15], s24 offen lds\n"
    "v_mov_b32 v237, v37\n"
    "v_mov_b32 v238, 0\n"
    "v_mov_b32 v239, 0\n"
    "v_and_b32 v240, 15, v1\n"
    "v_bfe_u32 v241, v240, 3, 1\n"
    "v_and_or_b32 v241, v240, 6, v241\n"
    "v_lshrrev_b32 v242, 5, v1\n"
    "s_add_u32 m0, s37, 0\n"
    "s_mov_b32 s24, 3670016\n"
    "buffer_load_dwordx4 v3, s[12:15], s24 offen lds\n"
    "v_or_b32 v242, s44, v242\n"
    "v_bfe_u32 v243, v1, 4, 1\n"
    "v_lshlrev_b32 v243, 3, v243\n"
    "v_xor_b32 v241, v241, v242\n"
    "v_lshl_or_b32 v241, v241, 4, v243\n"
    "v_mad_u32_u24 v241, v240, s43, v241\n"
    "v_add_u32 v60, s42, v241\n"
    "s_add_u32 m0, s37, 4096\n"
    "s_mov_b32 s24, 3932160\n"
    "buffer_load_dwordx4 v3, s[12:15], s24 offen lds\n"
    "v_xor_b32 v61, 64, v60\n"
    "s_mov_b32 s45, 0xbfb8aa3b\n"
    "s_mov_b32 s46, 0xc038aa3b\n"
    "s_brev_b32 s47, -2\n"
    "s_mov_b32 s48, 0xffff0000\n"
    "v_mov_b32 v32, -1\n"
    "v_mov_b32 v33, -1\n"
    "buffer_load_dword v30, v57, s[12:15], 0 offen\n"
    "v_mov_b32 v34, -1\n"
    "v_mov_b32 v35, -1\n"
    "s_xor_b32 s25, s39, 4096\n"
    "s_xor_b32 s26, s39, 8192\n"
    "s_xor_b32 s27, s39, 12288\n"
    "s_xor_b32 s28, s39, 16384\n"
    "s_xor_b32 s29, s39, 20480\n"
    "s_xor_b32 s30, s39, 24576\n"
    "s_xor_b32 s31, s39, 28672\n"
    "buffer_load_dwordx2 v[26:27], off, s[16:19], s41 sc1\n"
    "s_setpc_b64 s[54:55]\n"
    ".p2align 8\n"
    "LP0_%=:\n"
    "s_waitcnt vmcnt(11)\n"
    "s_barrier\n"
    "ds_read_b128 a[176:179], v10 offset:0\n"
    "ds_read_b64_tr_b16 a[130:131], v20 offset:40960\n"
    "ds_read_b64_tr_b16 a[132:133], v20 offset:41472\n"
    "ds_read_b128 a[180:183], v10 offset:128\n"
    "ds_read_b64_tr_b16 a[138:139], v20 offset:49152\n"
    "ds_read_b64_tr_b16 a[140:141], v20 offset:49664\n"
    "ds_read_b64_tr_b16 a[136:137], v20 offset:45056\n"
    "ds_read_b64_tr_b16 a[142:143], v20 offset:45568\n"
    "ds_read_b128 a[184:187], v10 offset:256\n"
    "ds_read_b128 a[188:191], v10 offset:384\n"
    "ds_read_b64_tr_b16 v[200:201], v18 offset:20736\n"
    "ds_read_b64_tr_b16 v[202:203], v18 offset:0\n"
    "ds_read_b64 v[216:217], v62 offset:20736\n"
    "s_waitcnt lgkmcnt(10)\n"
    "v_mfma_f32_16x16x32_bf16 v[64:67], a[130:133], a[176:179], 0\n"
    "s_waitcnt lgkmcnt(9)\n"
    "ds_read_b64 v[218:219], v62\n"
    "ds_read_b128 a[176:179], v10 offset:512\n"
    "v_mfma_f32_16x16x32_bf16 v[68:71], a[130:133], a[180:183], 0\n"
    "s_waitcnt lgkmcnt(8)\n"
    "ds_read_b64_tr_b16 v[204:205], v18 offset:20864\n"
    "ds_read_b64_tr_b16 v[206:207], v18 offset:128\n"
    "v_mfma_f32_16x16x32_bf16 v[176:179], v[36:39], a[136:139], 0\n"
    "s_waitcnt lgkmcnt(9)\n"
    "ds_read_b128 a[192:195], v10 offset:640\n"
    "ds_read_b64_tr_b16 v[208:209], v18 offset:20992\n"
    "v_mfma_f32_16x16x32_bf16 v[180:183], v[36:39], a[140:143], 0\n"
    "s_waitcnt lgkmcnt(10)\n"
    "ds_read_b64_tr_b16 v[210:211], v18 offset:256\n"
    "ds_read_b128 a[196:199], v10 offset:768\n"
    "v_mfma_f32_16x16x32_bf16 v[72:75], a[130:133], a[184:187], 0\n"
    "s_waitcnt lgkmcnt(11)\n"
    "ds_read_b64_tr_b16 v[212:213], v18 offset:21120\n"
    "ds_read_b64_tr_b16 v[214:215], v18 offset:384\n"
    "v_mfma_f32_16x16x32_bf16 v[76:79], a[130:133], a[188:191], 0\n"
    "s_waitcnt lgkmcnt(11)\n"
    "ds_read_b128 a[200:203], v10 offset:896\n"
    "ds_read_b64_tr_b16 a[146:147], v20 offset:57344\n"
    "v_mfma_f32_16x16x32_bf16 v[128:131], v[200:203], v[196:199], 0\n"
    "s_waitcnt lgkmcnt(10)\n"
    "ds_read_b64_tr_b16 a[148:149], v20 offset:57856\n"
    "ds_read_b64_tr_b16 a[144:145], v20 offset:53248\n"
    "v_cvt_pk_bf16_f32 v52, v176, v177\n"
    "v_lshlrev_b32 v240, 16, v216\n"
    "v_and_b32 v241, s48, v216\n"
    "v_mfma_f32_16x16x32_bf16 v[80:83], a[130:133], a[176:179], 0\n"
    "s_waitcnt lgkmcnt(10)\n"
    "ds_read_b64_tr_b16 a[150:151], v20 offset:53760\n"
    "ds_read_b64_tr_b16 v[220:221], v18\n"
    "v_cvt_pk_bf16_f32 v53, v178, v179\n"
    "v_lshlrev_b32 v242, 16, v217\n"
    "v_and_b32 v243, s48, v217\n"
    "v_lshlrev_b32 v244, 16, v218\n"
    "v_mfma_f32_16x16x32_bf16 v[132:135], v[204:207], v[196:199], 0\n"
    "s_waitcnt lgkmcnt(11)\n"
    "ds_read_b64_tr_b16 v[222:223], v18 offset:128\n"
    "ds_read_b64 v[230:231], v16\n"
    "v_cvt_pk_bf16_f32 v54, v180, v181\n"
    "v_and_b32 v245, s48, v218\n"
    "v_lshlrev_b32 v246, 16, v219\n"
    "v_and_b32 v247, s48, v219\n"
    "v_mfma_f32_16x16x32_bf16 v[84:87], a[130:133], a[192:195], 0\n"
    "s_waitcnt lgkmcnt(11)\n"
    "ds_read_b64 v[232:233], v63\n"
    "ds_read_b64_tr_b16 v[224:225], v18 offset:256\n"
    "v_cvt_pk_bf16_f32 v55, v182, v183\n"
    "v_sub_f32 v240, v240, v244\n"
    "v_sub_f32 v241, v241, v245\n"
    "v_sub_f32 v242, v242, v246\n"
    "v_mfma_f32_16x16x32_bf16 v[136:139], v[208:211], v[196:199], 0\n"
    "s_waitcnt lgkmcnt(12)\n"
    "v_sub_f32 v243, v243, v247\n"
    "v_cvt_pk_bf16_f32 v240, v240, v241\n"
    "v_cvt_pk_bf16_f32 v241, v242, v243\n"
    "v_mfma_f32_16x16x32_bf16 v[88:91], a[130:133], a[196:199], 0\n"
    "s_waitcnt lgkmcnt(10)\n"
    "ds_read_b64_tr_b16 v[226:227], v18 offset:384\n"
    "ds_write_b64 v62, v[240:241] offset:20736\n"
    "v_cvt_pk_bf16_f32 v128, v128, v129\n"
    "v_cvt_pk_bf16_f32 v129, v130, v131\n"
    "v_mfma_f32_16x16x32_bf16 v[140:143], v[212:215], v[196:199], 0\n"
    "s_waitcnt lgkmcnt(11)\n"
    "ds_read_b64_tr_b16 v[228:229], v2\n"
    "ds_write_b64 v60, v[128:129] offset:20736\n"
    "v_cvt_pk_bf16_f32 v132, v132, v133\n"
    "v_mfma_f32_16x16x32_bf16 v[92:95], a[130:133], a[200:203], 0\n"
    "s_waitcnt lgkmcnt(10)\n"
    "ds_read_b128 a[204:207], v11 offset:0\n"
    "v_cvt_pk_bf16_f32 v133, v134, v135\n"
    "v_mfma_f32_16x16x32_bf16 v[184:187], v[36:39], a[144:147], 0\n"
    "s_waitcnt lgkmcnt(10)\n"
    "ds_read_b64_tr_b16 a[154:155], v21 offset:0\n"
    "ds_write_b64 v60, v[132:133] offset:20864\n"
    "v_mfma_f32_16x16x32_bf16 v[188:191], v[36:39], a[148:151], 0\n"
    "ds_read_b64_tr_b16 a[156:157], v21 offset:512\n"
    "v_cvt_pk_bf16_f32 v136, v136, v137\n"
    "v_mfma_f32_16x16x32_bf16 v[64:67], v[52:55], a[180:183], v[64:67]\n"
    "ds_read_b64_tr_b16 a[152:153], v20 offset:61440\n"
    "v_cvt_pk_bf16_f32 v137, v138, v139\n"
    "v_mfma_f32_16x16x32_bf16 v[68:71], v[52:55], a[184:187], v[68:71]\n"
    "s_waitcnt lgkmcnt(12)\n"
    "ds_read_b64_tr_b16 a[158:159], v20 offset:61952\n"
    "ds_write_b64 v60, v[136:137] offset:20992\n"
    "v_mfma_f32_16x16x32_bf16 v[144:147], v[220:223], v[196:199], 0\n"
    "v_cvt_pk_bf16_f32 v48, v184, v185\n"
    "v_cvt_pk_bf16_f32 v140, v140, v141\n"
    "v_mfma_f32_16x16x32_bf16 v[72:75], v[52:55], a[188:191], v[72:75]\n"
    "s_waitcnt lgkmcnt(11)\n"
    "v_cvt_pk_bf16_f32 v49, v186, v187\n"
    "v_cvt_pk_bf16_f32 v141, v142, v143\n"
    "v_mfma_f32_16x16x32_bf16 v[148:151], v[222:225], v[252:255], 0\n"
    "ds_write_b64 v60, v[140:141] offset:21120\n"
    "v_cvt_pk_bf16_f32 v50, v188, v189\n"
    "v_mfma_f32_16x16x32_bf16 v[76:79], v[52:55], a[176:179], v[76:79]\n"
    "s_waitcnt lgkmcnt(11)\n"
    "v_cvt_pk_bf16_f32 v51, v190, v191\n"
    "v_lshlrev_b32 v240, 16, v230\n"
    "v_and_b32 v241, s48, v230\n"
    "v_mfma_f32_16x16x32_bf16 v[152:155], v[224:227], v[196:199], 0\n"
    "v_lshlrev_b32 v242, 16, v231\n"
    "v_and_b32 v243, s48, v231\n"
    "v_lshlrev_b32 v244, 16, v232\n"
    "v_mfma_f32_16x16x32_bf16 v[80:83], v[52:55], a[192:195], v[80:83]\n"
    "s_waitcnt lgkmcnt(9)\n"
    "ds_read_b128 a[180:183], v11 offset:128\n"
    "v_and_b32 v245, s48, v232\n"
    "v_lshlrev_b32 v246, 16, v233\n"
    "v_mfma_f32_16x16x32_bf16 v[156:159], v[226:229], v[252:255], 0\n"
    "s_waitcnt vmcnt(5) lgkmcnt(1)\n"
    "s_barrier\n"
    "ds_read_b128 a[208:211], v10 offset:20736\n"
    "v_and_b32 v247, s48, v233\n"
    "v_mfma_f32_16x16x32_bf16 v[84:87], v[52:55], a[196:199], v[84:87]\n"
    "ds_read_b128 a[212:215], v10 offset:20864\n"
    "v_sub_f32 v240, v240, v244\n"
    "v_mfma_f32_16x16x32_bf16 v[88:91], v[52:55], a[200:203], v[88:91]\n"
    "ds_read_b128 a[216:219], v10 offset:20992\n"
    "v_sub_f32 v241, v241, v245\n"
    "v_mfma_f32_16x16x32_bf16 v[92:95], v[52:55], a[204:207], v[92:95]\n"
    "ds_read_b128 a[220:223], v10 offset:21120\n"
    "v_sub_f32 v242, v242, v246\n"
    "v_mfma_f32_16x16x32_bf16 v[40:43], v[36:39], a[152:155], 0\n"
    "ds_read_b128 a[224:227], v10 offset:21248\n"
    "v_sub_f32 v243, v243, v247\n"
    "v_mfma_f32_16x16x32_bf16 v[44:47], v[36:39], a[156:159], 0\n"
    "ds_read_b128 a[228:231], v10 offset:21376\n"
    "v_cvt_pk_bf16_f32 v240, v240, v241\n"
    "v_mfma_f32_16x16x32_bf16 v[64:67], v[48:51], a[184:187], v[64:67]\n"
    "ds_read_b128 a[184:187], v10 offset:21504\n"
    "v_cvt_pk_bf16_f32 v241, v242, v243\n"
    "v_mfma_f32_16x16x32_bf16 v[68:71], v[48:51], a[188:191], v[68:71]\n"
    "ds_read_b128 a[232:235], v10 offset:21632\n"
    "ds_write_b64 v62, v[240:241] offset:41472\n"
    "v_mfma_f32_16x16x32_bf16 v[72:75], v[48:51], a[176:179], v[72:75]\n"
    "ds_read_b64_tr_b16 a[162:163], v21 offset:8192\n"
    "v_cvt_pk_bf16_f32 v52, v40, v41\n"
    "v_cvt_pk_bf16_f32 v144, v144, v145\n"
    "v_mfma_f32_16x16x32_bf16 v[76:79], v[48:51], a[192:195], v[76:79]\n"
    "ds_read_b64_tr_b16 a[164:165], v21 offset:8704\n"
    "v_cvt_pk_bf16_f32 v53, v42, v43\n"
    "v_cvt_pk_bf16_f32 v145, v146, v147\n"
    "v_mfma_f32_16x16x32_bf16 v[80:83], v[48:51], a[196:199], v[80:83]\n"
    "ds_read_b64_tr_b16 a[160:161], v21 offset:4096\n"
    "ds_write_b64 v60, v[144:145] offset:41472\n"
    "v_cvt_pk_bf16_f32 v54, v44, v45\n"
    "v_mfma_f32_16x16x32_bf16 v[84:87], v[48:51], a[200:203], v[84:87]\n"
    "ds_read_b64_tr_b16 a[166:167], v21 offset:4608\n"
    "v_cvt_pk_bf16_f32 v55, v46, v47\n"
    "v_cvt_pk_bf16_f32 v148, v148, v149\n"
    "v_mfma_f32_16x16x32_bf16 v[88:91], v[48:51], a[204:207], v[88:91]\n"
    "s_waitcnt lgkmcnt(14)\n"
    "v_cvt_pk_bf16_f32 v149, v150, v151\n"
    "v_mfma_f32_16x16x32_bf16 v[92:95], v[48:51], a[180:183], v[92:95]\n"
    "s_waitcnt lgkmcnt(13)\n"
    "ds_write_b64 v60, v[148:149] offset:41600\n"
    "v_mfma_f32_16x16x32_bf16 v[96:99], a[130:133], a[208:211], 0\n"
    "s_waitcnt lgkmcnt(13)\n"
    "v_cvt_pk_bf16_f32 v152, v152, v153\n"
    "v_mfma_f32_16x16x32_bf16 v[100:103], a[130:133], a[212:215], 0\n"
    "s_waitcnt lgkmcnt(12)\n"
    "ds_read_b128 a[208:211], v11 offset:256\n"
    "v_cvt_pk_bf16_f32 v153, v154, v155\n"
    "v_mfma_f32_16x16x32_bf16 v[104:107], a[130:133], a[216:219], 0\n"
    "s_waitcnt lgkmcnt(12)\n"
    "s_add_u32 m0, s37, 8192\n"
    "ds_read_b128 a[236:239], v11 offset:20736\n"
    "ds_write_b64 v60, v[152:153] offset:41728\n"
    "v_mfma_f32_16x16x32_bf16 v[108:111], a[130:133], a[220:223], 0\n"
    "s_waitcnt lgkmcnt(13)\n"
    "s_mov_b32 s24, 4194304\n"
    "ds_read_b64_tr_b16 a[170:171], v21 offset:16384\n"
    "v_cvt_pk_bf16_f32 v156, v156, v157\n"
    "v_mfma_f32_16x16x32_bf16 v[112:115], a[130:133], a[224:227], 0\n"
    "s_waitcnt lgkmcnt(13)\n"
    "ds_read_b64_tr_b16 a[172:173], v21 offset:16896\n"
    "v_cvt_pk_bf16_f32 v157, v158, v159\n"
    "v_mfma_f32_16x16x32_bf16 v[116:119], a[130:133], a[228:231], 0\n"
    "s_waitcnt lgkmcnt(13)\n"
    "ds_read_b64_tr_b16 a[168:169], v21 offset:12288\n"
    "ds_write_b64 v60, v[156:157] offset:41856\n"
    "v_mfma_f32_16x16x32_bf16 v[120:123], a[130:133], a[184:187], 0\n"
    "s_waitcnt lgkmcnt(14)\n"
    "ds_read_b64_tr_b16 a[174:175], v21 offset:12800\n"
    "v_mfma_f32_16x16x32_bf16 v[124:127], a[130:133], a[232:235], 0\n"
    "s_waitcnt lgkmcnt(11)\n"
    "ds_read_b64_tr_b16 a[20:21], v21 offset:12288\n"
    "v_mfma_f32_16x16x32_bf16 v[176:179], v[36:39], a[160:163], 0\n"
    "s_waitcnt lgkmcnt(10)\n"
    "ds_read_b64_tr_b16 a[22:23], v21 offset:12800\n"
    "v_mfma_f32_16x16x32_bf16 v[180:183], v[36:39], a[164:167], 0\n"
    "s_waitcnt vmcnt(2) lgkmcnt(3)\n"
    "s_barrier\n"
    "ds_read_b128 a[240:243], v10 offset:41984\n"
    "v_mfma_f32_16x16x32_bf16 v[64:67], v[52:55], a[188:191], v[64:67]\n"
    "ds_read_b128 a[188:191], v10 offset:42240\n"
    "v_mfma_f32_16x16x32_bf16 v[68:71], v[52:55], a[176:179], v[68:71]\n"
    "ds_read_b128 a[176:179], v11 offset:41472\n"
    "v_mfma_f32_16x16x32_bf16 v[72:75], v[52:55], a[192:195], v[72:75]\n"
    "buffer_load_dwordx4 v3, s[12:15], s24 offen lds\n"
    "v_mfma_f32_16x16x32_bf16 v[76:79], v[52:55], a[196:199], v[76:79]\n"
    "s_add_u32 m0, s37, 12288\n"
    "ds_read_b128 a[196:199], v11 offset:41728\n"
    "v_cvt_pk_bf16_f32 v48, v176, v177\n"
    "v_mfma_f32_16x16x32_bf16 v[80:83], v[52:55], a[200:203], v[80:83]\n"
    "s_mov_b32 s24, 4456448\n"
    "ds_read_b64_tr_b16 a[4:5], v20 offset:45056\n"
    "v_cvt_pk_bf16_f32 v49, v178, v179\n"
    "v_mfma_f32_16x16x32_bf16 v[84:87], v[52:55], a[204:207], v[84:87]\n"
    "ds_read_b64_tr_b16 a[6:7], v20 offset:45568\n"
    "v_cvt_pk_bf16_f32 v50, v180, v181\n"
    "v_mfma_f32_16x16x32_bf16 v[88:91], v[52:55], a[180:183], v[88:91]\n"
    "ds_read_b64_tr_b16 a[8:9], v20 offset:53248\n"
    "v_cvt_pk_bf16_f32 v51, v182, v183\n"
    "v_mfma_f32_16x16x32_bf16 v[92:95], v[52:55], a[208:211], v[92:95]\n"
    "buffer_load_dwordx4 v3, s[12:15], s24 offen lds\n"
    "v_mfma_f32_16x16x32_bf16 v[96:99], a[138:141], a[212:215], v[96:99]\n"
    "s_add_u32 m0, s37, 16384\n"
    "ds_read_b64_tr_b16 a[10:11], v20 offset:53760\n"
    "v_mfma_f32_16x16x32_bf16 v[100:103], a[138:141], a[216:219], v[100:103]\n"
    "s_mov_b32 s24, 4718592\n"
    "ds_read_b128 a[204:207], v11 offset:20864\n"
    "v_mfma_f32_16x16x32_bf16 v[104:107], a[138:141], a[220:223], v[104:107]\n"
    "ds_read_b64_tr_b16 a[130:131], v21 offset:24576\n"
    "v_mfma_f32_16x16x32_bf16 v[108:111], a[138:141], a[224:227], v[108:111]\n"
    "ds_read_b64_tr_b16 a[132:133], v21 offset:25088\n"
    "v_mfma_f32_16x16x32_bf16 v[112:115], a[138:141], a[228:231], v[112:115]\n"
    "buffer_load_dwordx4 v3, s[12:15], s24 offen lds\n"
    "v_mfma_f32_16x16x32_bf16 v[116:119], a[138:141], a[184:187], v[116:119]\n"
    "s_add_u32 m0, s37, 20480\n"
    "ds_read_b64_tr_b16 a[128:129], v21 offset:20480\n"
    "v_mfma_f32_16x16x32_bf16 v[120:123], a[138:141], a[232:235], v[120:123]\n"
    "s_mov_b32 s24, 4980736\n"
    "s_waitcnt lgkmcnt(10)\n"
    "ds_read_b64_tr_b16 a[134:135], v21 offset:20992\n"
    "v_mfma_f32_16x16x32_bf16 v[124:127], a[138:141], a[236:239], v[124:127]\n"
    "ds_read_b64_tr_b16 a[24:25], v21 offset:20480\n"
    "v_mfma_f32_16x16x32_bf16 v[184:187], v[36:39], a[168:171], 0\n"
    "ds_read_b64_tr_b16 a[26:27], v21 offset:20992\n"
    "v_mfma_f32_16x16x32_bf16 v[188:191], v[36:39], a[172:175], 0\n"
    "buffer_load_dwordx4 v3, s[12:15], s24 offen lds\n"
    "v_mfma_f32_16x16x32_bf16 v[64:67], v[48:51], a[240:243], v[64:67]\n"
    "s_add_u32 m0, s37, 24576\n"
    "ds_read_b128 a[208:211], v10 offset:42112\n"
    "v_mfma_f32_16x16x32_bf16 v[72:75], v[48:51], a[188:191], v[72:75]\n"
    "s_waitcnt lgkmcnt(13)\n"
    "s_mov_b32 s24, 5242880\n"
    "ds_read_b128 a[212:215], v10 offset:42368\n"
    "v_mfma_f32_16x16x32_bf16 v[80:83], v[48:51], a[176:179], v[80:83]\n"
    "s_waitcnt lgkmcnt(13)\n"
    "ds_read_b128 a[240:243], v11 offset:41600\n"
    "v_cvt_pk_bf16_f32 v52, v184, v185\n"
    "v_mfma_f32_16x16x32_bf16 v[88:91], v[48:51], a[196:199], v[88:91]\n"
    "ds_read_b128 a[244:247], v11 offset:41856\n"
    "v_cvt_pk_bf16_f32 v53, v186, v187\n"
    "v_mfma_f32_16x16x32_bf16 v[96:99], a[146:149], a[216:219], v[96:99]\n"
    "buffer_load_dwordx4 v3, s[12:15], s24 offen lds\n"
    "v_mfma_f32_16x16x32_bf16 v[100:103], a[146:149], a[220:223], v[100:103]\n"
    "s_add_u32 m0, s37, 28672\n"
    "s_waitcnt lgkmcnt(10)\n"
    "ds_read_b64_tr_b16 a[12:13], v20 offset:61440\n"
    "v_cvt_pk_bf16_f32 v54, v188, v189\n"
    "v_mfma_f32_16x16x32_bf16 v[104:107], a[146:149], a[224:227], v[104:107]\n"
    "s_mov_b32 s24, 5505024\n"
    "ds_read_b64_tr_b16 a[14:15], v20 offset:61952\n"
    "v_cvt_pk_bf16_f32 v55, v190, v191\n"
    "v_mfma_f32_16x16x32_bf16 v[108:111], a[146:149], a[228:231], v[108:111]\n"
    "ds_read_b64_tr_b16 a[16:17], v21 offset:4096\n"
    "v_add_f32 v176, v176, v184\n"
    "v_mfma_f32_16x16x32_bf16 v[112:115], a[146:149], a[184:187], v[112:115]\n"
    "ds_read_b64_tr_b16 a[18:19], v21 offset:4608\n"
    "v_add_f32 v177, v177, v185\n"
    "v_mfma_f32_16x16x32_bf16 v[116:119], a[146:149], a[232:235], v[116:119]\n"
    "buffer_load_dwordx4 v3, s[12:15], s24 offen lds\n"
    "v_mfma_f32_16x16x32_bf16 v[120:123], a[146:149], a[236:239], v[120:123]\n"
    "s_add_u32 m0, s37, 32768\n"
    "ds_read_b128 a[216:219], v11 offset:20992\n"
    "v_add_f32 v178, v178, v186\n"
    "v_mfma_f32_16x16x32_bf16 v[124:127], a[146:149], a[204:207], v[124:127]\n"
    "s_waitcnt lgkmcnt(12)\n"
    "s_mov_b32 s24, 5767168\n"
    "ds_read_b128 a[248:251], v11 offset:384\n"
    "v_mfma_f32_16x16x32_bf16 v[40:43], v[36:39], a[128:131], 0\n"
    "s_waitcnt lgkmcnt(12)\n"
    "ds_read_b64_tr_b16 a[138:139], v20 offset:0\n"
    "v_add_f32 v179, v179, v187\n"
    "v_mfma_f32_16x16x32_bf16 v[44:47], v[36:39], a[132:135], 0\n"
    "s_waitcnt lgkmcnt(10)\n"
    "ds_read_b64_tr_b16 a[140:141], v20 offset:512\n"
    "v_add_f32 v180, v180, v188\n"
    "v_mfma_f32_16x16x32_bf16 v[68:71], v[52:55], a[208:211], v[68:71]\n"
    "s_waitcnt lgkmcnt(10)\n"
    "buffer_load_dwordx4 v3, s[12:15], s24 offen lds\n"
    "v_mfma_f32_16x16x32_bf16 v[76:79], v[52:55], a[212:215], v[76:79]\n"
    "s_waitcnt lgkmcnt(9)\n"
    "s_add_u32 m0, s37, 36864\n"
    "ds_read_b64_tr_b16 a[136:137], v21 offset:28672\n"
    "v_mfma_f32_16x16x32_bf16 v[84:87], v[52:55], a[240:243], v[84:87]\n"
    "s_waitcnt lgkmcnt(9)\n"
    "s_mov_b32 s24, 6029312\n"
    "ds_read_b64_tr_b16 a[142:143], v21 offset:29184\n"
    "v_mfma_f32_16x16x32_bf16 v[92:95], v[52:55], a[244:247], v[92:95]\n"
    "ds_read_b64_tr_b16 a[28:29], v21 offset:28672\n"
    "v_add_f32 v181, v181, v189\n"
    "v_mfma_f32_16x16x32_bf16 v[96:99], a[154:157], a[220:223], v[96:99]\n"
    "ds_read_b64_tr_b16 a[30:31], v21 offset:29184\n"
    "v_add_f32 v182, v182, v190\n"
    "v_mfma_f32_16x16x32_bf16 v[100:103], a[154:157], a[224:227], v[100:103]\n"
    "buffer_load_dwordx4 v3, s[12:15], s24 offen lds\n"
    "v_mfma_f32_16x16x32_bf16 v[104:107], a[154:157], a[228:231], v[104:107]\n"
    "s_add_u32 m0, s37, 40960\n"
    "v_add_f32 v183, v183, v191\n"
    "v_cvt_pk_bf16_f32 v192, v176, v177\n"
    "v_mfma_f32_16x16x32_bf16 v[108:111], a[154:157], a[184:187], v[108:111]\n"
    "s_waitcnt vmcnt(6) lgkmcnt(5)\n"
    "s_barrier\n"
    "s_mov_b32 s24, 6291456\n"
    "v_mfma_f32_16x16x32_bf16 v[112:115], a[154:157], a[232:235], v[112:115]\n"
    "v_cvt_pk_bf16_f32 v193, v178, v179\n"
    "v_cvt_pk_bf16_f32 v194, v180, v181\n"
    "v_mfma_f32_16x16x32_bf16 v[116:119], a[154:157], a[236:239], v[116:119]\n"
    "ds_read_b128 a[208:211], v11 offset:41984\n"
    "v_cvt_pk_bf16_f32 v195, v182, v183\n"
    "v_mfma_f32_16x16x32_bf16 v[120:123], a[154:157], a[204:207], v[120:123]\n"
    "v_cvt_pk_bf16_f32 v48, v40, v41\n"
    "v_cvt_pk_bf16_f32 v49, v42, v43\n"
    "v_mfma_f32_16x16x32_bf16 v[124:127], a[154:157], a[216:219], v[124:127]\n"
    "v_cvt_pk_bf16_f32 v50, v44, v45\n"
    "v_cvt_pk_bf16_f32 v51, v46, v47\n"
    "v_mfma_f32_16x16x32_bf16 v[160:163], v[192:195], a[192:195], 0\n"
    "ds_read_b128 a[192:195], v11 offset:21120\n"
    "v_mfma_f32_16x16x32_bf16 v[164:167], v[192:195], a[200:203], 0\n"
    "ds_read_b64_tr_b16 a[146:147], v20 offset:8192\n"
    "v_mfma_f32_16x16x32_bf16 v[168:171], v[192:195], a[180:183], 0\n"
    "ds_read_b64_tr_b16 a[148:149], v20 offset:8704\n"
    "v_mfma_f32_16x16x32_bf16 v[172:175], v[192:195], a[248:251], 0\n"
    "s_waitcnt lgkmcnt(7)\n"
    "buffer_load_dwordx4 v3, s[12:15], s24 offen lds\n"
    "v_mfma_f32_16x16x32_bf16 v[176:179], v[36:39], a[136:139], 0\n"
    "s_waitcnt lgkmcnt(6)\n"
    "s_add_u32 m0, s37, 45056\n"
    "ds_read_b64_tr_b16 a[144:145], v20 offset:4096\n"
    "v_mfma_f32_16x16x32_bf16 v[180:183], v[36:39], a[140:143], 0\n"
    "s_mov_b32 s24, 6553600\n"
    "ds_read_b64_tr_b16 a[150:151], v20 offset:4608\n"
    "v_mfma_f32_16x16x32_bf16 v[64:67], v[48:51], a[188:191], v[64:67]\n"
    "ds_read_b64_tr_b16 a[32:33], v20 offset:4096\n"
    "v_mfma_f32_16x16x32_bf16 v[72:75], v[48:51], a[176:179], v[72:75]\n"
    "ds_read_b64_tr_b16 a[34:35], v20 offset:4608\n"
    "v_mfma_f32_16x16x32_bf16 v[80:83], v[48:51], a[196:199], v[80:83]\n"
    "s_waitcnt lgkmcnt(7)\n"
    "buffer_load_dwordx4 v3, s[12:15], s24 offen lds\n"
    "v_mfma_f32_16x16x32_bf16 v[88:91], v[48:51], a[208:211], v[88:91]\n"
    "s_add_u32 m0, s37, 49152\n"
    "v_cvt_pk_bf16_f32 v52, v176, v177\n"
    "v_cvt_pk_bf16_f32 v53, v178, v179\n"
    "v_mfma_f32_16x16x32_bf16 v[96:99], a[162:165], a[224:227], v[96:99]\n"
    "s_mov_b32 s24, 6815744\n"
    "v_cvt_pk_bf16_f32 v54, v180, v181\n"
    "v_cvt_pk_bf16_f32 v55, v182, v183\n"
    "v_mfma_f32_16x16x32_bf16 v[100:103], a[162:165], a[228:231], v[100:103]\n"
    "v_add_f32 v40, v40, v176\n"
    "v_add_f32 v41, v41, v177\n"
    "v_mfma_f32_16x16x32_bf16 v[104:107], a[162:165], a[184:187], v[104:107]\n"
    "ds_read_b128 a[188:191], v11 offset:42112\n"
    "v_add_f32 v42, v42, v178\n"
    "v_mfma_f32_16x16x32_bf16 v[108:111], a[162:165], a[232:235], v[108:111]\n"
    "buffer_load_dwordx4 v3, s[12:15], s24 offen lds\n"
    "v_mfma_f32_16x16x32_bf16 v[112:115], a[162:165], a[236:239], v[112:115]\n"
    "s_add_u32 m0, s37, 53248\n"
    "v_add_f32 v43, v43, v179\n"
    "v_add_f32 v44, v44, v180\n"
    "v_mfma_f32_16x16x32_bf16 v[116:119], a[162:165], a[204:207], v[116:119]\n"
    "s_mov_b32 s24, 7077888\n"
    "v_add_f32 v45, v45, v181\n"
    "v_add_f32 v46, v46, v182\n"
    "v_mfma_f32_16x16x32_bf16 v[120:123], a[162:165], a[216:219], v[120:123]\n"
    "s_waitcnt lgkmcnt(7)\n"
    "ds_read_b128 a[220:223], v11 offset:21248\n"
    "v_add_f32 v47, v47, v183\n"
    "v_mfma_f32_16x16x32_bf16 v[124:127], a[162:165], a[192:195], v[124:127]\n"
    "s_waitcnt lgkmcnt(5)\n"
    "ds_read_b128 a[224:227], v11 offset:640\n"
    "v_cvt_pk_bf16_f32 v192, v40, v41\n"
    "v_mfma_f32_16x16x32_bf16 v[184:187], v[36:39], a[144:147], 0\n"
    "s_waitcnt lgkmcnt(5)\n"
    "buffer_load_dwordx4 v3, s[12:15], s24 offen lds\n"
    "v_mfma_f32_16x16x32_bf16 v[188:191], v[36:39], a[148:151], 0\n"
    "s_waitcnt vmcnt(5) lgkmcnt(3)\n"
    "s_barrier\n"
    "ds_read_b64_tr_b16 a[154:155], v20 offset:16384\n"
    "v_mfma_f32_16x16x32_bf16 v[68:71], v[52:55], a[212:215], v[68:71]\n"
    "s_add_u32 m0, s37, 57344\n"
    "ds_read_b64_tr_b16 a[156:157], v20 offset:16896\n"
    "v_cvt_pk_bf16_f32 v193, v42, v43\n"
    "v_mfma_f32_16x16x32_bf16 v[76:79], v[52:55], a[240:243], v[76:79]\n"
    "s_mov_b32 s24, 7340032\n"
    "ds_read_b64_tr_b16 a[152:153], v20 offset:12288\n"
    "v_cvt_pk_bf16_f32 v194, v44, v45\n"
    "v_mfma_f32_16x16x32_bf16 v[84:87], v[52:55], a[244:247], v[84:87]\n"
    "s_waitcnt lgkmcnt(5)\n"
    "ds_read_b64_tr_b16 a[158:159], v20 offset:12800\n"
    "v_cvt_pk_bf16_f32 v195, v46, v47\n"
    "v_mfma_f32_16x16x32_bf16 v[92:95], v[52:55], a[188:191], v[92:95]\n"
    "buffer_load_dwordx4 v3, s[12:15], s24 offen lds\n"
    "v_mfma_f32_16x16x32_bf16 v[96:99], a[170:173], a[228:231], v[96:99]\n"
    "s_add_u32 m0, s37, 61440\n"
    "ds_read_b64_tr_b16 a[36:37], v20 offset:12288\n"
    "v_cvt_pk_bf16_f32 v48, v184, v185\n"
    "v_mfma_f32_16x16x32_bf16 v[100:103], a[170:173], a[184:187], v[100:103]\n"
    "s_mov_b32 s24, 7602176\n"
    "ds_read_b64_tr_b16 a[38:39], v20 offset:12800\n"
    "v_cvt_pk_bf16_f32 v49, v186, v187\n"
    "v_mfma_f32_16x16x32_bf16 v[104:107], a[170:173], a[232:235], v[104:107]\n"
    "v_cvt_pk_bf16_f32 v50, v188, v189\n"
    "v_cvt_pk_bf16_f32 v51, v190, v191\n"
    "v_mfma_f32_16x16x32_bf16 v[108:111], a[170:173], a[236:239], v[108:111]\n"
    "buffer_load_dwordx4 v3, s[12:15], s24 offen lds\n"
    "v_mfma_f32_16x16x32_bf16 v[112:115], a[170:173], a[204:207], v[112:115]\n"
    "s_add_u32 m0, s37, 65536\n"
    "v_mfma_f32_16x16x32_bf16 v[116:119], a[170:173], a[216:219], v[116:119]\n"
    "s_mov_b32 s24, 7864320\n"
    "v_mfma_f32_16x16x32_bf16 v[120:123], a[170:173], a[192:195], v[120:123]\n"
    "s_waitcnt lgkmcnt(7)\n"
    "ds_read_b128 a[212:215], v11 offset:42240\n"
    "v_mfma_f32_16x16x32_bf16 v[124:127], a[170:173], a[220:223], v[124:127]\n"
    "ds_read_b128 a[228:231], v11 offset:21376\n"
    "v_mfma_f32_16x16x32_bf16 v[160:163], v[192:195], a[200:203], v[160:163]\n"
    "ds_read_b64_tr_b16 a[162:163], v20 offset:24576\n"
    "v_mfma_f32_16x16x32_bf16 v[164:167], v[192:195], a[180:183], v[164:167]\n"
    "buffer_load_dwordx4 v3, s[12:15], s24 offen lds\n"
    "v_mfma_f32_16x16x32_bf16 v[168:171], v[192:195], a[248:251], v[168:171]\n"
    "s_waitcnt lgkmcnt(9)\n"
    "s_add_u32 m0, s37, 69632\n"
    "ds_read_b64_tr_b16 a[164:165], v20 offset:25088\n"
    "v_mfma_f32_16x16x32_bf16 v[172:175], v[192:195], a[224:227], v[172:175]\n"
    "s_waitcnt lgkmcnt(7)\n"
    "s_mov_b32 s24, 32768\n"
    "ds_read_b64_tr_b16 a[160:161], v20 offset:20480\n"
    "v_mfma_f32_16x16x32_bf16 v[40:43], v[36:39], a[152:155], 0\n"
    "s_waitcnt lgkmcnt(7)\n"
    "ds_read_b64_tr_b16 a[166:167], v20 offset:20992\n"
    "v_mfma_f32_16x16x32_bf16 v[44:47], v[36:39], a[156:159], 0\n"
    "ds_read_b64_tr_b16 a[40:41], v20 offset:20480\n"
    "v_mfma_f32_16x16x32_bf16 v[64:67], v[48:51], a[176:179], v[64:67]\n"
    "buffer_load_dwordx4 v3, s[12:15], s24 offen lds\n"
    "v_mfma_f32_16x16x32_bf16 v[72:75], v[48:51], a[196:199], v[72:75]\n"
    "s_add_u32 m0, s37, 73728\n"
    "ds_read_b64_tr_b16 a[42:43], v20 offset:20992\n"
    "v_mfma_f32_16x16x32_bf16 v[80:83], v[48:51], a[208:211], v[80:83]\n"
    "s_waitcnt lgkmcnt(7)\n"
    "s_mov_b32 s24, 294912\n"
    "v_cvt_pk_bf16_f32 v52, v40, v41\n"
    "v_mfma_f32_16x16x32_bf16 v[88:91], v[48:51], a[212:215], v[88:91]\n"
    "v_cvt_pk_bf16_f32 v53, v42, v43\n"
    "v_cvt_pk_bf16_f32 v54, v44, v45\n"
    "v_mfma_f32_16x16x32_bf16 v[96:99], a[130:133], a[184:187], v[96:99]\n"
    "v_cvt_pk_bf16_f32 v55, v46, v47\n"
    "v_add_f32 v184, v184, v40\n"
    "v_mfma_f32_16x16x32_bf16 v[100:103], a[130:133], a[232:235], v[100:103]\n"
    "buffer_load_dwordx4 v3, s[12:15], s24 offen lds\n"
    "v_mfma_f32_16x16x32_bf16 v[104:107], a[130:133], a[236:239], v[104:107]\n"
    "s_add_u32 m0, s37, 77824\n"
    "ds_read_b128 a[176:179], v11 offset:42368\n"
    "v_add_f32 v185, v185, v41\n"
    "v_mfma_f32_16x16x32_bf16 v[108:111], a[130:133], a[204:207], v[108:111]\n"
    "s_mov_b32 s24, 557056\n"
    "v_add_f32 v186, v186, v42\n"
    "v_add_f32 v187, v187, v43\n"
    "v_mfma_f32_16x16x32_bf16 v[112:115], a[130:133], a[216:219], v[112:115]\n"
    "ds_read_b128 a[184:187], v11 offset:21504\n"
    "v_add_f32 v188, v188, v44\n"
    "v_mfma_f32_16x16x32_bf16 v[116:119], a[130:133], a[192:195], v[116:119]\n"
    "ds_read_b128 a[200:203], v11 offset:896\n"
    "v_add_f32 v189, v189, v45\n"
    "v_mfma_f32_16x16x32_bf16 v[120:123], a[130:133], a[220:223], v[120:123]\n"
    "s_waitcnt lgkmcnt(9)\n"
    "buffer_load_dwordx4 v3, s[12:15], s24 offen lds\n"
    "v_mfma_f32_16x16x32_bf16 v[124:127], a[130:133], a[228:231], v[124:127]\n"
    "s_waitcnt lgkmcnt(6)\n"
    "s_add_u32 m0, s37, 81920\n"
    "ds_read_b64_tr_b16 a[170:171], v20 offset:32768\n"
    "v_mfma_f32_16x16x32_bf16 v[176:179], v[36:39], a[160:163], 0\n"
    "s_waitcnt lgkmcnt(6)\n"
    "s_mov_b32 s24, 819200\n"
    "ds_read_b64_tr_b16 a[172:173], v20 offset:33280\n"
    "v_mfma_f32_16x16x32_bf16 v[180:183], v[36:39], a[164:167], 0\n"
    "ds_read_b64_tr_b16 a[168:169], v20 offset:28672\n"
    "v_add_f32 v190, v190, v46\n"
    "v_mfma_f32_16x16x32_bf16 v[68:71], v[52:55], a[240:243], v[68:71]\n"
    "ds_read_b64_tr_b16 a[174:175], v20 offset:29184\n"
    "v_add_f32 v191, v191, v47\n"
    "v_mfma_f32_16x16x32_bf16 v[76:79], v[52:55], a[244:247], v[76:79]\n"
    "buffer_load_dwordx4 v3, s[12:15], s24 offen lds\n"
    "v_mfma_f32_16x16x32_bf16 v[84:87], v[52:55], a[188:191], v[84:87]\n"
    "s_waitcnt lgkmcnt(6)\n"
    "s_add_u32 m0, s37, 86016\n"
    "ds_read_b64_tr_b16 a[44:45], v20 offset:28672\n"
    "v_mfma_f32_16x16x32_bf16 v[92:95], v[52:55], a[176:179], v[92:95]\n"
    "s_mov_b32 s24, 1081344\n"
    "ds_read_b64_tr_b16 a[46:47], v20 offset:29184\n"
    "v_cvt_pk_bf16_f32 v192, v184, v185\n"
    "v_mfma_f32_16x16x32_bf16 v[96:99], a[138:141], a[232:235], v[96:99]\n"
    "ds_read_b64_tr_b16 v[200:201], v19 offset:20736\n"
    "v_cvt_pk_bf16_f32 v193, v186, v187\n"
    "v_mfma_f32_16x16x32_bf16 v[100:103], a[138:141], a[236:239], v[100:103]\n"
    "ds_read_b64_tr_b16 v[202:203], v19 offset:0\n"
    "v_cvt_pk_bf16_f32 v194, v188, v189\n"
    "v_mfma_f32_16x16x32_bf16 v[104:107], a[138:141], a[204:207], v[104:107]\n"
    "buffer_load_dwordx4 v3, s[12:15], s24 offen lds\n"
    "v_mfma_f32_16x16x32_bf16 v[108:111], a[138:141], a[216:219], v[108:111]\n"
    "s_add_u32 m0, s37, 90112\n"
    "ds_read_b64_tr_b16 v[204:205], v19 offset:20864\n"
    "v_cvt_pk_bf16_f32 v195, v190, v191\n"
    "v_mfma_f32_16x16x32_bf16 v[112:115], a[138:141], a[192:195], v[112:115]\n"
    "s_mov_b32 s24, 1343488\n"
    "ds_read_b64_tr_b16 v[206:207], v19 offset:128\n"
    "v_cvt_pk_bf16_f32 v48, v176, v177\n"
    "v_mfma_f32_16x16x32_bf16 v[116:119], a[138:141], a[220:223], v[116:119]\n"
    "ds_read_b128 a[232:235], v12 offset:41472\n"
    "v_cvt_pk_bf16_f32 v49, v178, v179\n"
    "v_mfma_f32_16x16x32_bf16 v[120:123], a[138:141], a[228:231], v[120:123]\n"
    "s_waitcnt lgkmcnt(12)\n"
    "ds_read_b64_tr_b16 v[208:209], v19 offset:20992\n"
    "v_cvt_pk_bf16_f32 v50, v180, v181\n"
    "v_mfma_f32_16x16x32_bf16 v[124:127], a[138:141], a[184:187], v[124:127]\n"
    "v_cvt_pk_bf16_f32 v51, v182, v183\n"
    "v_mfma_f32_16x16x32_bf16 v[160:163], v[192:195], a[180:183], v[160:163]\n"
    "ds_read_b64_tr_b16 v[210:211], v19 offset:256\n"
    "v_mfma_f32_16x16x32_bf16 v[164:167], v[192:195], a[248:251], v[164:167]\n"
    "ds_read_b64_tr_b16 v[212:213], v19 offset:21120\n"
    "v_mfma_f32_16x16x32_bf16 v[168:171], v[192:195], a[224:227], v[168:171]\n"
    "s_waitcnt lgkmcnt(14)\n"
    "ds_read_b64_tr_b16 v[214:215], v19 offset:384\n"
    "v_mfma_f32_16x16x32_bf16 v[172:175], v[192:195], a[200:203], v[172:175]\n"
    "s_waitcnt lgkmcnt(12)\n"
    "ds_read_b128 a[180:183], v11 offset:21632\n"
    "v_mfma_f32_16x16x32_bf16 v[184:187], v[36:39], a[168:171], 0\n"
    "s_waitcnt lgkmcnt(12)\n"
    "buffer_load_dwordx4 v3, s[12:15], s24 offen lds\n"
    "v_mfma_f32_16x16x32_bf16 v[188:191], v[36:39], a[172:175], 0\n"
    "s_waitcnt vmcnt(5) lgkmcnt(10)\n"
    "s_barrier\n"
    "ds_read_b64_tr_b16 a[130:131], v20 offset:40960\n"
    "v_mfma_f32_16x16x32_bf16 v[64:67], v[48:51], a[196:199], v[64:67]\n"
    "s_add_u32 m0, s37, 94208\n"
    "ds_read_b64_tr_b16 a[132:133], v20 offset:41472\n"
    "v_mfma_f32_16x16x32_bf16 v[72:75], v[48:51], a[208:211], v[72:75]\n"
    "s_waitcnt lgkmcnt(10)\n"
    "s_mov_b32 s24, 1605632\n"
    "ds_read_b64_tr_b16 a[128:129], v20 offset:36864\n"
    "v_mfma_f32_16x16x32_bf16 v[128:131], v[200:203], v[196:199], 0\n"
    "ds_read_b64_tr_b16 a[134:135], v20 offset:37376\n"
    "v_cvt_pk_bf16_f32 v52, v184, v185\n"
    "v_mfma_f32_16x16x32_bf16 v[80:83], v[48:51], a[212:215], v[80:83]\n"
    "s_waitcnt lgkmcnt(10)\n"
    "buffer_load_dwordx4 v3, s[12:15], s24 offen lds\n"
    "v_mfma_f32_16x16x32_bf16 v[132:135], v[204:207], v[196:199], 0\n"
    "s_waitcnt lgkmcnt(9)\n"
    "ds_read_b64_tr_b16 a[48:49], v20 offset:36864\n"
    "v_cvt_pk_bf16_f32 v53, v186, v187\n"
    "v_mfma_f32_16x16x32_bf16 v[88:91], v[48:51], a[232:235], v[88:91]\n"
    "s_waitcnt lgkmcnt(8)\n"
    "ds_read_b64_tr_b16 a[50:51], v20 offset:37376\n"
    "v_cvt_pk_bf16_f32 v54, v188, v189\n"
    "v_mfma_f32_16x16x32_bf16 v[136:139], v[208:211], v[196:199], 0\n"
    "v_cvt_pk_bf16_f32 v55, v190, v191\n"
    "v_cvt_pk_bf16_f32 v128, v128, v129\n"
    "v_mfma_f32_16x16x32_bf16 v[96:99], a[146:149], a[236:239], v[96:99]\n"
    "s_waitcnt lgkmcnt(7)\n"
    "buffer_load_dword v30, v58, s[12:15], 0 offen\n"
    "v_mfma_f32_16x16x32_bf16 v[140:143], v[212:215], v[196:199], 0\n"
    "global_load_dword v31, v59, s[50:51]\n"
    "v_mfma_f32_16x16x32_bf16 v[100:103], a[146:149], a[204:207], v[100:103]\n"
    "v_cvt_pk_bf16_f32 v129, v130, v131\n"
    "v_add_f32 v176, v176, v184\n"
    "v_mfma_f32_16x16x32_bf16 v[104:107], a[146:149], a[216:219], v[104:107]\n"
    "ds_read_b128 a[196:199], v12 offset:41600\n"
    "v_add_f32 v177, v177, v185\n"
    "v_mfma_f32_16x16x32_bf16 v[108:111], a[146:149], a[192:195], v[108:111]\n"
    "ds_write_b64 v61, v[128:129] offset:20736\n"
    "v_cvt_pk_bf16_f32 v132, v132, v133\n"
    "v_mfma_f32_16x16x32_bf16 v[112:115], a[146:149], a[220:223], v[112:115]\n"
    "v_cvt_pk_bf16_f32 v133, v134, v135\n"
    "v_add_f32 v178, v178, v186\n"
    "v_mfma_f32_16x16x32_bf16 v[116:119], a[146:149], a[228:231], v[116:119]\n"
    "ds_write_b64 v61, v[132:133] offset:20864\n"
    "v_cvt_pk_bf16_f32 v136, v136, v137\n"
    "v_mfma_f32_16x16x32_bf16 v[120:123], a[146:149], a[184:187], v[120:123]\n"
    "s_waitcnt lgkmcnt(9)\n"
    "ds_read_b128 a[236:239], v12 offset:20736\n"
    "v_cvt_pk_bf16_f32 v137, v138, v139\n"
    "v_mfma_f32_16x16x32_bf16 v[124:127], a[146:149], a[180:183], v[124:127]\n"
    "s_waitcnt lgkmcnt(7)\n"
    "ds_read_b128 a[240:243], v12 offset:128\n"
    "v_add_f32 v179, v179, v187\n"
    "v_mfma_f32_16x16x32_bf16 v[40:43], v[36:39], a[128:131], 0\n"
    "s_waitcnt lgkmcnt(7)\n"
    "ds_read_b64_tr_b16 a[138:139], v20 offset:49152\n"
    "v_add_f32 v180, v180, v188\n"
    "v_mfma_f32_16x16x32_bf16 v[44:47], v[36:39], a[132:135], 0\n"
    "ds_read_b64_tr_b16 a[140:141], v20 offset:49664\n"
    "v_add_f32 v181, v181, v189\n"
    "v_mfma_f32_16x16x32_bf16 v[68:71], v[52:55], a[244:247], v[68:71]\n"
    "ds_write_b64 v61, v[136:137] offset:20992\n"
    "v_cvt_pk_bf16_f32 v140, v140, v141\n"
    "v_mfma_f32_16x16x32_bf16 v[76:79], v[52:55], a[188:191], v[76:79]\n"
    "ds_read_b64_tr_b16 a[136:137], v20 offset:45056\n"
    "v_cvt_pk_bf16_f32 v141, v142, v143\n"
    "v_mfma_f32_16x16x32_bf16 v[84:87], v[52:55], a[176:179], v[84:87]\n"
    "s_waitcnt lgkmcnt(8)\n"
    "ds_read_b64_tr_b16 a[142:143], v20 offset:45568\n"
    "v_add_f32 v182, v182, v190\n"
    "v_mfma_f32_16x16x32_bf16 v[92:95], v[52:55], a[196:199], v[92:95]\n"
    "ds_read_b64_tr_b16 a[52:53], v20 offset:45056\n"
    "v_add_f32 v183, v183, v191\n"
    "v_mfma_f32_16x16x32_bf16 v[96:99], a[154:157], a[204:207], v[96:99]\n"
    "ds_read_b64_tr_b16 a[54:55], v20 offset:45568\n"
    "v_cvt_pk_bf16_f32 v192, v176, v177\n"
    "v_mfma_f32_16x16x32_bf16 v[100:103], a[154:157], a[216:219], v[100:103]\n"
    "ds_write_b64 v61, v[140:141] offset:21120\n"
    "v_cvt_pk_bf16_f32 v193, v178, v179\n"
    "v_mfma_f32_16x16x32_bf16 v[104:107], a[154:157], a[192:195], v[104:107]\n"
    "v_cvt_pk_bf16_f32 v194, v180, v181\n"
    "v_cvt_pk_bf16_f32 v195, v182, v183\n"
    "v_mfma_f32_16x16x32_bf16 v[108:111], a[154:157], a[220:223], v[108:111]\n"
    "v_cvt_pk_bf16_f32 v48, v40, v41\n"
    "v_cvt_pk_bf16_f32 v49, v42, v43\n"
    "v_mfma_f32_16x16x32_bf16 v[112:115], a[154:157], a[228:231], v[112:115]\n"
    "ds_read_b64_tr_b16 v[220:221], v19\n"
    "v_cvt_pk_bf16_f32 v50, v44, v45\n"
    "v_mfma_f32_16x16x32_bf16 v[116:119], a[154:157], a[184:187], v[116:119]\n"
    "ds_read_b64_tr_b16 v[222:223], v19 offset:128\n"
    "v_cvt_pk_bf16_f32 v51, v46, v47\n"
    "v_mfma_f32_16x16x32_bf16 v[120:123], a[154:157], a[180:183], v[120:123]\n"
    "s_waitcnt lgkmcnt(11)\n"
    "v_mfma_f32_16x16x32_bf16 v[124:127], a[154:157], a[236:239], v[124:127]\n"
    "ds_read_b64_tr_b16 v[224:225], v19 offset:256\n"
    "v_mfma_f32_16x16x32_bf16 v[160:163], v[192:195], a[248:251], v[160:163]\n"
    "ds_read_b128 a[204:207], v12 offset:41728\n"
    "v_mfma_f32_16x16x32_bf16 v[164:167], v[192:195], a[224:227], v[164:167]\n"
    "ds_read_b64_tr_b16 v[226:227], v19 offset:384\n"
    "v_mfma_f32_16x16x32_bf16 v[168:171], v[192:195], a[200:203], v[168:171]\n"
    "s_waitcnt lgkmcnt(13)\n"
    "ds_read_b64_tr_b16 v[228:229], v4\n"
    "v_mfma_f32_16x16x32_bf16 v[172:175], v[192:195], a[240:243], v[172:175]\n"
    "s_waitcnt lgkmcnt(10)\n"
    "v_mfma_f32_16x16x32_bf16 v[176:179], v[36:39], a[136:139], 0\n"
    "s_waitcnt lgkmcnt(9)\n"
    "ds_read_b128 a[244:247], v12 offset:20864\n"
    "v_mfma_f32_16x16x32_bf16 v[180:183], v[36:39], a[140:143], 0\n"
    "ds_read_b64_tr_b16 a[146:147], v20 offset:57344\n"
    "v_mfma_f32_16x16x32_bf16 v[64:67], v[48:51], a[208:211], v[64:67]\n"
    "ds_read_b64_tr_b16 a[148:149], v20 offset:57856\n"
    "v_mfma_f32_16x16x32_bf16 v[72:75], v[48:51], a[212:215], v[72:75]\n"
    "s_waitcnt lgkmcnt(7)\n"
    "ds_read_b64_tr_b16 a[144:145], v20 offset:53248\n"
    "v_mfma_f32_16x16x32_bf16 v[144:147], v[220:223], v[196:199], 0\n"
    "v_cvt_pk_bf16_f32 v52, v176, v177\n"
    "v_cvt_pk_bf16_f32 v53, v178, v179\n"
    "v_mfma_f32_16x16x32_bf16 v[80:83], v[48:51], a[232:235], v[80:83]\n"
    "s_waitcnt lgkmcnt(7)\n"
    "ds_read_b64_tr_b16 a[150:151], v20 offset:53760\n"
    "v_cvt_pk_bf16_f32 v54, v180, v181\n"
    "v_mfma_f32_16x16x32_bf16 v[148:151], v[222:225], v[252:255], 0\n"
    "s_waitcnt lgkmcnt(7)\n"
    "ds_read_b64_tr_b16 a[56:57], v20 offset:53248\n"
    "v_cvt_pk_bf16_f32 v55, v182, v183\n"
    "v_mfma_f32_16x16x32_bf16 v[88:91], v[48:51], a[204:207], v[88:91]\n"
    "s_waitcnt lgkmcnt(7)\n"
    "ds_read_b64_tr_b16 a[58:59], v20 offset:53760\n"
    "v_add_f32 v40, v40, v176\n"
    "v_mfma_f32_16x16x32_bf16 v[152:155], v[224:227], v[196:199], 0\n"
    "v_cvt_pk_bf16_f32 v144, v144, v145\n"
    "v_cvt_pk_bf16_f32 v145, v146, v147\n"
    "v_mfma_f32_16x16x32_bf16 v[96:99], a[162:165], a[216:219], v[96:99]\n"
    "s_waitcnt lgkmcnt(7)\n"
    "ds_write_b64 v61, v[144:145] offset:41472\n"
    "v_add_f32 v41, v41, v177\n"
    "v_mfma_f32_16x16x32_bf16 v[156:159], v[226:229], v[252:255], 0\n"
    "s_add_u32 m0, s37, 0\n"
    "v_cvt_pk_bf16_f32 v148, v148, v149\n"
    "v_cvt_pk_bf16_f32 v149, v150, v151\n"
    "v_mfma_f32_16x16x32_bf16 v[100:103], a[162:165], a[192:195], v[100:103]\n"
    "s_mov_b32 s24, 1867776\n"
    "ds_write_b64 v61, v[148:149] offset:41600\n"
    "v_add_f32 v42, v42, v178\n"
    "v_mfma_f32_16x16x32_bf16 v[104:107], a[162:165], a[220:223], v[104:107]\n"
    "ds_read_b128 a[208:211], v12 offset:41856\n"
    "v_cvt_pk_bf16_f32 v152, v152, v153\n"
    "v_mfma_f32_16x16x32_bf16 v[108:111], a[162:165], a[228:231], v[108:111]\n"
    "buffer_load_dwordx4 v3, s[12:15], s24 offen lds\n"
    "v_mfma_f32_16x16x32_bf16 v[112:115], a[162:165], a[184:187], v[112:115]\n"
    "s_add_u32 m0, s37, 4096\n"
    "v_cvt_pk_bf16_f32 v153, v154, v155\n"
    "v_add_f32 v43, v43, v179\n"
    "v_mfma_f32_16x16x32_bf16 v[116:119], a[162:165], a[180:183], v[116:119]\n"
    "s_mov_b32 s24, 2129920\n"
    "ds_write_b64 v61, v[152:153] offset:41728\n"
    "v_cvt_pk_bf16_f32 v156, v156, v157\n"
    "v_mfma_f32_16x16x32_bf16 v[120:123], a[162:165], a[236:239], v[120:123]\n"
    "s_waitcnt lgkmcnt(10)\n"
    "ds_read_b128 a[216:219], v12 offset:20992\n"
    "v_cvt_pk_bf16_f32 v157, v158, v159\n"
    "v_mfma_f32_16x16x32_bf16 v[124:127], a[162:165], a[244:247], v[124:127]\n"
    "s_waitcnt lgkmcnt(8)\n"
    "ds_read_b128 a[248:251], v12 offset:384\n"
    "v_add_f32 v44, v44, v180\n"
    "v_mfma_f32_16x16x32_bf16 v[184:187], v[36:39], a[144:147], 0\n"
    "s_waitcnt lgkmcnt(8)\n"
    "ds_read_b64_tr_b16 a[154:155], v21 offset:0\n"
    "v_add_f32 v45, v45, v181\n"
    "v_mfma_f32_16x16x32_bf16 v[188:191], v[36:39], a[148:151], 0\n"
    "buffer_load_dwordx4 v3, s[12:15], s24 offen lds\n"
    "v_mfma_f32_16x16x32_bf16 v[68:71], v[52:55], a[188:191], v[68:71]\n"
    "s_add_u32 m0, s37, 8192\n"
    "ds_read_b64_tr_b16 a[156:157], v21 offset:512\n"
    "v_add_f32 v46, v46, v182\n"
    "v_mfma_f32_16x16x32_bf16 v[76:79], v[52:55], a[176:179], v[76:79]\n"
    "s_mov_b32 s24, 2392064\n"
    "ds_read_b64_tr_b16 a[152:153], v20 offset:61440\n"
    "v_add_f32 v47, v47, v183\n"
    "v_mfma_f32_16x16x32_bf16 v[84:87], v[52:55], a[196:199], v[84:87]\n"
    "s_waitcnt lgkmcnt(6)\n"
    "ds_read_b64_tr_b16 a[158:159], v20 offset:61952\n"
    "v_cvt_pk_bf16_f32 v192, v40, v41\n"
    "v_mfma_f32_16x16x32_bf16 v[92:95], v[52:55], a[208:211], v[92:95]\n"
    "ds_read_b64_tr_b16 a[60:61], v20 offset:61440\n"
    "v_cvt_pk_bf16_f32 v193, v42, v43\n"
    "v_mfma_f32_16x16x32_bf16 v[96:99], a[170:173], a[192:195], v[96:99]\n"
    "buffer_load_dwordx4 v3, s[12:15], s24 offen lds\n"
    "v_mfma_f32_16x16x32_bf16 v[100:103], a[170:173], a[220:223], v[100:103]\n"
    "s_add_u32 m0, s37, 12288\n"
    "ds_read_b64_tr_b16 a[62:63], v20 offset:61952\n"
    "v_cvt_pk_bf16_f32 v194, v44, v45\n"
    "v_mfma_f32_16x16x32_bf16 v[104:107], a[170:173], a[228:231], v[104:107]\n"
    "s_mov_b32 s24, 2654208\n"
    "ds_write_b64 v61, v[156:157] offset:41856\n"
    "v_cvt_pk_bf16_f32 v195, v46, v47\n"
    "v_mfma_f32_16x16x32_bf16 v[108:111], a[170:173], a[184:187], v[108:111]\n"
    "v_cvt_pk_bf16_f32 v48, v184, v185\n"
    "v_cvt_pk_bf16_f32 v49, v186, v187\n"
    "v_mfma_f32_16x16x32_bf16 v[112:115], a[170:173], a[180:183], v[112:115]\n"
    "v_cvt_pk_bf16_f32 v50, v188, v189\n"
    "v_cvt_pk_bf16_f32 v51, v190, v191\n"
    "v_mfma_f32_16x16x32_bf16 v[116:119], a[170:173], a[236:239], v[116:119]\n"
    "buffer_load_dwordx4 v3, s[12:15], s24 offen lds\n"
    "v_mfma_f32_16x16x32_bf16 v[120:123], a[170:173], a[244:247], v[120:123]\n"
    "s_waitcnt lgkmcnt(8)\n"
    "s_add_u32 m0, s37, 16384\n"
    "ds_read_b128 a[188:191], v12 offset:41984\n"
    "v_mfma_f32_16x16x32_bf16 v[124:127], a[170:173], a[216:219], v[124:127]\n"
    "s_mov_b32 s24, 2916352\n"
    "v_mfma_f32_16x16x32_bf16 v[160:163], v[192:195], a[224:227], v[160:163]\n"
    "v_mfma_f32_16x16x32_bf16 v[164:167], v[192:195], a[200:203], v[164:167]\n"
    "buffer_load_dwordx4 v3, s[12:15], s24 offen lds\n"
    "v_mfma_f32_16x16x32_bf16 v[168:171], v[192:195], a[240:243], v[168:171]\n"
    "s_waitcnt lgkmcnt(8)\n"
    "s_add_u32 m0, s37, 20480\n"
    "v_mfma_f32_16x16x32_bf16 v[172:175], v[192:195], a[248:251], v[172:175]\n"
    "s_waitcnt lgkmcnt(5)\n"
    "s_mov_b32 s24, 3178496\n"
    "v_mfma_f32_16x16x32_bf16 v[40:43], v[36:39], a[152:155], 0\n"
    "s_waitcnt lgkmcnt(4)\n"
    "v_mfma_f32_16x16x32_bf16 v[44:47], v[36:39], a[156:159], 0\n"
    "buffer_load_dwordx4 v3, s[12:15], s24 offen lds\n"
    "v_mfma_f32_16x16x32_bf16 v[64:67], v[48:51], a[212:215], v[64:67]\n"
    "s_add_u32 m0, s37, 24576\n"
    "ds_read_b128 a[192:195], v12 offset:21120\n"
    "v_mfma_f32_16x16x32_bf16 v[72:75], v[48:51], a[232:235], v[72:75]\n"
    "s_mov_b32 s24, 3440640\n"
    "v_mfma_f32_16x16x32_bf16 v[80:83], v[48:51], a[204:207], v[80:83]\n"
    "s_waitcnt lgkmcnt(1)\n"
    "ds_read_b128 a[204:207], v12 offset:42112\n"
    "v_cvt_pk_bf16_f32 v52, v40, v41\n"
    "v_mfma_f32_16x16x32_bf16 v[88:91], v[48:51], a[188:191], v[88:91]\n"
    "ds_read_b128 a[188:191], v12 offset:21248\n"
    "v_cvt_pk_bf16_f32 v53, v42, v43\n"
    "v_mfma_f32_16x16x32_bf16 v[96:99], a[130:133], a[220:223], v[96:99]\n"
    "ds_read_b128 a[212:215], v12 offset:640\n"
    "v_cvt_pk_bf16_f32 v54, v44, v45\n"
    "v_mfma_f32_16x16x32_bf16 v[100:103], a[130:133], a[228:231], v[100:103]\n"
    "s_waitcnt vmcnt(5)\n"
    "s_barrier\n"
    "ds_read_b64_tr_b16 a[170:171], v21 offset:12288\n"
    "v_cvt_pk_bf16_f32 v55, v46, v47\n"
    "v_mfma_f32_16x16x32_bf16 v[104:107], a[130:133], a[184:187], v[104:107]\n"
    "buffer_load_dwordx4 v3, s[12:15], s24 offen lds\n"
    "v_mfma_f32_16x16x32_bf16 v[108:111], a[130:133], a[180:183], v[108:111]\n"
    "s_add_u32 m0, s37, 28672\n"
    "ds_read_b64_tr_b16 a[172:173], v21 offset:12800\n"
    "v_add_f32 v184, v184, v40\n"
    "v_mfma_f32_16x16x32_bf16 v[112:115], a[130:133], a[236:239], v[112:115]\n"
    "s_mov_b32 s24, 3702784\n"
    "ds_read_b64_tr_b16 a[168:169], v21 offset:8192\n"
    "v_add_f32 v185, v185, v41\n"
    "v_mfma_f32_16x16x32_bf16 v[116:119], a[130:133], a[244:247], v[116:119]\n"
    "ds_read_b64_tr_b16 a[174:175], v21 offset:8704\n"
    "v_add_f32 v186, v186, v42\n"
    "v_mfma_f32_16x16x32_bf16 v[120:123], a[130:133], a[216:219], v[120:123]\n"
    "s_waitcnt lgkmcnt(7)\n"
    "ds_read_b64_tr_b16 a[68:69], v21 offset:8192\n"
    "v_add_f32 v187, v187, v43\n"
    "v_mfma_f32_16x16x32_bf16 v[124:127], a[130:133], a[192:195], v[124:127]\n"
    "buffer_load_dwordx4 v3, s[12:15], s24 offen lds\n"
    "v_mfma_f32_16x16x32_bf16 v[68:71], v[52:55], a[176:179], v[68:71]\n"
    "s_add_u32 m0, s37, 32768\n"
    "ds_read_b64_tr_b16 a[70:71], v21 offset:8704\n"
    "v_add_f32 v188, v188, v44\n"
    "v_mfma_f32_16x16x32_bf16 v[76:79], v[52:55], a[196:199], v[76:79]\n"
    "s_mov_b32 s24, 3964928\n"
    "ds_read_b64_tr_b16 a[162:163], v21 offset:4096\n"
    "v_add_f32 v189, v189, v45\n"
    "v_mfma_f32_16x16x32_bf16 v[84:87], v[52:55], a[208:211], v[84:87]\n"
    "s_waitcnt lgkmcnt(9)\n"
    "ds_read_b64_tr_b16 a[164:165], v21 offset:4608\n"
    "v_add_f32 v190, v190, v46\n"
    "v_mfma_f32_16x16x32_bf16 v[92:95], v[52:55], a[204:207], v[92:95]\n"
    "ds_read_b128 a[176:179], v13 offset:41472\n"
    "v_add_f32 v191, v191, v47\n"
    "v_mfma_f32_16x16x32_bf16 v[96:99], a[138:141], a[228:231], v[96:99]\n"
    "buffer_load_dwordx4 v3, s[12:15], s24 offen lds\n"
    "v_mfma_f32_16x16x32_bf16 v[100:103], a[138:141], a[184:187], v[100:103]\n"
    "s_add_u32 m0, s37, 36864\n"
    "ds_read_b128 a[196:199], v13 offset:41728\n"
    "v_cvt_pk_bf16_f32 v192, v184, v185\n"
    "v_mfma_f32_16x16x32_bf16 v[104:107], a[138:141], a[180:183], v[104:107]\n"
    "s_mov_b32 s24, 4227072\n"
    "ds_read_b128 a[204:207], v13 offset:41984\n"
    "v_cvt_pk_bf16_f32 v193, v186, v187\n"
    "v_mfma_f32_16x16x32_bf16 v[108:111], a[138:141], a[236:239], v[108:111]\n"
    "ds_read_b128 a[208:211], v13 offset:42240\n"
    "v_cvt_pk_bf16_f32 v194, v188, v189\n"
    "v_mfma_f32_16x16x32_bf16 v[112:115], a[138:141], a[244:247], v[112:115]\n"
    "ds_read_b128 a[220:223], v12 offset:21376\n"
    "v_cvt_pk_bf16_f32 v195, v190, v191\n"
    "v_mfma_f32_16x16x32_bf16 v[116:119], a[138:141], a[216:219], v[116:119]\n"
    "buffer_load_dwordx4 v3, s[12:15], s24 offen lds\n"
    "v_mfma_f32_16x16x32_bf16 v[120:123], a[138:141], a[192:195], v[120:123]\n"
    "s_waitcnt lgkmcnt(14)\n"
    "s_add_u32 m0, s37, 40960\n"
    "ds_read_b64_tr_b16 a[130:131], v21 offset:20480\n"
    "v_mfma_f32_16x16x32_bf16 v[124:127], a[138:141], a[188:191], v[124:127]\n"
    "s_mov_b32 s24, 4489216\n"
    "s_waitcnt lgkmcnt(10)\n"
    "ds_read_b64_tr_b16 a[132:133], v21 offset:20992\n"
    "v_mfma_f32_16x16x32_bf16 v[160:163], v[192:195], a[200:203], v[160:163]\n"
    "ds_read_b64_tr_b16 a[128:129], v21 offset:16384\n"
    "v_mfma_f32_16x16x32_bf16 v[164:167], v[192:195], a[240:243], v[164:167]\n"
    "ds_read_b64_tr_b16 a[134:135], v21 offset:16896\n"
    "v_mfma_f32_16x16x32_bf16 v[168:171], v[192:195], a[248:251], v[168:171]\n"
    "buffer_load_dwordx4 v3, s[12:15], s24 offen lds\n"
    "v_mfma_f32_16x16x32_bf16 v[172:175], v[192:195], a[212:215], v[172:175]\n"
    "s_add_u32 m0, s37, 45056\n"
    "ds_read_b64_tr_b16 a[72:73], v21 offset:16384\n"
    "v_mfma_f32_16x16x32_bf16 v[184:187], v[36:39], a[168:171], 0\n"
    "s_mov_b32 s24, 4751360\n"
    "ds_read_b64_tr_b16 a[74:75], v21 offset:16896\n"
    "v_mfma_f32_16x16x32_bf16 v[188:191], v[36:39], a[172:175], 0\n"
    "s_waitcnt lgkmcnt(11)\n"
    "ds_read_b128 a[200:203], v13 offset:41600\n"
    "v_mfma_f32_16x16x32_bf16 v[176:179], v[236:239], a[162:165], v[184:187]\n"
    "ds_read_b128 a[212:215], v13 offset:41856\n"
    "v_mfma_f32_16x16x32_bf16 v[180:183], v[234:237], a[162:165], v[188:191]\n"
    "s_waitcnt lgkmcnt(12)\n"
    "buffer_load_dwordx4 v3, s[12:15], s24 offen lds\n"
    "v_mfma_f32_16x16x32_bf16 v[64:67], a[162:165], a[176:179], v[64:67]\n"
    "s_waitcnt lgkmcnt(11)\n"
    "s_add_u32 m0, s37, 49152\n"
    "ds_read_b128 a[176:179], v13 offset:42112\n"
    "v_mfma_f32_16x16x32_bf16 v[72:75], a[162:165], a[196:199], v[72:75]\n"
    "s_waitcnt lgkmcnt(11)\n"
    "s_mov_b32 s24, 5013504\n"
    "ds_read_b128 a[224:227], v13 offset:42368\n"
    "v_mfma_f32_16x16x32_bf16 v[80:83], a[162:165], a[204:207], v[80:83]\n"
    "s_waitcnt lgkmcnt(11)\n"
    "ds_read_b128 a[228:231], v12 offset:21504\n"
    "v_cvt_pk_bf16_f32 v52, v184, v185\n"
    "v_mfma_f32_16x16x32_bf16 v[88:91], a[162:165], a[208:211], v[88:91]\n"
    "ds_read_b128 a[232:235], v13 offset:128\n"
    "v_cvt_pk_bf16_f32 v53, v186, v187\n"
    "v_mfma_f32_16x16x32_bf16 v[96:99], a[146:149], a[184:187], v[96:99]\n"
    "buffer_load_dwordx4 v3, s[12:15], s24 offen lds\n"
    "v_mfma_f32_16x16x32_bf16 v[100:103], a[146:149], a[180:183], v[100:103]\n"
    "s_add_u32 m0, s37, 53248\n"
    "ds_read_b128 a[184:187], v13 offset:384\n"
    "v_cvt_pk_bf16_f32 v54, v188, v189\n"
    "v_mfma_f32_16x16x32_bf16 v[104:107], a[146:149], a[236:239], v[104:107]\n"
    "s_mov_b32 s24, 5275648\n"
    "ds_read_b128 a[240:243], v13 offset:640\n"
    "v_cvt_pk_bf16_f32 v55, v190, v191\n"
    "v_mfma_f32_16x16x32_bf16 v[108:111], a[146:149], a[244:247], v[108:111]\n"
    "s_waitcnt lgkmcnt(10)\n"
    "ds_read_b128 a[248:251], v13 offset:896\n"
    "v_cvt_pk_bf16_f32 v192, v176, v177\n"
    "v_mfma_f32_16x16x32_bf16 v[112:115], a[146:149], a[216:219], v[112:115]\n"
    "ds_read_b64_tr_b16 a[138:139], v21 offset:28672\n"
    "v_cvt_pk_bf16_f32 v193, v178, v179\n"
    "v_mfma_f32_16x16x32_bf16 v[116:119], a[146:149], a[192:195], v[116:119]\n"
    "buffer_load_dwordx4 v3, s[12:15], s24 offen lds\n"
    "v_mfma_f32_16x16x32_bf16 v[120:123], a[146:149], a[188:191], v[120:123]\n"
    "s_add_u32 m0, s37, 57344\n"
    "ds_read_b64_tr_b16 a[140:141], v21 offset:29184\n"
    "v_cvt_pk_bf16_f32 v194, v180, v181\n"
    "v_mfma_f32_16x16x32_bf16 v[124:127], a[146:149], a[220:223], v[124:127]\n"
    "s_mov_b32 s24, 5537792\n"
    "ds_read_b64_tr_b16 a[136:137], v21 offset:24576\n"
    "v_cvt_pk_bf16_f32 v195, v182, v183\n"
    "v_mfma_f32_16x16x32_bf16 v[40:43], v[36:39], a[128:131], 0\n"
    "ds_read_b64_tr_b16 a[142:143], v21 offset:25088\n"
    "v_mfma_f32_16x16x32_bf16 v[44:47], v[36:39], a[132:135], 0\n"
    "s_waitcnt lgkmcnt(12)\n"
    "ds_read_b64_tr_b16 a[76:77], v21 offset:24576\n"
    "v_mfma_f32_16x16x32_bf16 v[68:71], v[52:55], a[200:203], v[68:71]\n"
    "s_waitcnt lgkmcnt(12)\n"
    "buffer_load_dwordx4 v3, s[12:15], s24 offen lds\n"
    "v_mfma_f32_16x16x32_bf16 v[76:79], v[52:55], a[212:215], v[76:79]\n"
    "s_waitcnt lgkmcnt(11)\n"
    "s_add_u32 m0, s37, 61440\n"
    "ds_read_b64_tr_b16 a[78:79], v21 offset:25088\n"
    "v_mfma_f32_16x16x32_bf16 v[84:87], v[52:55], a[176:179], v[84:87]\n"
    "s_waitcnt lgkmcnt(11)\n"
    "ds_read_b128 a[200:203], v14 offset:41472\n"
    "v_cvt_pk_bf16_f32 v48, v40, v41\n"
    "v_mfma_f32_16x16x32_bf16 v[92:95], v[52:55], a[224:227], v[92:95]\n"
    "s_mov_b32 s24, 5799936\n"
    "ds_read_b128 a[252:255], v13 offset:20736\n"
    "v_cvt_pk_bf16_f32 v49, v42, v43\n"
    "v_mfma_f32_16x16x32_bf16 v[96:99], a[154:157], a[180:183], v[96:99]\n"
    "ds_read_b128 a[180:183], v13 offset:20864\n"
    "v_cvt_pk_bf16_f32 v50, v44, v45\n"
    "v_mfma_f32_16x16x32_bf16 v[100:103], a[154:157], a[236:239], v[100:103]\n"
    "buffer_load_dwordx4 v3, s[12:15], s24 offen lds\n"
    "v_mfma_f32_16x16x32_bf16 v[104:107], a[154:157], a[244:247], v[104:107]\n"
    "s_add_u32 m0, s37, 65536\n"
    "ds_read_b128 a[236:239], v13 offset:20992\n"
    "v_cvt_pk_bf16_f32 v51, v46, v47\n"
    "v_mfma_f32_16x16x32_bf16 v[108:111], a[154:157], a[216:219], v[108:111]\n"
    "s_mov_b32 s24, 6062080\n"
    "s_waitcnt lgkmcnt(10)\n"
    "ds_read_b128 a[216:219], v13 offset:21120\n"
    "v_mfma_f32_16x16x32_bf16 v[112:115], a[154:157], a[192:195], v[112:115]\n"
    "ds_read_b128 a[192:195], v13 offset:21248\n"
    "v_mfma_f32_16x16x32_bf16 v[116:119], a[154:157], a[188:191], v[116:119]\n"
    "ds_read_b128 a[188:191], v13 offset:21376\n"
    "v_mfma_f32_16x16x32_bf16 v[120:123], a[154:157], a[220:223], v[120:123]\n"
    "s_waitcnt vmcnt(5) lgkmcnt(7)\n"
    "s_barrier\n"
    "v_mfma_f32_16x16x32_bf16 v[124:127], a[154:157], a[228:231], v[124:127]\n"
    "ds_read_b128 a[220:223], v13 offset:21504\n"
    "v_mfma_f32_16x16x32_bf16 v[160:163], v[192:195], a[232:235], v[160:163]\n"
    "ds_read_b128 a[228:231], v13 offset:21632\n"
    "v_mfma_f32_16x16x32_bf16 v[164:167], v[192:195], a[184:187], v[164:167]\n"
    "ds_read_b64_tr_b16 a[146:147], v20 offset:4096\n"
    "v_mfma_f32_16x16x32_bf16 v[168:171], v[192:195], a[240:243], v[168:171]\n"
    "ds_read_b64_tr_b16 a[148:149], v20 offset:4608\n"
    "v_mfma_f32_16x16x32_bf16 v[172:175], v[192:195], a[248:251], v[172:175]\n"
    "buffer_load_dwordx4 v3, s[12:15], s24 offen lds\n"
    "v_mfma_f32_16x16x32_bf16 v[176:179], v[36:39], a[136:139], 0\n"
    "s_add_u32 m0, s37, 69632\n"
    "ds_read_b64_tr_b16 a[144:145], v20 offset:0\n"
    "v_mfma_f32_16x16x32_bf16 v[180:183], v[36:39], a[140:143], 0\n"
    "s_mov_b32 s24, 6324224\n"
    "ds_read_b64_tr_b16 a[150:151], v20 offset:512\n"
    "v_mfma_f32_16x16x32_bf16 v[64:67], v[48:51], a[196:199], v[64:67]\n"
    "ds_read_b64_tr_b16 a[80:81], v20 offset:0\n"
    "v_mfma_f32_16x16x32_bf16 v[72:75], v[48:51], a[204:207], v[72:75]\n"
    "ds_read_b64_tr_b16 a[82:83], v20 offset:512\n"
    "v_mfma_f32_16x16x32_bf16 v[80:83], v[48:51], a[208:211], v[80:83]\n"
    "s_waitcnt lgkmcnt(14)\n"
    "buffer_load_dwordx4 v3, s[12:15], s24 offen lds\n"
    "v_mfma_f32_16x16x32_bf16 v[88:91], v[48:51], a[200:203], v[88:91]\n"
    "s_waitcnt lgkmcnt(13)\n"
    "s_add_u32 m0, s37, 73728\n"
    "v_cvt_pk_bf16_f32 v52, v176, v177\n"
    "v_mfma_f32_16x16x32_bf16 v[96:99], a[162:165], a[252:255], v[96:99]\n"
    "s_waitcnt lgkmcnt(12)\n"
    "s_mov_b32 s24, 6586368\n"
    "v_cvt_pk_bf16_f32 v53, v178, v179\n"
    "v_mfma_f32_16x16x32_bf16 v[100:103], a[162:165], a[180:183], v[100:103]\n"
    "s_waitcnt lgkmcnt(11)\n"
    "v_cvt_pk_bf16_f32 v54, v180, v181\n"
    "v_cvt_pk_bf16_f32 v55, v182, v183\n"
    "v_mfma_f32_16x16x32_bf16 v[104:107], a[162:165], a[236:239], v[104:107]\n"
    "s_waitcnt lgkmcnt(10)\n"
    "ds_read_b128 a[196:199], v14 offset:41600\n"
    "v_add_f32 v40, v40, v176\n"
    "v_mfma_f32_16x16x32_bf16 v[108:111], a[162:165], a[216:219], v[108:111]\n"
    "s_waitcnt lgkmcnt(10)\n"
    "buffer_load_dwordx4 v3, s[12:15], s24 offen lds\n"
    "v_mfma_f32_16x16x32_bf16 v[112:115], a[162:165], a[192:195], v[112:115]\n"
    "s_waitcnt lgkmcnt(9)\n"
    "s_add_u32 m0, s37, 77824\n"
    "v_add_f32 v41, v41, v177\n"
    "v_mfma_f32_16x16x32_bf16 v[116:119], a[162:165], a[188:191], v[116:119]\n"
    "s_waitcnt lgkmcnt(8)\n"
    "s_mov_b32 s24, 6848512\n"
    "v_add_f32 v42, v42, v178\n"
    "v_mfma_f32_16x16x32_bf16 v[120:123], a[162:165], a[220:223], v[120:123]\n"
    "s_waitcnt lgkmcnt(7)\n"
    "ds_read_b128 a[232:235], v14 offset:20736\n"
    "v_add_f32 v43, v43, v179\n"
    "v_mfma_f32_16x16x32_bf16 v[124:127], a[162:165], a[228:231], v[124:127]\n"
    "s_waitcnt lgkmcnt(5)\n"
    "ds_read_b128 a[244:247], v14 offset:128\n"
    "v_add_f32 v44, v44, v180\n"
    "v_mfma_f32_16x16x32_bf16 v[184:187], v[36:39], a[144:147], 0\n"
    "s_waitcnt lgkmcnt(5)\n"
    "buffer_load_dwordx4 v3, s[12:15], s24 offen lds\n"
    "v_mfma_f32_16x16x32_bf16 v[188:191], v[36:39], a[148:151], 0\n"
    "s_add_u32 m0, s37, 81920\n"
    "ds_read_b64_tr_b16 a[154:155], v20 offset:12288\n"
    "v_add_f32 v45, v45, v181\n"
    "v_mfma_f32_16x16x32_bf16 v[68:71], v[52:55], a[212:215], v[68:71]\n"
    "s_mov_b32 s24, 7110656\n"
    "ds_read_b64_tr_b16 a[156:157], v20 offset:12800\n"
    "v_add_f32 v46, v46, v182\n"
    "v_mfma_f32_16x16x32_bf16 v[76:79], v[52:55], a[176:179], v[76:79]\n"
    "ds_read_b64_tr_b16 a[152:153], v20 offset:8192\n"
    "v_add_f32 v47, v47, v183\n"
    "v_mfma_f32_16x16x32_bf16 v[84:87], v[52:55], a[224:227], v[84:87]\n"
    "s_waitcnt lgkmcnt(5)\n"
    "ds_read_b64_tr_b16 a[158:159], v20 offset:8704\n"
    "v_cvt_pk_bf16_f32 v192, v40, v41\n"
    "v_mfma_f32_16x16x32_bf16 v[92:95], v[52:55], a[196:199], v[92:95]\n"
    "buffer_load_dwordx4 v3, s[12:15], s24 offen lds\n"
    "v_mfma_f32_16x16x32_bf16 v[96:99], a[170:173], a[180:183], v[96:99]\n"
    "s_add_u32 m0, s37, 86016\n"
    "ds_read_b64_tr_b16 a[84:85], v20 offset:8192\n"
    "v_cvt_pk_bf16_f32 v193, v42, v43\n"
    "v_mfma_f32_16x16x32_bf16 v[100:103], a[170:173], a[236:239], v[100:103]\n"
    "s_mov_b32 s24, 7372800\n"
    "ds_read_b64_tr_b16 a[86:87], v20 offset:8704\n"
    "v_cvt_pk_bf16_f32 v194, v44, v45\n"
    "v_mfma_f32_16x16x32_bf16 v[104:107], a[170:173], a[216:219], v[104:107]\n"
    "v_cvt_pk_bf16_f32 v195, v46, v47\n"
    "v_cvt_pk_bf16_f32 v48, v184, v185\n"
    "v_mfma_f32_16x16x32_bf16 v[108:111], a[170:173], a[192:195], v[108:111]\n"
    "v_cvt_pk_bf16_f32 v49, v186, v187\n"
    "v_cvt_pk_bf16_f32 v50, v188, v189\n"
    "v_mfma_f32_16x16x32_bf16 v[112:115], a[170:173], a[188:191], v[112:115]\n"
    "v_cvt_pk_bf16_f32 v51, v190, v191\n"
    "v_mfma_f32_16x16x32_bf16 v[116:119], a[170:173], a[220:223], v[116:119]\n"
    "buffer_load_dwordx4 v3, s[12:15], s24 offen lds\n"
    "v_mfma_f32_16x16x32_bf16 v[120:123], a[170:173], a[228:231], v[120:123]\n"
    "s_waitcnt lgkmcnt(7)\n"
    "s_add_u32 m0, s37, 90112\n"
    "ds_read_b128 a[180:183], v14 offset:41728\n"
    "v_mfma_f32_16x16x32_bf16 v[124:127], a[170:173], a[232:235], v[124:127]\n"
    "s_mov_b32 s24, 7634944\n"
    "ds_read_b128 a[212:215], v14 offset:20864\n"
    "v_mfma_f32_16x16x32_bf16 v[160:163], v[192:195], a[184:187], v[160:163]\n"
    "ds_read_b64_tr_b16 a[162:163], v20 offset:20480\n"
    "v_mfma_f32_16x16x32_bf16 v[164:167], v[192:195], a[240:243], v[164:167]\n"
    "buffer_load_dwordx4 v3, s[12:15], s24 offen lds\n"
    "v_mfma_f32_16x16x32_bf16 v[168:171], v[192:195], a[248:251], v[168:171]\n"
    "s_waitcnt lgkmcnt(9)\n"
    "s_add_u32 m0, s37, 94208\n"
    "ds_read_b64_tr_b16 a[164:165], v20 offset:20992\n"
    "v_mfma_f32_16x16x32_bf16 v[172:175], v[192:195], a[244:247], v[172:175]\n"
    "s_waitcnt lgkmcnt(7)\n"
    "s_mov_b32 s24, 7897088\n"
    "ds_read_b64_tr_b16 a[160:161], v20 offset:16384\n"
    "v_mfma_f32_16x16x32_bf16 v[40:43], v[36:39], a[152:155], 0\n"
    "s_waitcnt lgkmcnt(7)\n"
    "ds_read_b64_tr_b16 a[166:167], v20 offset:16896\n"
    "v_mfma_f32_16x16x32_bf16 v[44:47], v[36:39], a[156:159], 0\n"
    "ds_read_b64_tr_b16 a[88:89], v20 offset:16384\n"
    "v_mfma_f32_16x16x32_bf16 v[64:67], v[48:51], a[204:207], v[64:67]\n"
    "buffer_load_dwordx4 v3, s[12:15], s24 offen lds\n"
    "v_mfma_f32_16x16x32_bf16 v[72:75], v[48:51], a[208:211], v[72:75]\n"
    "ds_read_b64_tr_b16 a[90:91], v20 offset:16896\n"
    "v_mfma_f32_16x16x32_bf16 v[80:83], v[48:51], a[200:203], v[80:83]\n"
    "s_waitcnt lgkmcnt(7)\n"
    "v_cvt_pk_bf16_f32 v52, v40, v41\n"
    "v_cvt_pk_bf16_f32 v53, v42, v43\n"
    "v_mfma_f32_16x16x32_bf16 v[88:91], v[48:51], a[180:183], v[88:91]\n"
    "v_cvt_pk_bf16_f32 v54, v44, v45\n"
    "v_cvt_pk_bf16_f32 v55, v46, v47\n"
    "v_mfma_f32_16x16x32_bf16 v[96:99], a[130:133], a[236:239], v[96:99]\n"
    "buffer_store_dwordx4 v[32:35], v22, s[16:19], s25 offen sc1\n"
    "v_mfma_f32_16x16x32_bf16 v[100:103], a[130:133], a[216:219], v[100:103]\n"
    "v_add_f32 v184, v184, v40\n"
    "v_add_f32 v185, v185, v41\n"
    "v_mfma_f32_16x16x32_bf16 v[104:107], a[130:133], a[192:195], v[104:107]\n"
    "ds_read_b128 a[184:187], v14 offset:41856\n"
    "v_add_f32 v186, v186, v42\n"
    "v_mfma_f32_16x16x32_bf16 v[108:111], a[130:133], a[188:191], v[108:111]\n"
    "v_add_f32 v187, v187, v43\n"
    "v_add_f32 v188, v188, v44\n"
    "v_mfma_f32_16x16x32_bf16 v[112:115], a[130:133], a[220:223], v[112:115]\n"
    "buffer_store_dwordx4 v[32:35], v22, s[16:19], s26 offen sc1\n"
    "v_mfma_f32_16x16x32_bf16 v[116:119], a[130:133], a[228:231], v[116:119]\n"
    "v_add_f32 v189, v189, v45\n"
    "v_add_f32 v190, v190, v46\n"
    "v_mfma_f32_16x16x32_bf16 v[120:123], a[130:133], a[232:235], v[120:123]\n"
    "s_waitcnt vmcnt(6) lgkmcnt(1)\n"
    "s_barrier\n"
    "s_add_u32 m0, s38, 62208\n"
    "v_mfma_f32_16x16x32_bf16 v[124:127], a[130:133], a[212:215], v[124:127]\n"
    "ds_read_b128 a[204:207], v14 offset:20992\n"
    "v_add_u32 v5, 4294965248, v5\n"
    "v_mfma_f32_16x16x32_bf16 v[176:179], v[36:39], a[160:163], 0\n"
    "ds_read_b128 a[236:239], v14 offset:384\n"
    "v_add_f32 v191, v191, v47\n"
    "v_mfma_f32_16x16x32_bf16 v[180:183], v[36:39], a[164:167], 0\n"
    "ds_read_b64_tr_b16 a[170:171], v20 offset:28672\n"
    "v_cvt_pk_bf16_f32 v192, v184, v185\n"
    "v_mfma_f32_16x16x32_bf16 v[68:71], v[52:55], a[176:179], v[68:71]\n"
    "ds_read_b64_tr_b16 a[172:173], v20 offset:29184\n"
    "v_cvt_pk_bf16_f32 v193, v186, v187\n"
    "v_mfma_f32_16x16x32_bf16 v[76:79], v[52:55], a[224:227], v[76:79]\n"
    "buffer_load_dwordx4 v5, s[8:11], 0 offen lds\n"
    "v_mfma_f32_16x16x32_bf16 v[84:87], v[52:55], a[196:199], v[84:87]\n"
    "s_waitcnt lgkmcnt(4)\n"
    "s_add_u32 m0, s38, 66816\n"
    "ds_read_b64_tr_b16 a[168:169], v20 offset:24576\n"
    "v_mfma_f32_16x16x32_bf16 v[92:95], v[52:55], a[184:187], v[92:95]\n"
    "ds_read_b64_tr_b16 a[174:175], v20 offset:25088\n"
    "v_add_u32 v6, 4294965248, v6\n"
    "v_mfma_f32_16x16x32_bf16 v[96:99], a[138:141], a[216:219], v[96:99]\n"
    "ds_read_b64_tr_b16 a[92:93], v20 offset:24576\n"
    "v_cvt_pk_bf16_f32 v194, v188, v189\n"
    "v_mfma_f32_16x16x32_bf16 v[100:103], a[138:141], a[192:195], v[100:103]\n"
    "ds_read_b64_tr_b16 a[94:95], v20 offset:25088\n"
    "v_cvt_pk_bf16_f32 v195, v190, v191\n"
    "v_mfma_f32_16x16x32_bf16 v[104:107], a[138:141], a[188:191], v[104:107]\n"
    "buffer_load_dwordx4 v6, s[8:11], 0 offen lds\n"
    "v_mfma_f32_16x16x32_bf16 v[108:111], a[138:141], a[220:223], v[108:111]\n"
    "s_add_u32 m0, s38, 71424\n"
    "v_cvt_pk_bf16_f32 v48, v176, v177\n"
    "v_cvt_pk_bf16_f32 v49, v178, v179\n"
    "v_mfma_f32_16x16x32_bf16 v[112:115], a[138:141], a[228:231], v[112:115]\n"
    "v_cvt_pk_bf16_f32 v50, v180, v181\n"
    "v_add_u32 v7, 4294965248, v7\n"
    "v_mfma_f32_16x16x32_bf16 v[116:119], a[138:141], a[232:235], v[116:119]\n"
    "v_cvt_pk_bf16_f32 v51, v182, v183\n"
    "v_mfma_f32_16x16x32_bf16 v[120:123], a[138:141], a[212:215], v[120:123]\n"
    "s_waitcnt lgkmcnt(7)\n"
    "ds_read_b128 a[176:179], v14 offset:41984\n"
    "v_mfma_f32_16x16x32_bf16 v[124:127], a[138:141], a[204:207], v[124:127]\n"
    "buffer_load_dwordx4 v7, s[8:11], 0 offen lds\n"
    "v_mfma_f32_16x16x32_bf16 v[160:163], v[192:195], a[240:243], v[160:163]\n"
    "s_add_u32 m0, s38, 76032\n"
    "ds_read_b128 a[216:219], v14 offset:21120\n"
    "v_mfma_f32_16x16x32_bf16 v[164:167], v[192:195], a[248:251], v[164:167]\n"
    "ds_read_b64_tr_b16 a[130:131], v20 offset:36864\n"
    "v_add_u32 v8, 4294965248, v8\n"
    "v_mfma_f32_16x16x32_bf16 v[168:171], v[192:195], a[244:247], v[168:171]\n"
    "s_waitcnt lgkmcnt(9)\n"
    "ds_read_b64_tr_b16 a[132:133], v20 offset:37376\n"
    "v_mfma_f32_16x16x32_bf16 v[172:175], v[192:195], a[236:239], v[172:175]\n"
    "s_waitcnt lgkmcnt(7)\n"
    "ds_read_b64_tr_b16 a[128:129], v20 offset:32768\n"
    "v_mfma_f32_16x16x32_bf16 v[184:187], v[36:39], a[168:171], 0\n"
    "s_waitcnt lgkmcnt(7)\n"
    "buffer_load_dwordx4 v8, s[8:11], 0 offen lds\n"
    "v_mfma_f32_16x16x32_bf16 v[188:191], v[36:39], a[172:175], 0\n"
    "s_add_u32 m0, s7, 62208\n"
    "ds_read_b64_tr_b16 a[134:135], v20 offset:33280\n"
    "v_mfma_f32_16x16x32_bf16 v[64:67], v[48:51], a[208:211], v[64:67]\n"
    "ds_read_b64_tr_b16 a[96:97], v20 offset:32768\n"
    "v_add_u32 v9, 4294965248, v9\n"
    "v_mfma_f32_16x16x32_bf16 v[72:75], v[48:51], a[200:203], v[72:75]\n"
    "ds_read_b64_tr_b16 a[98:99], v20 offset:33280\n"
    "v_mfma_f32_16x16x32_bf16 v[80:83], v[48:51], a[180:183], v[80:83]\n"
    "s_waitcnt lgkmcnt(7)\n"
    "v_cvt_pk_bf16_f32 v52, v184, v185\n"
    "v_cvt_pk_bf16_f32 v53, v186, v187\n"
    "v_mfma_f32_16x16x32_bf16 v[88:91], v[48:51], a[176:179], v[88:91]\n"
    "buffer_load_dwordx4 v9, s[8:11], 0 offen lds\n"
    "v_mfma_f32_16x16x32_bf16 v[96:99], a[146:149], a[192:195], v[96:99]\n"
    "v_cvt_pk_bf16_f32 v54, v188, v189\n"
    "v_cvt_pk_bf16_f32 v55, v190, v191\n"
    "v_mfma_f32_16x16x32_bf16 v[100:103], a[146:149], a[188:191], v[100:103]\n"
    "v_add_f32 v176, v176, v184\n"
    "v_add_f32 v177, v177, v185\n"
    "v_mfma_f32_16x16x32_bf16 v[104:107], a[146:149], a[220:223], v[104:107]\n"
    "ds_read_b128 a[192:195], v14 offset:42112\n"
    "v_add_f32 v178, v178, v186\n"
    "v_mfma_f32_16x16x32_bf16 v[108:111], a[146:149], a[228:231], v[108:111]\n"
    "buffer_store_dwordx4 v[32:35], v22, s[16:19], s27 offen sc1\n"
    "v_mfma_f32_16x16x32_bf16 v[112:115], a[146:149], a[232:235], v[112:115]\n"
    "v_add_f32 v179, v179, v187\n"
    "v_add_f32 v180, v180, v188\n"
    "v_mfma_f32_16x16x32_bf16 v[116:119], a[146:149], a[212:215], v[116:119]\n"
    "v_add_f32 v181, v181, v189\n"
    "v_add_f32 v182, v182, v190\n"
    "v_mfma_f32_16x16x32_bf16 v[120:123], a[146:149], a[204:207], v[120:123]\n"
    "s_waitcnt lgkmcnt(7)\n"
    "ds_read_b128 a[208:211], v14 offset:21248\n"
    "v_add_f32 v183, v183, v191\n"
    "v_mfma_f32_16x16x32_bf16 v[124:127], a[146:149], a[216:219], v[124:127]\n"
    "s_waitcnt lgkmcnt(5)\n"
    "ds_read_b128 a[240:243], v14 offset:640\n"
    "v_cvt_pk_bf16_f32 v192, v176, v177\n"
    "v_mfma_f32_16x16x32_bf16 v[40:43], v[36:39], a[128:131], 0\n"
    "s_waitcnt lgkmcnt(5)\n"
    "ds_read_b64_tr_b16 a[138:139], v20 offset:45056\n"
    "v_cvt_pk_bf16_f32 v193, v178, v179\n"
    "v_mfma_f32_16x16x32_bf16 v[44:47], v[36:39], a[132:135], 0\n"
    "buffer_store_dwordx4 v[32:35], v22, s[16:19], s28 offen sc1\n"
    "v_mfma_f32_16x16x32_bf16 v[68:71], v[52:55], a[224:227], v[68:71]\n"
    "ds_read_b64_tr_b16 a[140:141], v20 offset:45568\n"
    "v_cvt_pk_bf16_f32 v194, v180, v181\n"
    "v_mfma_f32_16x16x32_bf16 v[76:79], v[52:55], a[196:199], v[76:79]\n"
    "ds_read_b64_tr_b16 a[136:137], v20 offset:40960\n"
    "v_cvt_pk_bf16_f32 v195, v182, v183\n"
    "v_mfma_f32_16x16x32_bf16 v[84:87], v[52:55], a[184:187], v[84:87]\n"
    "s_waitcnt lgkmcnt(5)\n"
    "ds_read_b64_tr_b16 a[142:143], v20 offset:41472\n"
    "v_cvt_pk_bf16_f32 v48, v40, v41\n"
    "v_mfma_f32_16x16x32_bf16 v[92:95], v[52:55], a[192:195], v[92:95]\n"
    "ds_read_b64_tr_b16 a[100:101], v20 offset:40960\n"
    "v_cvt_pk_bf16_f32 v49, v42, v43\n"
    "v_mfma_f32_16x16x32_bf16 v[96:99], a[154:157], a[188:191], v[96:99]\n"
    "buffer_store_dwordx4 v[32:35], v22, s[16:19], s29 offen sc1\n"
    "v_mfma_f32_16x16x32_bf16 v[100:103], a[154:157], a[220:223], v[100:103]\n"
    "ds_read_b64_tr_b16 a[102:103], v20 offset:41472\n"
    "v_cvt_pk_bf16_f32 v50, v44, v45\n"
    "v_mfma_f32_16x16x32_bf16 v[104:107], a[154:157], a[228:231], v[104:107]\n"
    "v_cvt_pk_bf16_f32 v51, v46, v47\n"
    "v_mfma_f32_16x16x32_bf16 v[108:111], a[154:157], a[232:235], v[108:111]\n"
    "v_mfma_f32_16x16x32_bf16 v[112:115], a[154:157], a[212:215], v[112:115]\n"
    "buffer_store_dwordx4 v[32:35], v22, s[16:19], s30 offen sc1\n"
    "v_mfma_f32_16x16x32_bf16 v[116:119], a[154:157], a[204:207], v[116:119]\n"
    "v_mfma_f32_16x16x32_bf16 v[120:123], a[154:157], a[216:219], v[120:123]\n"
    "s_waitcnt lgkmcnt(7)\n"
    "ds_read_b128 a[188:191], v14 offset:42240\n"
    "v_mfma_f32_16x16x32_bf16 v[124:127], a[154:157], a[208:211], v[124:127]\n"
    "ds_read_b128 a[224:227], v14 offset:21376\n"
    "v_mfma_f32_16x16x32_bf16 v[160:163], v[192:195], a[248:251], v[160:163]\n"
    "ds_read_b64_tr_b16 a[146:147], v20 offset:53248\n"
    "v_mfma_f32_16x16x32_bf16 v[164:167], v[192:195], a[244:247], v[164:167]\n"
    "ds_read_b64_tr_b16 a[148:149], v20 offset:53760\n"
    "v_mfma_f32_16x16x32_bf16 v[168:171], v[192:195], a[236:239], v[168:171]\n"
    "s_waitcnt lgkmcnt(10)\n"
    "buffer_store_dwordx4 v[32:35], v22, s[16:19], s31 offen sc1\n"
    "v_mfma_f32_16x16x32_bf16 v[172:175], v[192:195], a[240:243], v[172:175]\n"
    "s_waitcnt lgkmcnt(7)\n"
    "ds_read_b64_tr_b16 a[144:145], v20 offset:49152\n"
    "v_mfma_f32_16x16x32_bf16 v[176:179], v[36:39], a[136:139], 0\n"
    "s_waitcnt lgkmcnt(7)\n"
    "ds_read_b64_tr_b16 a[150:151], v20 offset:49664\n"
    "v_mfma_f32_16x16x32_bf16 v[180:183], v[36:39], a[140:143], 0\n"
    "ds_read_b64_tr_b16 a[104:105], v20 offset:49152\n"
    "v_mfma_f32_16x16x32_bf16 v[64:67], v[48:51], a[200:203], v[64:67]\n"
    "ds_read_b64_tr_b16 a[106:107], v20 offset:49664\n"
    "v_mfma_f32_16x16x32_bf16 v[72:75], v[48:51], a[180:183], v[72:75]\n"
    "v_mfma_f32_16x16x32_bf16 v[80:83], v[48:51], a[176:179], v[80:83]\n"
    "s_waitcnt lgkmcnt(7)\n"
    "v_cvt_pk_bf16_f32 v52, v176, v177\n"
    "v_cvt_pk_bf16_f32 v53, v178, v179\n"
    "v_mfma_f32_16x16x32_bf16 v[88:91], v[48:51], a[188:191], v[88:91]\n"
    "v_cvt_pk_bf16_f32 v54, v180, v181\n"
    "v_cvt_pk_bf16_f32 v55, v182, v183\n"
    "v_mfma_f32_16x16x32_bf16 v[96:99], a[162:165], a[220:223], v[96:99]\n"
    "ds_read_b128 a[200:203], v14 offset:42368\n"
    "v_add_f32 v40, v40, v176\n"
    "v_mfma_f32_16x16x32_bf16 v[100:103], a[162:165], a[228:231], v[100:103]\n"
    "ds_read_b128 a[220:223], v14 offset:21504\n"
    "v_add_f32 v41, v41, v177\n"
    "v_mfma_f32_16x16x32_bf16 v[104:107], a[162:165], a[232:235], v[104:107]\n"
    "v_add_f32 v42, v42, v178\n"
    "v_add_f32 v43, v43, v179\n"
    "v_mfma_f32_16x16x32_bf16 v[108:111], a[162:165], a[212:215], v[108:111]\n"
    "ds_read_b128 a[248:251], v14 offset:896\n"
    "v_add_f32 v44, v44, v180\n"
    "v_mfma_f32_16x16x32_bf16 v[112:115], a[162:165], a[204:207], v[112:115]\n"
    "ds_read_b64_tr_b16 a[154:155], v20 offset:61440\n"
    "v_add_f32 v45, v45, v181\n"
    "v_mfma_f32_16x16x32_bf16 v[116:119], a[162:165], a[216:219], v[116:119]\n"
    "ds_read_b64_tr_b16 a[156:157], v20 offset:61952\n"
    "v_add_f32 v46, v46, v182\n"
    "v_mfma_f32_16x16x32_bf16 v[120:123], a[162:165], a[208:211], v[120:123]\n"
    "s_waitcnt lgkmcnt(11)\n"
    "ds_read_b64_tr_b16 a[152:153], v20 offset:57344\n"
    "v_add_f32 v47, v47, v183\n"
    "v_mfma_f32_16x16x32_bf16 v[124:127], a[162:165], a[224:227], v[124:127]\n"
    "s_waitcnt lgkmcnt(9)\n"
    "v_cvt_pk_bf16_f32 v192, v40, v41\n"
    "v_cvt_pk_bf16_f32 v193, v42, v43\n"
    "v_mfma_f32_16x16x32_bf16 v[184:187], v[36:39], a[144:147], 0\n"
    "s_waitcnt lgkmcnt(8)\n"
    "ds_read_b64_tr_b16 a[158:159], v20 offset:57856\n"
    "v_cvt_pk_bf16_f32 v194, v44, v45\n"
    "v_mfma_f32_16x16x32_bf16 v[188:191], v[36:39], a[148:151], 0\n"
    "ds_read_b64_tr_b16 a[108:109], v20 offset:57344\n"
    "v_cvt_pk_bf16_f32 v195, v46, v47\n"
    "v_mfma_f32_16x16x32_bf16 v[68:71], v[52:55], a[196:199], v[68:71]\n"
    "ds_read_b64_tr_b16 a[110:111], v20 offset:57856\n"
    "v_mfma_f32_16x16x32_bf16 v[76:79], v[52:55], a[184:187], v[76:79]\n"
    "s_waitcnt vmcnt(5) lgkmcnt(9)\n"
    "s_barrier\n"
    "ds_read_b64_tr_b16 v[200:201], v18 offset:62208\n"
    "v_mfma_f32_16x16x32_bf16 v[84:87], v[52:55], a[192:195], v[84:87]\n"
    "s_waitcnt lgkmcnt(9)\n"
    "v_cvt_pk_bf16_f32 v48, v184, v185\n"
    "v_cvt_pk_bf16_f32 v49, v186, v187\n"
    "v_mfma_f32_16x16x32_bf16 v[92:95], v[52:55], a[200:203], v[92:95]\n"
    "ds_read_b64_tr_b16 v[202:203], v18 offset:0\n"
    "v_cvt_pk_bf16_f32 v50, v188, v189\n"
    "v_mfma_f32_16x16x32_bf16 v[96:99], a[170:173], a[228:231], v[96:99]\n"
    "ds_read_b64 v[216:217], v62 offset:62208\n"
    "v_cvt_pk_bf16_f32 v51, v190, v191\n"
    "v_mfma_f32_16x16x32_bf16 v[100:103], a[170:173], a[232:235], v[100:103]\n"
    "ds_read_b64 v[218:219], v62\n"
    "v_mfma_f32_16x16x32_bf16 v[104:107], a[170:173], a[212:215], v[104:107]\n"
    "ds_read_b64_tr_b16 v[204:205], v18 offset:62336\n"
    "v_mfma_f32_16x16x32_bf16 v[108:111], a[170:173], a[204:207], v[108:111]\n"
    "v_mfma_f32_16x16x32_bf16 v[112:115], a[170:173], a[216:219], v[112:115]\n"
    "ds_read_b64_tr_b16 v[206:207], v18 offset:128\n"
    "v_mfma_f32_16x16x32_bf16 v[116:119], a[170:173], a[208:211], v[116:119]\n"
    "ds_read_b128 a[196:199], v15 offset:41472\n"
    "v_mfma_f32_16x16x32_bf16 v[120:123], a[170:173], a[224:227], v[120:123]\n"
    "s_waitcnt lgkmcnt(14)\n"
    "ds_read_b64_tr_b16 v[208:209], v18 offset:62464\n"
    "v_mfma_f32_16x16x32_bf16 v[124:127], a[170:173], a[220:223], v[124:127]\n"
    "s_waitcnt lgkmcnt(10)\n"
    "ds_read_b64_tr_b16 v[210:211], v18 offset:256\n"
    "v_mfma_f32_16x16x32_bf16 v[160:163], v[192:195], a[244:247], v[160:163]\n"
    "v_mfma_f32_16x16x32_bf16 v[164:167], v[192:195], a[236:239], v[164:167]\n"
    "ds_read_b64_tr_b16 v[212:213], v18 offset:62592\n"
    "v_mfma_f32_16x16x32_bf16 v[168:171], v[192:195], a[240:243], v[168:171]\n"
    "ds_read_b64_tr_b16 v[214:215], v18 offset:384\n"
    "v_mfma_f32_16x16x32_bf16 v[172:175], v[192:195], a[248:251], v[172:175]\n"
    "ds_read_b128 a[228:231], v14 offset:21632\n"
    "v_mfma_f32_16x16x32_bf16 v[40:43], v[36:39], a[152:155], 0\n"
    "ds_read_b64_tr_b16 a[162:163], v21 offset:4096\n"
    "v_mfma_f32_16x16x32_bf16 v[44:47], v[36:39], a[156:159], 0\n"
    "v_mfma_f32_16x16x32_bf16 v[64:67], v[48:51], a[180:183], v[64:67]\n"
    "s_waitcnt lgkmcnt(10)\n"
    "ds_read_b64_tr_b16 a[164:165], v21 offset:4608\n"
    "v_mfma_f32_16x16x32_bf16 v[72:75], v[48:51], a[176:179], v[72:75]\n"
    "ds_read_b64_tr_b16 a[160:161], v21 offset:0\n"
    "v_mfma_f32_16x16x32_bf16 v[128:131], v[200:203], v[196:199], 0\n"
    "s_waitcnt lgkmcnt(11)\n"
    "ds_read_b64_tr_b16 a[166:167], v21 offset:512\n"
    "v_cvt_pk_bf16_f32 v52, v40, v41\n"
    "v_mfma_f32_16x16x32_bf16 v[80:83], v[48:51], a[188:191], v[80:83]\n"
    "s_waitcnt lgkmcnt(10)\n"
    "ds_read_b64_tr_b16 a[112:113], v21 offset:0\n"
    "v_cvt_pk_bf16_f32 v53, v42, v43\n"
    "v_mfma_f32_16x16x32_bf16 v[132:135], v[204:207], v[196:199], 0\n"
    "s_waitcnt lgkmcnt(10)\n"
    "v_lshlrev_b32 v240, 16, v216\n"
    "v_and_b32 v241, s48, v216\n"
    "v_mfma_f32_16x16x32_bf16 v[88:91], v[48:51], a[196:199], v[88:91]\n"
    "s_waitcnt lgkmcnt(8)\n"
    "ds_read_b64_tr_b16 a[114:115], v21 offset:512\n"
    "v_cvt_pk_bf16_f32 v54, v44, v45\n"
    "v_mfma_f32_16x16x32_bf16 v[136:139], v[208:211], v[196:199], 0\n"
    "v_cvt_pk_bf16_f32 v55, v46, v47\n"
    "v_lshlrev_b32 v242, 16, v217\n"
    "v_mfma_f32_16x16x32_bf16 v[96:99], a[130:133], a[232:235], v[96:99]\n"
    "s_waitcnt lgkmcnt(7)\n"
    "v_and_b32 v243, s48, v217\n"
    "v_lshlrev_b32 v244, 16, v218\n"
    "v_mfma_f32_16x16x32_bf16 v[140:143], v[212:215], v[196:199], 0\n"
    "v_and_b32 v245, s48, v218\n"
    "v_lshlrev_b32 v246, 16, v219\n"
    "v_mfma_f32_16x16x32_bf16 v[100:103], a[130:133], a[212:215], v[100:103]\n"
    "v_and_b32 v247, s48, v219\n"
    "v_sub_f32 v240, v240, v244\n"
    "v_mfma_f32_16x16x32_bf16 v[104:107], a[130:133], a[204:207], v[104:107]\n"
    "ds_read_b128 a[180:183], v15 offset:41600\n"
    "v_sub_f32 v241, v241, v245\n"
    "v_mfma_f32_16x16x32_bf16 v[108:111], a[130:133], a[216:219], v[108:111]\n"
    "v_sub_f32 v242, v242, v246\n"
    "v_sub_f32 v243, v243, v247\n"
    "v_mfma_f32_16x16x32_bf16 v[112:115], a[130:133], a[208:211], v[112:115]\n"
    "v_cvt_pk_bf16_f32 v240, v240, v241\n"
    "v_cvt_pk_bf16_f32 v241, v242, v243\n"
    "v_mfma_f32_16x16x32_bf16 v[116:119], a[130:133], a[224:227], v[116:119]\n"
    "ds_write_b64 v62, v[240:241] offset:62208\n"
    "v_cvt_pk_bf16_f32 v128, v128, v129\n"
    "v_mfma_f32_16x16x32_bf16 v[120:123], a[130:133], a[220:223], v[120:123]\n"
    "s_waitcnt lgkmcnt(8)\n"
    "v_cvt_pk_bf16_f32 v129, v130, v131\n"
    "v_add_f32 v184, v184, v40\n"
    "v_mfma_f32_16x16x32_bf16 v[124:127], a[130:133], a[228:231], v[124:127]\n"
    "s_waitcnt lgkmcnt(5)\n"
    "ds_read_b128 a[232:235], v15 offset:20736\n"
    "v_add_f32 v185, v185, v41\n"
    "v_mfma_f32_16x16x32_bf16 v[176:179], v[36:39], a[160:163], 0\n"
    "s_waitcnt lgkmcnt(5)\n"
    "ds_read_b128 a[244:247], v15 offset:128\n"
    "v_add_f32 v186, v186, v42\n"
    "v_mfma_f32_16x16x32_bf16 v[180:183], v[36:39], a[164:167], 0\n"
    "ds_read_b64_tr_b16 a[170:171], v21 offset:12288\n"
    "v_add_f32 v187, v187, v43\n"
    "v_mfma_f32_16x16x32_bf16 v[68:71], v[52:55], a[184:187], v[68:71]\n"
    "ds_read_b64_tr_b16 a[172:173], v21 offset:12800\n"
    "v_add_f32 v188, v188, v44\n"
    "v_mfma_f32_16x16x32_bf16 v[76:79], v[52:55], a[192:195], v[76:79]\n"
    "ds_write_b64 v60, v[128:129] offset:62208\n"
    "v_cvt_pk_bf16_f32 v132, v132, v133\n"
    "v_mfma_f32_16x16x32_bf16 v[84:87], v[52:55], a[200:203], v[84:87]\n"
    "s_waitcnt lgkmcnt(6)\n"
    "ds_read_b64_tr_b16 a[168:169], v21 offset:8192\n"
    "v_cvt_pk_bf16_f32 v133, v134, v135\n"
    "v_mfma_f32_16x16x32_bf16 v[92:95], v[52:55], a[180:183], v[92:95]\n"
    "ds_read_b64_tr_b16 a[174:175], v21 offset:8704\n"
    "v_add_f32 v189, v189, v45\n"
    "v_mfma_f32_16x16x32_bf16 v[96:99], a[138:141], a[212:215], v[96:99]\n"
    "ds_read_b64_tr_b16 a[116:117], v21 offset:8192\n"
    "v_add_f32 v190, v190, v46\n"
    "v_mfma_f32_16x16x32_bf16 v[100:103], a[138:141], a[204:207], v[100:103]\n"
    "ds_read_b64_tr_b16 a[118:119], v21 offset:8704\n"
    "v_add_f32 v191, v191, v47\n"
    "v_mfma_f32_16x16x32_bf16 v[104:107], a[138:141], a[216:219], v[104:107]\n"
    "ds_write_b64 v60, v[132:133] offset:62336\n"
    "v_cvt_pk_bf16_f32 v136, v136, v137\n"
    "v_mfma_f32_16x16x32_bf16 v[108:111], a[138:141], a[208:211], v[108:111]\n"
    "v_cvt_pk_bf16_f32 v137, v138, v139\n"
    "v_cvt_pk_bf16_f32 v192, v184, v185\n"
    "v_mfma_f32_16x16x32_bf16 v[112:115], a[138:141], a[224:227], v[112:115]\n"
    "ds_write_b64 v60, v[136:137] offset:62464\n"
    "v_cvt_pk_bf16_f32 v140, v140, v141\n"
    "v_mfma_f32_16x16x32_bf16 v[116:119], a[138:141], a[220:223], v[116:119]\n"
    "ds_read_b128 a[184:187], v15 offset:41728\n"
    "v_cvt_pk_bf16_f32 v141, v142, v143\n"
    "v_mfma_f32_16x16x32_bf16 v[120:123], a[138:141], a[228:231], v[120:123]\n"
    "s_waitcnt lgkmcnt(11)\n"
    "ds_read_b128 a[212:215], v15 offset:20864\n"
    "v_cvt_pk_bf16_f32 v193, v186, v187\n"
    "v_mfma_f32_16x16x32_bf16 v[124:127], a[138:141], a[232:235], v[124:127]\n"
    "ds_write_b64 v60, v[140:141] offset:62592\n"
    "v_cvt_pk_bf16_f32 v194, v188, v189\n"
    "v_cvt_pk_bf16_f32 v195, v190, v191\n"
    "s_nop 1\n"
    "v_mfma_f32_16x16x32_bf16 v[160:163], v[192:195], a[236:239], v[160:163]\n"
    "ds_read_b64_tr_b16 a[130:131], v21 offset:20480\n"
    "v_cvt_pk_bf16_f32 v48, v176, v177\n"
    "v_mfma_f32_16x16x32_bf16 v[164:167], v[192:195], a[240:243], v[164:167]\n"
    "ds_read_b64_tr_b16 a[132:133], v21 offset:20992\n"
    "v_cvt_pk_bf16_f32 v49, v178, v179\n"
    "v_mfma_f32_16x16x32_bf16 v[168:171], v[192:195], a[248:251], v[168:171]\n"
    "s_waitcnt lgkmcnt(14)\n"
    "ds_read_b64_tr_b16 a[128:129], v21 offset:16384\n"
    "v_cvt_pk_bf16_f32 v50, v180, v181\n"
    "v_mfma_f32_16x16x32_bf16 v[172:175], v[192:195], a[244:247], v[172:175]\n"
    "s_waitcnt lgkmcnt(11)\n"
    "ds_read_b64_tr_b16 a[134:135], v21 offset:16896\n"
    "v_cvt_pk_bf16_f32 v51, v182, v183\n"
    "v_mfma_f32_16x16x32_bf16 v[184:187], v[36:39], a[168:171], 0\n"
    "s_waitcnt lgkmcnt(11)\n"
    "v_mfma_f32_16x16x32_bf16 v[188:191], v[36:39], a[172:175], 0\n"
    "ds_read_b64_tr_b16 a[120:121], v21 offset:16384\n"
    "v_mfma_f32_16x16x32_bf16 v[64:67], v[48:51], a[176:179], v[64:67]\n"
    "s_waitcnt vmcnt(0)\n"
    "ds_read_b64_tr_b16 a[122:123], v21 offset:16896\n"
    "v_lshl_add_u64 v[26:27], v[26:27], 0, 1\n"
    "v_mfma_f32_16x16x32_bf16 v[72:75], v[48:51], a[188:191], v[72:75]\n"
    "ds_read_b64_tr_b16 v[200:201], v19 offset:62208\n"
    "v_mfma_f32_16x16x32_bf16 v[80:83], v[48:51], a[196:199], v[80:83]\n"
    "s_waitcnt lgkmcnt(9)\n"
    "ds_read_b64_tr_b16 v[202:203], v19 offset:0\n"
    "v_cvt_pk_bf16_f32 v52, v184, v185\n"
    "v_mfma_f32_16x16x32_bf16 v[88:91], v[48:51], a[184:187], v[88:91]\n"
    "buffer_store_dwordx2 v[26:27], v24, s[16:19], s40 offen sc1\n"
    "v_mfma_f32_16x16x32_bf16 v[96:99], a[146:149], a[204:207], v[96:99]\n"
    "ds_read_b64_tr_b16 v[204:205], v19 offset:62336\n"
    "v_cvt_pk_bf16_f32 v53, v186, v187\n"
    "v_mfma_f32_16x16x32_bf16 v[100:103], a[146:149], a[216:219], v[100:103]\n"
    "ds_read_b64_tr_b16 v[206:207], v19 offset:128\n"
    "v_cvt_pk_bf16_f32 v54, v188, v189\n"
    "v_mfma_f32_16x16x32_bf16 v[104:107], a[146:149], a[208:211], v[104:107]\n"
    "ds_read_b128 a[176:179], v15 offset:41856\n"
    "v_cvt_pk_bf16_f32 v55, v190, v191\n"
    "v_mfma_f32_16x16x32_bf16 v[108:111], a[146:149], a[224:227], v[108:111]\n"
    "ds_read_b64_tr_b16 v[208:209], v19 offset:62464\n"
    "v_add_f32 v176, v176, v184\n"
    "v_mfma_f32_16x16x32_bf16 v[112:115], a[146:149], a[220:223], v[112:115]\n"
    "v_add_f32 v177, v177, v185\n"
    "v_add_f32 v178, v178, v186\n"
    "v_mfma_f32_16x16x32_bf16 v[116:119], a[146:149], a[228:231], v[116:119]\n"
    "ds_read_b64_tr_b16 v[210:211], v19 offset:256\n"
    "v_add_f32 v179, v179, v187\n"
    "v_mfma_f32_16x16x32_bf16 v[120:123], a[146:149], a[232:235], v[120:123]\n"
    "s_waitcnt lgkmcnt(14)\n"
    "ds_read_b64_tr_b16 v[212:213], v19 offset:62592\n"
    "v_add_f32 v180, v180, v188\n"
    "v_mfma_f32_16x16x32_bf16 v[124:127], a[146:149], a[212:215], v[124:127]\n"
    "s_waitcnt lgkmcnt(11)\n"
    "ds_read_b64_tr_b16 v[214:215], v19 offset:384\n"
    "v_add_f32 v181, v181, v189\n"
    "v_mfma_f32_16x16x32_bf16 v[40:43], v[36:39], a[128:131], 0\n"
    "s_waitcnt lgkmcnt(11)\n"
    "v_add_f32 v182, v182, v190\n"
    "v_add_f32 v183, v183, v191\n"
    "v_mfma_f32_16x16x32_bf16 v[44:47], v[36:39], a[132:135], 0\n"
    "v_cvt_pk_bf16_f32 v192, v176, v177\n"
    "v_cvt_pk_bf16_f32 v193, v178, v179\n"
    "v_mfma_f32_16x16x32_bf16 v[68:71], v[52:55], a[192:195], v[68:71]\n"
    "ds_read_b128 a[192:195], v15 offset:20992\n"
    "v_cvt_pk_bf16_f32 v194, v180, v181\n"
    "v_mfma_f32_16x16x32_bf16 v[76:79], v[52:55], a[200:203], v[76:79]\n"
    "s_waitcnt lgkmcnt(8)\n"
    "ds_read_b128 a[204:207], v15 offset:384\n"
    "v_cvt_pk_bf16_f32 v195, v182, v183\n"
    "v_mfma_f32_16x16x32_bf16 v[128:131], v[200:203], v[196:199], 0\n"
    "ds_read_b64_tr_b16 a[138:139], v21 offset:28672\n"
    "v_cvt_pk_bf16_f32 v48, v40, v41\n"
    "v_mfma_f32_16x16x32_bf16 v[84:87], v[52:55], a[180:183], v[84:87]\n"
    "s_waitcnt lgkmcnt(8)\n"
    "ds_read_b64_tr_b16 a[140:141], v21 offset:29184\n"
    "v_cvt_pk_bf16_f32 v49, v42, v43\n"
    "v_mfma_f32_16x16x32_bf16 v[132:135], v[204:207], v[196:199], 0\n"
    "s_waitcnt lgkmcnt(8)\n"
    "v_cvt_pk_bf16_f32 v50, v44, v45\n"
    "v_cvt_pk_bf16_f32 v51, v46, v47\n"
    "v_mfma_f32_16x16x32_bf16 v[92:95], v[52:55], a[176:179], v[92:95]\n"
    "s_waitcnt lgkmcnt(6)\n"
    "ds_read_b64_tr_b16 a[136:137], v21 offset:24576\n"
    "v_mfma_f32_16x16x32_bf16 v[136:139], v[208:211], v[196:199], 0\n"
    "ds_read_b64_tr_b16 a[142:143], v21 offset:25088\n"
    "v_cvt_pk_bf16_f32 v128, v128, v129\n"
    "v_mfma_f32_16x16x32_bf16 v[96:99], a[154:157], a[216:219], v[96:99]\n"
    "s_waitcnt lgkmcnt(6)\n"
    "ds_read_b64_tr_b16 a[124:125], v21 offset:24576\n"
    "v_cvt_pk_bf16_f32 v129, v130, v131\n"
    "v_mfma_f32_16x16x32_bf16 v[140:143], v[212:215], v[196:199], 0\n"
    "ds_read_b64_tr_b16 a[126:127], v21 offset:25088\n"
    "v_mfma_f32_16x16x32_bf16 v[100:103], a[154:157], a[208:211], v[100:103]\n"
    "ds_write_b64 v61, v[128:129] offset:62208\n"
    "v_cvt_pk_bf16_f32 v132, v132, v133\n"
    "v_mfma_f32_16x16x32_bf16 v[104:107], a[154:157], a[224:227], v[104:107]\n"
    "v_cvt_pk_bf16_f32 v133, v134, v135\n"
    "v_mfma_f32_16x16x32_bf16 v[108:111], a[154:157], a[220:223], v[108:111]\n"
    "ds_write_b64 v61, v[132:133] offset:62336\n"
    "v_cvt_pk_bf16_f32 v136, v136, v137\n"
    "v_mfma_f32_16x16x32_bf16 v[112:115], a[154:157], a[228:231], v[112:115]\n"
    "v_cvt_pk_bf16_f32 v137, v138, v139\n"
    "v_mfma_f32_16x16x32_bf16 v[116:119], a[154:157], a[232:235], v[116:119]\n"
    "ds_read_b128 a[216:219], v15 offset:41984\n"
    "v_mfma_f32_16x16x32_bf16 v[120:123], a[154:157], a[212:215], v[120:123]\n"
    "s_waitcnt lgkmcnt(10)\n"
    "ds_write_b64 v61, v[136:137] offset:62464\n"
    "v_cvt_pk_bf16_f32 v140, v140, v141\n"
    "v_mfma_f32_16x16x32_bf16 v[124:127], a[154:157], a[192:195], v[124:127]\n"
    "v_cvt_pk_bf16_f32 v141, v142, v143\n"
    "v_mfma_f32_16x16x32_bf16 v[160:163], v[192:195], a[240:243], v[160:163]\n"
    "ds_write_b64 v61, v[140:141] offset:62592\n"
    "v_mfma_f32_16x16x32_bf16 v[164:167], v[192:195], a[248:251], v[164:167]\n"
    "v_mfma_f32_16x16x32_bf16 v[168:171], v[192:195], a[244:247], v[168:171]\n"
    "s_waitcnt lgkmcnt(11)\n"
    "v_mfma_f32_16x16x32_bf16 v[172:175], v[192:195], a[204:207], v[172:175]\n"
    "s_waitcnt lgkmcnt(8)\n"
    "v_mfma_f32_16x16x32_bf16 v[176:179], v[36:39], a[136:139], 0\n"
    "s_waitcnt lgkmcnt(7)\n"
    "v_mfma_f32_16x16x32_bf16 v[180:183], v[36:39], a[140:143], 0\n"
    "v_mfma_f32_16x16x32_bf16 v[64:67], v[48:51], a[188:191], v[64:67]\n"
    "ds_read_b128 a[188:191], v15 offset:21120\n"
    "v_mfma_f32_16x16x32_bf16 v[72:75], v[48:51], a[196:199], v[72:75]\n"
    "v_mfma_f32_16x16x32_bf16 v[80:83], v[48:51], a[184:187], v[80:83]\n"
    "s_waitcnt lgkmcnt(3)\n"
    "s_nop 0\n"
    "v_cvt_pk_bf16_f32 v52, v176, v177\n"
    "v_cvt_pk_bf16_f32 v53, v178, v179\n"
    "v_mfma_f32_16x16x32_bf16 v[88:91], v[48:51], a[216:219], v[88:91]\n"
    "v_cvt_pk_bf16_f32 v54, v180, v181\n"
    "v_cvt_pk_bf16_f32 v55, v182, v183\n"
    "v_mfma_f32_16x16x32_bf16 v[96:99], a[162:165], a[208:211], v[96:99]\n"
    "ds_read_b128 a[184:187], v15 offset:42112\n"
    "v_add_f32 v40, v40, v176\n"
    "v_mfma_f32_16x16x32_bf16 v[100:103], a[162:165], a[224:227], v[100:103]\n"
    "v_add_f32 v41, v41, v177\n"
    "v_add_f32 v42, v42, v178\n"
    "v_mfma_f32_16x16x32_bf16 v[104:107], a[162:165], a[220:223], v[104:107]\n"
    "v_add_f32 v43, v43, v179\n"
    "v_add_f32 v44, v44, v180\n"
    "v_mfma_f32_16x16x32_bf16 v[108:111], a[162:165], a[228:231], v[108:111]\n"
    "v_add_f32 v45, v45, v181\n"
    "v_add_f32 v46, v46, v182\n"
    "v_mfma_f32_16x16x32_bf16 v[112:115], a[162:165], a[232:235], v[112:115]\n"
    "v_add_f32 v47, v47, v183\n"
    "v_cvt_pk_bf16_f32 v192, v40, v41\n"
    "v_mfma_f32_16x16x32_bf16 v[116:119], a[162:165], a[212:215], v[116:119]\n"
    "v_cvt_pk_bf16_f32 v193, v42, v43\n"
    "v_cvt_pk_bf16_f32 v194, v44, v45\n"
    "v_mfma_f32_16x16x32_bf16 v[120:123], a[162:165], a[192:195], v[120:123]\n"
    "s_waitcnt lgkmcnt(1)\n"
    "v_cvt_pk_bf16_f32 v195, v46, v47\n"
    "v_mfma_f32_16x16x32_bf16 v[124:127], a[162:165], a[188:191], v[124:127]\n"
    "ds_read_b128 a[196:199], v15 offset:21248\n"
    "v_mfma_f32_16x16x32_bf16 v[68:71], v[52:55], a[200:203], v[68:71]\n"
    "v_mfma_f32_16x16x32_bf16 v[76:79], v[52:55], a[180:183], v[76:79]\n"
    "v_mfma_f32_16x16x32_bf16 v[84:87], v[52:55], a[176:179], v[84:87]\n"
    "s_waitcnt lgkmcnt(1)\n"
    "buffer_load_dwordx2 v[28:29], v23, s[16:19], s40 offen offset:0 sc1\n"
    "v_mfma_f32_16x16x32_bf16 v[92:95], v[52:55], a[184:187], v[92:95]\n"
    "v_mfma_f32_16x16x32_bf16 v[96:99], a[170:173], a[224:227], v[96:99]\n"
    "ds_read_b128 a[176:179], v15 offset:640\n"
    "v_mfma_f32_16x16x32_bf16 v[100:103], a[170:173], a[220:223], v[100:103]\n"
    "v_mfma_f32_16x16x32_bf16 v[104:107], a[170:173], a[228:231], v[104:107]\n"
    "v_mfma_f32_16x16x32_bf16 v[108:111], a[170:173], a[232:235], v[108:111]\n"
    "v_mfma_f32_16x16x32_bf16 v[112:115], a[170:173], a[212:215], v[112:115]\n"
    "v_mfma_f32_16x16x32_bf16 v[116:119], a[170:173], a[192:195], v[116:119]\n"
    "v_mfma_f32_16x16x32_bf16 v[120:123], a[170:173], a[188:191], v[120:123]\n"
    "s_waitcnt lgkmcnt(1)\n"
    "v_mfma_f32_16x16x32_bf16 v[124:127], a[170:173], a[196:199], v[124:127]\n"
    "v_mfma_f32_16x16x32_bf16 v[160:163], v[192:195], a[248:251], v[160:163]\n"
    "ds_read_b128 a[180:183], v15 offset:21376\n"
    "v_mfma_f32_16x16x32_bf16 v[164:167], v[192:195], a[244:247], v[164:167]\n"
    "v_mfma_f32_16x16x32_bf16 v[168:171], v[192:195], a[204:207], v[168:171]\n"
    "s_waitcnt lgkmcnt(1)\n"
    "v_mfma_f32_16x16x32_bf16 v[172:175], v[192:195], a[176:179], v[172:175]\n"
    "v_mfma_f32_16x16x32_bf16 v[96:99], a[130:133], a[220:223], v[96:99]\n"
    "v_mfma_f32_16x16x32_bf16 v[100:103], a[130:133], a[228:231], v[100:103]\n"
    "s_setpc_b64 s[52:53]\n"
    ".fill (LP0_%=+12288-.)/4, 4, 0xbf800000\n"
    "LP1_%=:\n"
    "s_waitcnt vmcnt(11)\n"
    "s_barrier\n"
    "ds_read_b128 a[176:179], v10 offset:0\n"
    "ds_read_b64_tr_b16 a[0:1], v20 offset:40960\n"
    "ds_read_b64_tr_b16 a[2:3], v20 offset:41472\n"
    "ds_read_b128 a[180:183], v10 offset:128\n"
    "ds_read_b64_tr_b16 a[138:139], v20 offset:45056\n"
    "ds_read_b64_tr_b16 a[140:141], v20 offset:45568\n"
    "ds_read_b64_tr_b16 a[136:137], v20 offset:49152\n"
    "ds_read_b64_tr_b16 a[142:143], v20 offset:49664\n"
    "ds_read_b128 a[184:187], v10 offset:256\n"
    "ds_read_b128 a[188:191], v10 offset:384\n"
    "ds_read_b64_tr_b16 v[200:201], v18 offset:20736\n"
    "ds_read_b64_tr_b16 v[202:203], v18 offset:0\n"
    "ds_read_b64 v[216:217], v62 offset:20736\n"
    "s_waitcnt lgkmcnt(10)\n"
    "v_mfma_f32_16x16x32_bf16 v[64:67], a[0:3], a[176:179], 0\n"
    "s_waitcnt lgkmcnt(9)\n"
    "ds_read_b64 v[218:219], v62\n"
    "ds_read_b128 a[176:179], v10 offset:512\n"
    "v_mfma_f32_16x16x32_bf16 v[68:71], a[0:3], a[180:183], 0\n"
    "s_waitcnt lgkmcnt(8)\n"
    "ds_read_b64_tr_b16 v[204:205], v18 offset:20864\n"
    "ds_read_b64_tr_b16 v[206:207], v18 offset:128\n"
    "v_mfma_f32_16x16x32_bf16 v[176:179], v[36:39], a[136:139], 0\n"
    "s_waitcnt lgkmcnt(9)\n"
    "ds_read_b128 a[192:195], v10 offset:640\n"
    "ds_read_b64_tr_b16 v[208:209], v18 offset:20992\n"
    "v_mfma_f32_16x16x32_bf16 v[180:183], v[36:39], a[140:143], 0\n"
    "s_waitcnt lgkmcnt(10)\n"
    "ds_read_b64_tr_b16 v[210:211], v18 offset:256\n"
    "ds_read_b128 a[196:199], v10 offset:768\n"
    "v_mfma_f32_16x16x32_bf16 v[72:75], a[0:3], a[184:187], 0\n"
    "s_waitcnt lgkmcnt(11)\n"
    "ds_read_b64_tr_b16 v[212:213], v18 offset:21120\n"
    "ds_read_b64_tr_b16 v[214:215], v18 offset:384\n"
    "v_mfma_f32_16x16x32_bf16 v[76:79], a[0:3], a[188:191], 0\n"
    "s_waitcnt lgkmcnt(11)\n"
    "ds_read_b128 a[200:203], v10 offset:896\n"
    "ds_read_b64_tr_b16 a[146:147], v20 offset:53248\n"
    "v_mfma_f32_16x16x32_bf16 v[128:131], v[200:203], v[196:199], 0\n"
    "s_waitcnt lgkmcnt(10)\n"
    "ds_read_b64_tr_b16 a[148:149], v20 offset:53760\n"
    "ds_read_b64_tr_b16 a[144:145], v20 offset:57344\n"
    "v_cvt_pk_bf16_f32 v52, v176, v177\n"
    "v_lshlrev_b32 v240, 16, v216\n"
    "v_and_b32 v241, s48, v216\n"
    "v_mfma_f32_16x16x32_bf16 v[80:83], a[0:3], a[176:179], 0\n"
    "s_waitcnt lgkmcnt(10)\n"
    "ds_read_b64_tr_b16 a[150:151], v20 offset:57856\n"
    "ds_read_b64_tr_b16 v[220:221], v18\n"
    "v_cvt_pk_bf16_f32 v53, v178, v179\n"
    "v_lshlrev_b32 v242, 16, v217\n"
    "v_and_b32 v243, s48, v217\n"
    "v_lshlrev_b32 v244, 16, v218\n"
    "v_mfma_f32_16x16x32_bf16 v[132:135], v[204:207], v[196:199], 0\n"
    "s_waitcnt lgkmcnt(11)\n"
    "ds_read_b64_tr_b16 v[222:223], v18 offset:128\n"
    "ds_read_b64 v[230:231], v16\n"
    "v_cvt_pk_bf16_f32 v54, v180, v181\n"
    "v_and_b32 v245, s48, v218\n"
    "v_lshlrev_b32 v246, 16, v219\n"
    "v_and_b32 v247, s48, v219\n"
    "v_mfma_f32_16x16x32_bf16 v[84:87], a[0:3], a[192:195], 0\n"
    "s_waitcnt lgkmcnt(11)\n"
    "ds_read_b64 v[232:233], v63\n"
    "ds_read_b64_tr_b16 v[224:225], v18 offset:256\n"
    "v_cvt_pk_bf16_f32 v55, v182, v183\n"
    "v_sub_f32 v240, v240, v244\n"
    "v_sub_f32 v241, v241, v245\n"
    "v_sub_f32 v242, v242, v246\n"
    "v_mfma_f32_16x16x32_bf16 v[136:139], v[208:211], v[196:199], 0\n"
    "s_waitcnt lgkmcnt(12)\n"
    "v_sub_f32 v243, v243, v247\n"
    "v_cvt_pk_bf16_f32 v240, v240, v241\n"
    "v_cvt_pk_bf16_f32 v241, v242, v243\n"
    "v_mfma_f32_16x16x32_bf16 v[88:91], a[0:3], a[196:199], 0\n"
    "s_waitcnt lgkmcnt(10)\n"
    "ds_read_b64_tr_b16 v[226:227], v18 offset:384\n"
    "ds_write_b64 v62, v[240:241] offset:20736\n"
    "v_cvt_pk_bf16_f32 v128, v128, v129\n"
    "v_cvt_pk_bf16_f32 v129, v130, v131\n"
    "v_mfma_f32_16x16x32_bf16 v[140:143], v[212:215], v[196:199], 0\n"
    "s_waitcnt lgkmcnt(11)\n"
    "ds_read_b64_tr_b16 v[228:229], v2\n"
    "ds_write_b64 v60, v[128:129] offset:20736\n"
    "v_cvt_pk_bf16_f32 v132, v132, v133\n"
    "v_mfma_f32_16x16x32_bf16 v[92:95], a[0:3], a[200:203], 0\n"
    "s_waitcnt lgkmcnt(10)\n"
    "ds_read_b128 a[204:207], v11 offset:0\n"
    "v_cvt_pk_bf16_f32 v133, v134, v135\n"
    "v_mfma_f32_16x16x32_bf16 v[184:187], v[36:39], a[144:147], 0\n"
    "s_waitcnt lgkmcnt(10)\n"
    "ds_read_b64_tr_b16 a[154:155], v20 offset:61440\n"
    "ds_write_b64 v60, v[132:133] offset:20864\n"
    "v_mfma_f32_16x16x32_bf16 v[188:191], v[36:39], a[148:151], 0\n"
    "ds_read_b64_tr_b16 a[156:157], v20 offset:61952\n"
    "v_cvt_pk_bf16_f32 v136, v136, v137\n"
    "v_mfma_f32_16x16x32_bf16 v[64:67], v[52:55], a[180:183], v[64:67]\n"
    "ds_read_b64_tr_b16 a[152:153], v21 offset:0\n"
    "v_cvt_pk_bf16_f32 v137, v138, v139\n"
    "v_mfma_f32_16x16x32_bf16 v[68:71], v[52:55], a[184:187], v[68:71]\n"
    "s_waitcnt lgkmcnt(12)\n"
    "ds_read_b64_tr_b16 a[158:159], v21 offset:512\n"
    "ds_write_b64 v60, v[136:137] offset:20992\n"
    "v_mfma_f32_16x16x32_bf16 v[144:147], v[220:223], v[196:199], 0\n"
    "v_cvt_pk_bf16_f32 v48, v184, v185\n"
    "v_cvt_pk_bf16_f32 v140, v140, v141\n"
    "v_mfma_f32_16x16x32_bf16 v[72:75], v[52:55], a[188:191], v[72:75]\n"
    "s_waitcnt lgkmcnt(11)\n"
    "v_cvt_pk_bf16_f32 v49, v186, v187\n"
    "v_cvt_pk_bf16_f32 v141, v142, v143\n"
    "v_mfma_f32_16x16x32_bf16 v[148:151], v[222:225], v[252:255], 0\n"
    "ds_write_b64 v60, v[140:141] offset:21120\n"
    "v_cvt_pk_bf16_f32 v50, v188, v189\n"
    "v_mfma_f32_16x16x32_bf16 v[76:79], v[52:55], a[176:179], v[76:79]\n"
    "s_waitcnt lgkmcnt(11)\n"
    "v_cvt_pk_bf16_f32 v51, v190, v191\n"
    "v_lshlrev_b32 v240, 16, v230\n"
    "v_and_b32 v241, s48, v230\n"
    "v_mfma_f32_16x16x32_bf16 v[152:155], v[224:227], v[196:199], 0\n"
    "v_lshlrev_b32 v242, 16, v231\n"
    "v_and_b32 v243, s48, v231\n"
    "v_lshlrev_b32 v244, 16, v232\n"
    "v_mfma_f32_16x16x32_bf16 v[80:83], v[52:55], a[192:195], v[80:83]\n"
    "s_waitcnt lgkmcnt(9)\n"
    "ds_read_b128 a[180:183], v11 offset:128\n"
    "v_and_b32 v245, s48, v232\n"
    "v_lshlrev_b32 v246, 16, v233\n"
    "v_mfma_f32_16x16x32_bf16 v[156:159], v[226:229], v[252:255], 0\n"
    "s_waitcnt vmcnt(5) lgkmcnt(1)\n"
    "s_barrier\n"
    "ds_read_b128 a[208:211], v10 offset:20864\n"
    "v_and_b32 v247, s48, v233\n"
    "v_mfma_f32_16x16x32_bf16 v[84:87], v[52:55], a[196:199], v[84:87]\n"
    "ds_read_b128 a[212:215], v10 offset:20992\n"
    "v_sub_f32 v240, v240, v244\n"
    "v_mfma_f32_16x16x32_bf16 v[88:91], v[52:55], a[200:203], v[88:91]\n"
    "ds_read_b128 a[216:219], v10 offset:21120\n"
    "v_sub_f32 v241, v241, v245\n"
    "v_mfma_f32_16x16x32_bf16 v[92:95], v[52:55], a[204:207], v[92:95]\n"
    "ds_read_b128 a[220:223], v10 offset:21248\n"
    "v_sub_f32 v242, v242, v246\n"
    "v_mfma_f32_16x16x32_bf16 v[40:43], v[36:39], a[152:155], 0\n"
    "ds_read_b128 a[224:227], v10 offset:21376\n"
    "v_sub_f32 v243, v243, v247\n"
    "v_mfma_f32_16x16x32_bf16 v[44:47], v[36:39], a[156:159], 0\n"
    "ds_read_b128 a[228:231], v10 offset:21504\n"
    "v_cvt_pk_bf16_f32 v240, v240, v241\n"
    "v_mfma_f32_16x16x32_bf16 v[64:67], v[48:51], a[184:187], v[64:67]\n"
    "ds_read_b128 a[184:187], v10 offset:21632\n"
    "v_cvt_pk_bf16_f32 v241, v242, v243\n"
    "v_mfma_f32_16x16x32_bf16 v[68:71], v[48:51], a[188:191], v[68:71]\n"
    "ds_read_b128 a[232:235], v11 offset:20736\n"
    "ds_write_b64 v62, v[240:241] offset:41472\n"
    "v_mfma_f32_16x16x32_bf16 v[72:75], v[48:51], a[176:179], v[72:75]\n"
    "ds_read_b64_tr_b16 a[162:163], v21 offset:4096\n"
    "v_cvt_pk_bf16_f32 v52, v40, v41\n"
    "v_cvt_pk_bf16_f32 v144, v144, v145\n"
    "v_mfma_f32_16x16x32_bf16 v[76:79], v[48:51], a[192:195], v[76:79]\n"
    "ds_read_b64_tr_b16 a[164:165], v21 offset:4608\n"
    "v_cvt_pk_bf16_f32 v53, v42, v43\n"
    "v_cvt_pk_bf16_f32 v145, v146, v147\n"
    "v_mfma_f32_16x16x32_bf16 v[80:83], v[48:51], a[196:199], v[80:83]\n"
    "ds_read_b64_tr_b16 a[160:161], v21 offset:8192\n"
    "ds_write_b64 v60, v[144:145] offset:41472\n"
    "v_cvt_pk_bf16_f32 v54, v44, v45\n"
    "v_mfma_f32_16x16x32_bf16 v[84:87], v[48:51], a[200:203], v[84:87]\n"
    "ds_read_b64_tr_b16 a[166:167], v21 offset:8704\n"
    "v_cvt_pk_bf16_f32 v55, v46, v47\n"
    "v_cvt_pk_bf16_f32 v148, v148, v149\n"
    "v_mfma_f32_16x16x32_bf16 v[88:91], v[48:51], a[204:207], v[88:91]\n"
    "s_waitcnt lgkmcnt(14)\n"
    "v_cvt_pk_bf16_f32 v149, v150, v151\n"
    "v_mfma_f32_16x16x32_bf16 v[92:95], v[48:51], a[180:183], v[92:95]\n"
    "s_waitcnt lgkmcnt(13)\n"
    "ds_write_b64 v60, v[148:149] offset:41600\n"
    "v_mfma_f32_16x16x32_bf16 v[96:99], a[138:141], a[208:211], 0\n"
    "s_waitcnt lgkmcnt(13)\n"
    "v_cvt_pk_bf16_f32 v152, v152, v153\n"
    "v_mfma_f32_16x16x32_bf16 v[100:103], a[138:141], a[212:215], 0\n"
    "s_waitcnt lgkmcnt(12)\n"
    "ds_read_b128 a[208:211], v11 offset:256\n"
    "v_cvt_pk_bf16_f32 v153, v154, v155\n"
    "v_mfma_f32_16x16x32_bf16 v[104:107], a[138:141], a[216:219], 0\n"
    "s_waitcnt lgkmcnt(12)\n"
    "s_add_u32 m0, s37, 8192\n"
    "ds_read_b128 a[236:239], v11 offset:20864\n"
    "ds_write_b64 v60, v[152:153] offset:41728\n"
    "v_mfma_f32_16x16x32_bf16 v[108:111], a[138:141], a[220:223], 0\n"
    "s_waitcnt lgkmcnt(13)\n"
    "s_mov_b32 s24, 4194304\n"
    "ds_read_b64_tr_b16 a[170:171], v21 offset:12288\n"
    "v_cvt_pk_bf16_f32 v156, v156, v157\n"
    "v_mfma_f32_16x16x32_bf16 v[112:115], a[138:141], a[224:227], 0\n"
    "s_waitcnt lgkmcnt(13)\n"
    "ds_read_b64_tr_b16 a[172:173], v21 offset:12800\n"
    "v_cvt_pk_bf16_f32 v157, v158, v159\n"
    "v_mfma_f32_16x16x32_bf16 v[116:119], a[138:141], a[228:231], 0\n"
    "s_waitcnt lgkmcnt(13)\n"
    "ds_read_b64_tr_b16 a[168:169], v21 offset:16384\n"
    "ds_write_b64 v60, v[156:157] offset:41856\n"
    "v_mfma_f32_16x16x32_bf16 v[120:123], a[138:141], a[184:187], 0\n"
    "s_waitcnt lgkmcnt(14)\n"
    "ds_read_b64_tr_b16 a[174:175], v21 offset:16896\n"
    "v_mfma_f32_16x16x32_bf16 v[124:127], a[138:141], a[232:235], 0\n"
    "s_waitcnt lgkmcnt(11)\n"
    "ds_read_b64_tr_b16 a[20:21], v21 offset:16384\n"
    "v_mfma_f32_16x16x32_bf16 v[176:179], v[36:39], a[160:163], 0\n"
    "s_waitcnt lgkmcnt(10)\n"
    "ds_read_b64_tr_b16 a[22:23], v21 offset:16896\n"
    "v_mfma_f32_16x16x32_bf16 v[180:183], v[36:39], a[164:167], 0\n"
    "s_waitcnt vmcnt(2) lgkmcnt(3)\n"
    "s_barrier\n"
    "ds_read_b128 a[240:243], v10 offset:41984\n"
    "v_mfma_f32_16x16x32_bf16 v[64:67], v[52:55], a[188:191], v[64:67]\n"
    "ds_read_b128 a[188:191], v10 offset:42240\n"
    "v_mfma_f32_16x16x32_bf16 v[68:71], v[52:55], a[176:179], v[68:71]\n"
    "ds_read_b128 a[176:179], v11 offset:41472\n"
    "v_mfma_f32_16x16x32_bf16 v[72:75], v[52:55], a[192:195], v[72:75]\n"
    "buffer_load_dwordx4 v3, s[12:15], s24 offen lds\n"
    "v_mfma_f32_16x16x32_bf16 v[76:79], v[52:55], a[196:199], v[76:79]\n"
    "s_add_u32 m0, s37, 12288\n"
    "ds_read_b128 a[196:199], v11 offset:41728\n"
    "v_cvt_pk_bf16_f32 v48, v176, v177\n"
    "v_mfma_f32_16x16x32_bf16 v[80:83], v[52:55], a[200:203], v[80:83]\n"
    "s_mov_b32 s24, 4456448\n"
    "ds_read_b64_tr_b16 a[4:5], v20 offset:49152\n"
    "v_cvt_pk_bf16_f32 v49, v178, v179\n"
    "v_mfma_f32_16x16x32_bf16 v[84:87], v[52:55], a[204:207], v[84:87]\n"
    "ds_read_b64_tr_b16 a[6:7], v20 offset:49664\n"
    "v_cvt_pk_bf16_f32 v50, v180, v181\n"
    "v_mfma_f32_16x16x32_bf16 v[88:91], v[52:55], a[180:183], v[88:91]\n"
    "ds_read_b64_tr_b16 a[8:9], v20 offset:57344\n"
    "v_cvt_pk_bf16_f32 v51, v182, v183\n"
    "v_mfma_f32_16x16x32_bf16 v[92:95], v[52:55], a[208:211], v[92:95]\n"
    "buffer_load_dwordx4 v3, s[12:15], s24 offen lds\n"
    "v_mfma_f32_16x16x32_bf16 v[96:99], a[146:149], a[212:215], v[96:99]\n"
    "s_add_u32 m0, s37, 16384\n"
    "ds_read_b64_tr_b16 a[10:11], v20 offset:57856\n"
    "v_mfma_f32_16x16x32_bf16 v[100:103], a[146:149], a[216:219], v[100:103]\n"
    "s_mov_b32 s24, 4718592\n"
    "ds_read_b128 a[204:207], v11 offset:20992\n"
    "v_mfma_f32_16x16x32_bf16 v[104:107], a[146:149], a[220:223], v[104:107]\n"
    "ds_read_b64_tr_b16 a[130:131], v21 offset:20480\n"
    "v_mfma_f32_16x16x32_bf16 v[108:111], a[146:149], a[224:227], v[108:111]\n"
    "ds_read_b64_tr_b16 a[132:133], v21 offset:20992\n"
    "v_mfma_f32_16x16x32_bf16 v[112:115], a[146:149], a[228:231], v[112:115]\n"
    "buffer_load_dwordx4 v3, s[12:15], s24 offen lds\n"
    "v_mfma_f32_16x16x32_bf16 v[116:119], a[146:149], a[184:187], v[116:119]\n"
    "s_add_u32 m0, s37, 20480\n"
    "ds_read_b64_tr_b16 a[128:129], v21 offset:24576\n"
    "v_mfma_f32_16x16x32_bf16 v[120:123], a[146:149], a[232:235], v[120:123]\n"
    "s_mov_b32 s24, 4980736\n"
    "s_waitcnt lgkmcnt(10)\n"
    "ds_read_b64_tr_b16 a[134:135], v21 offset:25088\n"
    "v_mfma_f32_16x16x32_bf16 v[124:127], a[146:149], a[236:239], v[124:127]\n"
    "ds_read_b64_tr_b16 a[24:25], v21 offset:24576\n"
    "v_mfma_f32_16x16x32_bf16 v[184:187], v[36:39], a[168:171], 0\n"
    "ds_read_b64_tr_b16 a[26:27], v21 offset:25088\n"
    "v_mfma_f32_16x16x32_bf16 v[188:191], v[36:39], a[172:175], 0\n"
    "buffer_load_dwordx4 v3, s[12:15], s24 offen lds\n"
    "v_mfma_f32_16x16x32_bf16 v[64:67], v[48:51], a[240:243], v[64:67]\n"
    "s_add_u32 m0, s37, 24576\n"
    "ds_read_b128 a[208:211], v10 offset:42112\n"
    "v_mfma_f32_16x16x32_bf16 v[72:75], v[48:51], a[188:191], v[72:75]\n"
    "s_waitcnt lgkmcnt(13)\n"
    "s_mov_b32 s24, 5242880\n"
    "ds_read_b128 a[212:215], v10 offset:42368\n"
    "v_mfma_f32_16x16x32_bf16 v[80:83], v[48:51], a[176:179], v[80:83]\n"
    "s_waitcnt lgkmcnt(13)\n"
    "ds_read_b128 a[240:243], v11 offset:41600\n"
    "v_cvt_pk_bf16_f32 v52, v184, v185\n"
    "v_mfma_f32_16x16x32_bf16 v[88:91], v[48:51], a[196:199], v[88:91]\n"
    "ds_read_b128 a[244:247], v11 offset:41856\n"
    "v_cvt_pk_bf16_f32 v53, v186, v187\n"
    "v_mfma_f32_16x16x32_bf16 v[96:99], a[154:157], a[216:219], v[96:99]\n"
    "buffer_load_dwordx4 v3, s[12:15], s24 offen lds\n"
    "v_mfma_f32_16x16x32_bf16 v[100:103], a[154:157], a[220:223], v[100:103]\n"
    "s_add_u32 m0, s37, 28672\n"
    "s_waitcnt lgkmcnt(10)\n"
    "ds_read_b64_tr_b16 a[12:13], v21 offset:0\n"
    "v_cvt_pk_bf16_f32 v54, v188, v189\n"
    "v_mfma_f32_16x16x32_bf16 v[104:107], a[154:157], a[224:227], v[104:107]\n"
    "s_mov_b32 s24, 5505024\n"
    "ds_read_b64_tr_b16 a[14:15], v21 offset:512\n"
    "v_cvt_pk_bf16_f32 v55, v190, v191\n"
    "v_mfma_f32_16x16x32_bf16 v[108:111], a[154:157], a[228:231], v[108:111]\n"
    "ds_read_b64_tr_b16 a[16:17], v21 offset:8192\n"
    "v_add_f32 v176, v176, v184\n"
    "v_mfma_f32_16x16x32_bf16 v[112:115], a[154:157], a[184:187], v[112:115]\n"
    "ds_read_b64_tr_b16 a[18:19], v21 offset:8704\n"
    "v_add_f32 v177, v177, v185\n"
    "v_mfma_f32_16x16x32_bf16 v[116:119], a[154:157], a[232:235], v[116:119]\n"
    "buffer_load_dwordx4 v3, s[12:15], s24 offen lds\n"
    "v_mfma_f32_16x16x32_bf16 v[120:123], a[154:157], a[236:239], v[120:123]\n"
    "s_add_u32 m0, s37, 32768\n"
    "ds_read_b128 a[216:219], v11 offset:21120\n"
    "v_add_f32 v178, v178, v186\n"
    "v_mfma_f32_16x16x32_bf16 v[124:127], a[154:157], a[204:207], v[124:127]\n"
    "s_waitcnt lgkmcnt(12)\n"
    "s_mov_b32 s24, 5767168\n"
    "ds_read_b128 a[248:251], v11 offset:384\n"
    "v_mfma_f32_16x16x32_bf16 v[40:43], v[36:39], a[128:131], 0\n"
    "s_waitcnt lgkmcnt(12)\n"
    "ds_read_b64_tr_b16 a[138:139], v21 offset:28672\n"
    "v_add_f32 v179, v179, v187\n"
    "v_mfma_f32_16x16x32_bf16 v[44:47], v[36:39], a[132:135], 0\n"
    "s_waitcnt lgkmcnt(10)\n"
    "ds_read_b64_tr_b16 a[140:141], v21 offset:29184\n"
    "v_add_f32 v180, v180, v188\n"
    "v_mfma_f32_16x16x32_bf16 v[68:71], v[52:55], a[208:211], v[68:71]\n"
    "s_waitcnt lgkmcnt(10)\n"
    "buffer_load_dwordx4 v3, s[12:15], s24 offen lds\n"
    "v_mfma_f32_16x16x32_bf16 v[76:79], v[52:55], a[212:215], v[76:79]\n"
    "s_waitcnt lgkmcnt(9)\n"
    "s_add_u32 m0, s37, 36864\n"
    "ds_read_b64_tr_b16 a[136:137], v20 offset:0\n"
    "v_mfma_f32_16x16x32_bf16 v[84:87], v[52:55], a[240:243], v[84:87]\n"
    "s_waitcnt lgkmcnt(9)\n"
    "s_mov_b32 s24, 6029312\n"
    "ds_read_b64_tr_b16 a[142:143], v20 offset:512\n"
    "v_mfma_f32_16x16x32_bf16 v[92:95], v[52:55], a[244:247], v[92:95]\n"
    "ds_read_b64_tr_b16 a[28:29], v20 offset:0\n"
    "v_add_f32 v181, v181, v189\n"
    "v_mfma_f32_16x16x32_bf16 v[96:99], a[162:165], a[220:223], v[96:99]\n"
    "ds_read_b64_tr_b16 a[30:31], v20 offset:512\n"
    "v_add_f32 v182, v182, v190\n"
    "v_mfma_f32_16x16x32_bf16 v[100:103], a[162:165], a[224:227], v[100:103]\n"
    "buffer_load_dwordx4 v3, s[12:15], s24 offen lds\n"
    "v_mfma_f32_16x16x32_bf16 v[104:107], a[162:165], a[228:231], v[104:107]\n"
    "s_add_u32 m0, s37, 40960\n"
    "v_add_f32 v183, v183, v191\n"
    "v_cvt_pk_bf16_f32 v192, v176, v177\n"
    "v_mfma_f32_16x16x32_bf16 v[108:111], a[162:165], a[184:187], v[108:111]\n"
    "s_mov_b32 s24, 6291456\n"
    "v_cvt_pk_bf16_f32 v193, v178, v179\n"
    "v_cvt_pk_bf16_f32 v194, v180, v181\n"
    "v_mfma_f32_16x16x32_bf16 v[112:115], a[162:165], a[232:235], v[112:115]\n"
    "v_cvt_pk_bf16_f32 v195, v182, v183\n"
    "v_cvt_pk_bf16_f32 v48, v40, v41\n"
    "v_mfma_f32_16x16x32_bf16 v[116:119], a[162:165], a[236:239], v[116:119]\n"
    "ds_read_b128 a[208:211], v11 offset:41984\n"
    "v_cvt_pk_bf16_f32 v49, v42, v43\n"
    "v_mfma_f32_16x16x32_bf16 v[120:123], a[162:165], a[204:207], v[120:123]\n"
    "s_waitcnt lgkmcnt(8)\n"
    "v_cvt_pk_bf16_f32 v50, v44, v45\n"
    "v_cvt_pk_bf16_f32 v51, v46, v47\n"
    "v_mfma_f32_16x16x32_bf16 v[124:127], a[162:165], a[216:219], v[124:127]\n"
    "buffer_load_dwordx4 v3, s[12:15], s24 offen lds\n"
    "v_mfma_f32_16x16x32_bf16 v[160:163], v[192:195], a[192:195], 0\n"
    "s_add_u32 m0, s37, 45056\n"
    "ds_read_b128 a[192:195], v11 offset:21248\n"
    "v_mfma_f32_16x16x32_bf16 v[164:167], v[192:195], a[200:203], 0\n"
    "s_waitcnt vmcnt(5) lgkmcnt(2)\n"
    "s_barrier\n"
    "ds_read_b64_tr_b16 a[146:147], v20 offset:4096\n"
    "v_mfma_f32_16x16x32_bf16 v[168:171], v[192:195], a[180:183], 0\n"
    "s_mov_b32 s24, 6553600\n"
    "ds_read_b64_tr_b16 a[148:149], v20 offset:4608\n"
    "v_mfma_f32_16x16x32_bf16 v[172:175], v[192:195], a[248:251], 0\n"
    "buffer_load_dwordx4 v3, s[12:15], s24 offen lds\n"
    "v_mfma_f32_16x16x32_bf16 v[176:179], v[36:39], a[136:139], 0\n"
    "s_add_u32 m0, s37, 49152\n"
    "ds_read_b64_tr_b16 a[144:145], v20 offset:8192\n"
    "v_mfma_f32_16x16x32_bf16 v[180:183], v[36:39], a[140:143], 0\n"
    "s_mov_b32 s24, 6815744\n"
    "ds_read_b64_tr_b16 a[150:151], v20 offset:8704\n"
    "v_mfma_f32_16x16x32_bf16 v[64:67], v[48:51], a[188:191], v[64:67]\n"
    "ds_read_b64_tr_b16 a[32:33], v20 offset:8192\n"
    "v_mfma_f32_16x16x32_bf16 v[72:75], v[48:51], a[176:179], v[72:75]\n"
    "ds_read_b64_tr_b16 a[34:35], v20 offset:8704\n"
    "v_mfma_f32_16x16x32_bf16 v[80:83], v[48:51], a[196:199], v[80:83]\n"
    "s_waitcnt lgkmcnt(7)\n"
    "buffer_load_dwordx4 v3, s[12:15], s24 offen lds\n"
    "v_mfma_f32_16x16x32_bf16 v[88:91], v[48:51], a[208:211], v[88:91]\n"
    "s_add_u32 m0, s37, 53248\n"
    "v_cvt_pk_bf16_f32 v52, v176, v177\n"
    "v_cvt_pk_bf16_f32 v53, v178, v179\n"
    "v_mfma_f32_16x16x32_bf16 v[96:99], a[170:173], a[224:227], v[96:99]\n"
    "s_mov_b32 s24, 7077888\n"
    "v_cvt_pk_bf16_f32 v54, v180, v181\n"
    "v_cvt_pk_bf16_f32 v55, v182, v183\n"
    "v_mfma_f32_16x16x32_bf16 v[100:103], a[170:173], a[228:231], v[100:103]\n"
    "v_add_f32 v40, v40, v176\n"
    "v_add_f32 v41, v41, v177\n"
    "v_mfma_f32_16x16x32_bf16 v[104:107], a[170:173], a[184:187], v[104:107]\n"
    "ds_read_b128 a[188:191], v11 offset:42112\n"
    "v_add_f32 v42, v42, v178\n"
    "v_mfma_f32_16x16x32_bf16 v[108:111], a[170:173], a[232:235], v[108:111]\n"
    "buffer_load_dwordx4 v3, s[12:15], s24 offen lds\n"
    "v_mfma_f32_16x16x32_bf16 v[112:115], a[170:173], a[236:239], v[112:115]\n"
    "s_add_u32 m0, s37, 57344\n"
    "v_add_f32 v43, v43, v179\n"
    "v_add_f32 v44, v44, v180\n"
    "v_mfma_f32_16x16x32_bf16 v[116:119], a[170:173], a[204:207], v[116:119]\n"
    "s_mov_b32 s24, 7340032\n"
    "v_add_f32 v45, v45, v181\n"
    "v_add_f32 v46, v46, v182\n"
    "v_mfma_f32_16x16x32_bf16 v[120:123], a[170:173], a[216:219], v[120:123]\n"
    "s_waitcnt lgkmcnt(7)\n"
    "ds_read_b128 a[220:223], v11 offset:21376\n"
    "v_add_f32 v47, v47, v183\n"
    "v_mfma_f32_16x16x32_bf16 v[124:127], a[170:173], a[192:195], v[124:127]\n"
    "s_waitcnt lgkmcnt(5)\n"
    "ds_read_b128 a[224:227], v11 offset:640\n"
    "v_cvt_pk_bf16_f32 v192, v40, v41\n"
    "v_mfma_f32_16x16x32_bf16 v[184:187], v[36:39], a[144:147], 0\n"
    "s_waitcnt lgkmcnt(5)\n"
    "buffer_load_dwordx4 v3, s[12:15], s24 offen lds\n"
    "v_mfma_f32_16x16x32_bf16 v[188:191], v[36:39], a[148:151], 0\n"
    "s_add_u32 m0, s37, 61440\n"
    "ds_read_b64_tr_b16 a[154:155], v20 offset:12288\n"
    "v_cvt_pk_bf16_f32 v193, v42, v43\n"
    "v_mfma_f32_16x16x32_bf16 v[68:71], v[52:55], a[212:215], v[68:71]\n"
    "s_mov_b32 s24, 7602176\n"
    "ds_read_b64_tr_b16 a[156:157], v20 offset:12800\n"
    "v_cvt_pk_bf16_f32 v194, v44, v45\n"
    "v_mfma_f32_16x16x32_bf16 v[76:79], v[52:55], a[240:243], v[76:79]\n"
    "ds_read_b64_tr_b16 a[152:153], v20 offset:16384\n"
    "v_cvt_pk_bf16_f32 v195, v46, v47\n"
    "v_mfma_f32_16x16x32_bf16 v[84:87], v[52:55], a[244:247], v[84:87]\n"
    "s_waitcnt lgkmcnt(5)\n"
    "ds_read_b64_tr_b16 a[158:159], v20 offset:16896\n"
    "v_cvt_pk_bf16_f32 v48, v184, v185\n"
    "v_mfma_f32_16x16x32_bf16 v[92:95], v[52:55], a[188:191], v[92:95]\n"
    "buffer_load_dwordx4 v3, s[12:15], s24 offen lds\n"
    "v_mfma_f32_16x16x32_bf16 v[96:99], a[130:133], a[228:231], v[96:99]\n"
    "s_add_u32 m0, s37, 65536\n"
    "ds_read_b64_tr_b16 a[36:37], v20 offset:16384\n"
    "v_cvt_pk_bf16_f32 v49, v186, v187\n"
    "v_mfma_f32_16x16x32_bf16 v[100:103], a[130:133], a[184:187], v[100:103]\n"
    "s_mov_b32 s24, 7864320\n"
    "ds_read_b64_tr_b16 a[38:39], v20 offset:16896\n"
    "v_cvt_pk_bf16_f32 v50, v188, v189\n"
    "v_mfma_f32_16x16x32_bf16 v[104:107], a[130:133], a[232:235], v[104:107]\n"
    "v_cvt_pk_bf16_f32 v51, v190, v191\n"
    "v_mfma_f32_16x16x32_bf16 v[108:111], a[130:133], a[236:239], v[108:111]\n"
    "buffer_load_dwordx4 v3, s[12:15], s24 offen lds\n"
    "v_mfma_f32_16x16x32_bf16 v[112:115], a[130:133], a[204:207], v[112:115]\n"
    "s_add_u32 m0, s37, 69632\n"
    "v_mfma_f32_16x16x32_bf16 v[116:119], a[130:133], a[216:219], v[116:119]\n"
    "s_mov_b32 s24, 32768\n"
    "v_mfma_f32_16x16x32_bf16 v[120:123], a[130:133], a[192:195], v[120:123]\n"
    "s_waitcnt lgkmcnt(7)\n"
    "ds_read_b128 a[212:215], v11 offset:42240\n"
    "v_mfma_f32_16x16x32_bf16 v[124:127], a[130:133], a[220:223], v[124:127]\n"
    "ds_read_b128 a[228:231], v11 offset:21504\n"
    "v_mfma_f32_16x16x32_bf16 v[160:163], v[192:195], a[200:203], v[160:163]\n"
    "ds_read_b64_tr_b16 a[162:163], v20 offset:20480\n"
    "v_mfma_f32_16x16x32_bf16 v[164:167], v[192:195], a[180:183], v[164:167]\n"
    "buffer_load_dwordx4 v3, s[12:15], s24 offen lds\n"
    "v_mfma_f32_16x16x32_bf16 v[168:171], v[192:195], a[248:251], v[168:171]\n"
    "s_waitcnt lgkmcnt(9)\n"
    "s_add_u32 m0, s37, 73728\n"
    "ds_read_b64_tr_b16 a[164:165], v20 offset:20992\n"
    "v_mfma_f32_16x16x32_bf16 v[172:175], v[192:195], a[224:227], v[172:175]\n"
    "s_waitcnt vmcnt(6) lgkmcnt(4)\n"
    "s_barrier\n"
    "ds_read_b64_tr_b16 a[160:161], v20 offset:24576\n"
    "v_mfma_f32_16x16x32_bf16 v[40:43], v[36:39], a[152:155], 0\n"
    "s_mov_b32 s24, 294912\n"
    "ds_read_b64_tr_b16 a[166:167], v20 offset:25088\n"
    "v_mfma_f32_16x16x32_bf16 v[44:47], v[36:39], a[156:159], 0\n"
    "ds_read_b64_tr_b16 a[40:41], v20 offset:24576\n"
    "v_mfma_f32_16x16x32_bf16 v[64:67], v[48:51], a[176:179], v[64:67]\n"
    "buffer_load_dwordx4 v3, s[12:15], s24 offen lds\n"
    "v_mfma_f32_16x16x32_bf16 v[72:75], v[48:51], a[196:199], v[72:75]\n"
    "s_add_u32 m0, s37, 77824\n"
    "ds_read_b64_tr_b16 a[42:43], v20 offset:25088\n"
    "v_mfma_f32_16x16x32_bf16 v[80:83], v[48:51], a[208:211], v[80:83]\n"
    "s_waitcnt lgkmcnt(7)\n"
    "s_mov_b32 s24, 557056\n"
    "v_cvt_pk_bf16_f32 v52, v40, v41\n"
    "v_mfma_f32_16x16x32_bf16 v[88:91], v[48:51], a[212:215], v[88:91]\n"
    "v_cvt_pk_bf16_f32 v53, v42, v43\n"
    "v_cvt_pk_bf16_f32 v54, v44, v45\n"
    "v_mfma_f32_16x16x32_bf16 v[96:99], a[138:141], a[184:187], v[96:99]\n"
    "v_cvt_pk_bf16_f32 v55, v46, v47\n"
    "v_add_f32 v184, v184, v40\n"
    "v_mfma_f32_16x16x32_bf16 v[100:103], a[138:141], a[232:235], v[100:103]\n"
    "buffer_load_dwordx4 v3, s[12:15], s24 offen lds\n"
    "v_mfma_f32_16x16x32_bf16 v[104:107], a[138:141], a[236:239], v[104:107]\n"
    "s_add_u32 m0, s37, 81920\n"
    "ds_read_b128 a[176:179], v11 offset:42368\n"
    "v_add_f32 v185, v185, v41\n"
    "v_mfma_f32_16x16x32_bf16 v[108:111], a[138:141], a[204:207], v[108:111]\n"
    "s_mov_b32 s24, 819200\n"
    "v_add_f32 v186, v186, v42\n"
    "v_add_f32 v187, v187, v43\n"
    "v_mfma_f32_16x16x32_bf16 v[112:115], a[138:141], a[216:219], v[112:115]\n"
    "ds_read_b128 a[184:187], v11 offset:21632\n"
    "v_add_f32 v188, v188, v44\n"
    "v_mfma_f32_16x16x32_bf16 v[116:119], a[138:141], a[192:195], v[116:119]\n"
    "ds_read_b128 a[200:203], v11 offset:896\n"
    "v_add_f32 v189, v189, v45\n"
    "v_mfma_f32_16x16x32_bf16 v[120:123], a[138:141], a[220:223], v[120:123]\n"
    "s_waitcnt lgkmcnt(9)\n"
    "buffer_load_dwordx4 v3, s[12:15], s24 offen lds\n"
    "v_mfma_f32_16x16x32_bf16 v[124:127], a[138:141], a[228:231], v[124:127]\n"
    "s_waitcnt lgkmcnt(6)\n"
    "s_add_u32 m0, s37, 86016\n"
    "ds_read_b64_tr_b16 a[170:171], v20 offset:28672\n"
    "v_mfma_f32_16x16x32_bf16 v[176:179], v[36:39], a[160:163], 0\n"
    "s_waitcnt lgkmcnt(6)\n"
    "s_mov_b32 s24, 1081344\n"
    "ds_read_b64_tr_b16 a[172:173], v20 offset:29184\n"
    "v_mfma_f32_16x16x32_bf16 v[180:183], v[36:39], a[164:167], 0\n"
    "ds_read_b64_tr_b16 a[168:169], v20 offset:32768\n"
    "v_add_f32 v190, v190, v46\n"
    "v_mfma_f32_16x16x32_bf16 v[68:71], v[52:55], a[240:243], v[68:71]\n"
    "ds_read_b64_tr_b16 a[174:175], v20 offset:33280\n"
    "v_add_f32 v191, v191, v47\n"
    "v_mfma_f32_16x16x32_bf16 v[76:79], v[52:55], a[244:247], v[76:79]\n"
    "buffer_load_dwordx4 v3, s[12:15], s24 offen lds\n"
    "v_mfma_f32_16x16x32_bf16 v[84:87], v[52:55], a[188:191], v[84:87]\n"
    "s_waitcnt lgkmcnt(6)\n"
    "s_add_u32 m0, s37, 90112\n"
    "ds_read_b64_tr_b16 a[44:45], v20 offset:32768\n"
    "v_mfma_f32_16x16x32_bf16 v[92:95], v[52:55], a[176:179], v[92:95]\n"
    "s_mov_b32 s24, 1343488\n"
    "ds_read_b64_tr_b16 a[46:47], v20 offset:33280\n"
    "v_cvt_pk_bf16_f32 v192, v184, v185\n"
    "v_mfma_f32_16x16x32_bf16 v[96:99], a[146:149], a[232:235], v[96:99]\n"
    "ds_read_b64_tr_b16 v[200:201], v19 offset:20736\n"
    "v_cvt_pk_bf16_f32 v193, v186, v187\n"
    "v_mfma_f32_16x16x32_bf16 v[100:103], a[146:149], a[236:239], v[100:103]\n"
    "ds_read_b64_tr_b16 v[202:203], v19 offset:0\n"
    "v_cvt_pk_bf16_f32 v194, v188, v189\n"
    "v_mfma_f32_16x16x32_bf16 v[104:107], a[146:149], a[204:207], v[104:107]\n"
    "buffer_load_dwordx4 v3, s[12:15], s24 offen lds\n"
    "v_mfma_f32_16x16x32_bf16 v[108:111], a[146:149], a[216:219], v[108:111]\n"
    "s_add_u32 m0, s37, 94208\n"
    "ds_read_b64_tr_b16 v[204:205], v19 offset:20864\n"
    "v_cvt_pk_bf16_f32 v195, v190, v191\n"
    "v_mfma_f32_16x16x32_bf16 v[112:115], a[146:149], a[192:195], v[112:115]\n"
    "s_mov_b32 s24, 1605632\n"
    "ds_read_b64_tr_b16 v[206:207], v19 offset:128\n"
    "v_cvt_pk_bf16_f32 v48, v176, v177\n"
    "v_mfma_f32_16x16x32_bf16 v[116:119], a[146:149], a[220:223], v[116:119]\n"
    "ds_read_b128 a[232:235], v12 offset:41472\n"
    "v_cvt_pk_bf16_f32 v49, v178, v179\n"
    "v_mfma_f32_16x16x32_bf16 v[120:123], a[146:149], a[228:231], v[120:123]\n"
    "s_waitcnt lgkmcnt(12)\n"
    "ds_read_b64_tr_b16 v[208:209], v19 offset:20992\n"
    "v_cvt_pk_bf16_f32 v50, v180, v181\n"
    "v_mfma_f32_16x16x32_bf16 v[124:127], a[146:149], a[184:187], v[124:127]\n"
    "v_cvt_pk_bf16_f32 v51, v182, v183\n"
    "v_mfma_f32_16x16x32_bf16 v[160:163], v[192:195], a[180:183], v[160:163]\n"
    "ds_read_b64_tr_b16 v[210:211], v19 offset:256\n"
    "v_mfma_f32_16x16x32_bf16 v[164:167], v[192:195], a[248:251], v[164:167]\n"
    "ds_read_b64_tr_b16 v[212:213], v19 offset:21120\n"
    "v_mfma_f32_16x16x32_bf16 v[168:171], v[192:195], a[224:227], v[168:171]\n"
    "s_waitcnt lgkmcnt(14)\n"
    "ds_read_b64_tr_b16 v[214:215], v19 offset:384\n"
    "v_mfma_f32_16x16x32_bf16 v[172:175], v[192:195], a[200:203], v[172:175]\n"
    "s_waitcnt lgkmcnt(12)\n"
    "ds_read_b128 a[180:183], v12 offset:20736\n"
    "v_mfma_f32_16x16x32_bf16 v[184:187], v[36:39], a[168:171], 0\n"
    "s_waitcnt lgkmcnt(12)\n"
    "buffer_load_dwordx4 v3, s[12:15], s24 offen lds\n"
    "v_mfma_f32_16x16x32_bf16 v[188:191], v[36:39], a[172:175], 0\n"
    "ds_read_b64_tr_b16 a[130:131], v20 offset:36864\n"
    "v_mfma_f32_16x16x32_bf16 v[64:67], v[48:51], a[196:199], v[64:67]\n"
    "ds_read_b64_tr_b16 a[132:133], v20 offset:37376\n"
    "v_mfma_f32_16x16x32_bf16 v[72:75], v[48:51], a[208:211], v[72:75]\n"
    "s_waitcnt lgkmcnt(10)\n"
    "ds_read_b64_tr_b16 a[128:129], v20 offset:40960\n"
    "v_mfma_f32_16x16x32_bf16 v[128:131], v[200:203], v[196:199], 0\n"
    "ds_read_b64_tr_b16 a[134:135], v20 offset:41472\n"
    "v_cvt_pk_bf16_f32 v52, v184, v185\n"
    "v_mfma_f32_16x16x32_bf16 v[80:83], v[48:51], a[212:215], v[80:83]\n"
    "s_waitcnt lgkmcnt(10)\n"
    "buffer_load_dword v30, v58, s[12:15], 0 offen\n"
    "v_mfma_f32_16x16x32_bf16 v[132:135], v[204:207], v[196:199], 0\n"
    "s_waitcnt lgkmcnt(9)\n"
    "ds_read_b64_tr_b16 a[48:49], v20 offset:40960\n"
    "v_cvt_pk_bf16_f32 v53, v186, v187\n"
    "v_mfma_f32_16x16x32_bf16 v[88:91], v[48:51], a[232:235], v[88:91]\n"
    "s_waitcnt lgkmcnt(8)\n"
    "ds_read_b64_tr_b16 a[50:51], v20 offset:41472\n"
    "v_cvt_pk_bf16_f32 v54, v188, v189\n"
    "v_mfma_f32_16x16x32_bf16 v[136:139], v[208:211], v[196:199], 0\n"
    "v_cvt_pk_bf16_f32 v55, v190, v191\n"
    "v_cvt_pk_bf16_f32 v128, v128, v129\n"
    "v_mfma_f32_16x16x32_bf16 v[96:99], a[154:157], a[236:239], v[96:99]\n"
    "s_waitcnt lgkmcnt(7)\n"
    "global_load_dword v31, v59, s[50:51]\n"
    "v_mfma_f32_16x16x32_bf16 v[140:143], v[212:215], v[196:199], 0\n"
    "v_cvt_pk_bf16_f32 v129, v130, v131\n"
    "v_add_f32 v176, v176, v184\n"
    "v_mfma_f32_16x16x32_bf16 v[100:103], a[154:157], a[204:207], v[100:103]\n"
    "ds_write_b64 v61, v[128:129] offset:20736\n"
    "v_cvt_pk_bf16_f32 v132, v132, v133\n"
    "v_mfma_f32_16x16x32_bf16 v[104:107], a[154:157], a[216:219], v[104:107]\n"
    "ds_read_b128 a[196:199], v12 offset:41600\n"
    "v_cvt_pk_bf16_f32 v133, v134, v135\n"
    "v_mfma_f32_16x16x32_bf16 v[108:111], a[154:157], a[192:195], v[108:111]\n"
    "ds_write_b64 v61, v[132:133] offset:20864\n"
    "v_cvt_pk_bf16_f32 v136, v136, v137\n"
    "v_mfma_f32_16x16x32_bf16 v[112:115], a[154:157], a[220:223], v[112:115]\n"
    "v_cvt_pk_bf16_f32 v137, v138, v139\n"
    "v_add_f32 v177, v177, v185\n"
    "v_mfma_f32_16x16x32_bf16 v[116:119], a[154:157], a[228:231], v[116:119]\n"
    "ds_write_b64 v61, v[136:137] offset:20992\n"
    "v_cvt_pk_bf16_f32 v140, v140, v141\n"
    "v_mfma_f32_16x16x32_bf16 v[120:123], a[154:157], a[184:187], v[120:123]\n"
    "s_waitcnt lgkmcnt(10)\n"
    "ds_read_b128 a[236:239], v12 offset:20864\n"
    "v_cvt_pk_bf16_f32 v141, v142, v143\n"
    "v_mfma_f32_16x16x32_bf16 v[124:127], a[154:157], a[180:183], v[124:127]\n"
    "s_waitcnt lgkmcnt(8)\n"
    "ds_read_b128 a[240:243], v12 offset:128\n"
    "v_add_f32 v178, v178, v186\n"
    "v_mfma_f32_16x16x32_bf16 v[40:43], v[36:39], a[128:131], 0\n"
    "s_waitcnt lgkmcnt(8)\n"
    "ds_read_b64_tr_b16 a[138:139], v20 offset:45056\n"
    "v_add_f32 v179, v179, v187\n"
    "v_mfma_f32_16x16x32_bf16 v[44:47], v[36:39], a[132:135], 0\n"
    "ds_read_b64_tr_b16 a[140:141], v20 offset:45568\n"
    "v_add_f32 v180, v180, v188\n"
    "v_mfma_f32_16x16x32_bf16 v[68:71], v[52:55], a[244:247], v[68:71]\n"
    "ds_write_b64 v61, v[140:141] offset:21120\n"
    "v_add_f32 v181, v181, v189\n"
    "v_mfma_f32_16x16x32_bf16 v[76:79], v[52:55], a[188:191], v[76:79]\n"
    "s_waitcnt vmcnt(3) lgkmcnt(9)\n"
    "s_barrier\n"
    "ds_read_b64_tr_b16 a[136:137], v20 offset:49152\n"
    "v_mfma_f32_16x16x32_bf16 v[84:87], v[52:55], a[176:179], v[84:87]\n"
    "s_waitcnt lgkmcnt(8)\n"
    "ds_read_b64_tr_b16 a[142:143], v20 offset:49664\n"
    "v_add_f32 v182, v182, v190\n"
    "v_mfma_f32_16x16x32_bf16 v[92:95], v[52:55], a[196:199], v[92:95]\n"
    "ds_read_b64_tr_b16 a[52:53], v20 offset:49152\n"
    "v_add_f32 v183, v183, v191\n"
    "v_mfma_f32_16x16x32_bf16 v[96:99], a[162:165], a[204:207], v[96:99]\n"
    "ds_read_b64_tr_b16 a[54:55], v20 offset:49664\n"
    "v_cvt_pk_bf16_f32 v192, v176, v177\n"
    "v_mfma_f32_16x16x32_bf16 v[100:103], a[162:165], a[216:219], v[100:103]\n"
    "v_cvt_pk_bf16_f32 v193, v178, v179\n"
    "v_cvt_pk_bf16_f32 v194, v180, v181\n"
    "v_mfma_f32_16x16x32_bf16 v[104:107], a[162:165], a[192:195], v[104:107]\n"
    "v_cvt_pk_bf16_f32 v195, v182, v183\n"
    "v_cvt_pk_bf16_f32 v48, v40, v41\n"
    "v_mfma_f32_16x16x32_bf16 v[108:111], a[162:165], a[220:223], v[108:111]\n"
    "v_cvt_pk_bf16_f32 v49, v42, v43\n"
    "v_cvt_pk_bf16_f32 v50, v44, v45\n"
    "v_mfma_f32_16x16x32_bf16 v[112:115], a[162:165], a[228:231], v[112:115]\n"
    "ds_read_b64_tr_b16 v[220:221], v19\n"
    "v_cvt_pk_bf16_f32 v51, v46, v47\n"
    "v_mfma_f32_16x16x32_bf16 v[116:119], a[162:165], a[184:187], v[116:119]\n"
    "ds_read_b64_tr_b16 v[222:223], v19 offset:128\n"
    "v_mfma_f32_16x16x32_bf16 v[120:123], a[162:165], a[180:183], v[120:123]\n"
    "s_waitcnt lgkmcnt(10)\n"
    "v_mfma_f32_16x16x32_bf16 v[124:127], a[162:165], a[236:239], v[124:127]\n"
    "ds_read_b64_tr_b16 v[224:225], v19 offset:256\n"
    "v_mfma_f32_16x16x32_bf16 v[160:163], v[192:195], a[248:251], v[160:163]\n"
    "ds_read_b128 a[204:207], v12 offset:41728\n"
    "v_mfma_f32_16x16x32_bf16 v[164:167], v[192:195], a[224:227], v[164:167]\n"
    "ds_read_b64_tr_b16 v[226:227], v19 offset:384\n"
    "v_mfma_f32_16x16x32_bf16 v[168:171], v[192:195], a[200:203], v[168:171]\n"
    "s_waitcnt lgkmcnt(12)\n"
    "ds_read_b64_tr_b16 v[228:229], v4\n"
    "v_mfma_f32_16x16x32_bf16 v[172:175], v[192:195], a[240:243], v[172:175]\n"
    "s_waitcnt lgkmcnt(9)\n"
    "v_mfma_f32_16x16x32_bf16 v[176:179], v[36:39], a[136:139], 0\n"
    "s_waitcnt lgkmcnt(8)\n"
    "s_add_u32 m0, s37, 0\n"
    "ds_read_b128 a[244:247], v12 offset:20992\n"
    "v_mfma_f32_16x16x32_bf16 v[180:183], v[36:39], a[140:143], 0\n"
    "s_mov_b32 s24, 1867776\n"
    "ds_read_b64_tr_b16 a[146:147], v20 offset:53248\n"
    "v_mfma_f32_16x16x32_bf16 v[64:67], v[48:51], a[208:211], v[64:67]\n"
    "ds_read_b64_tr_b16 a[148:149], v20 offset:53760\n"
    "v_mfma_f32_16x16x32_bf16 v[72:75], v[48:51], a[212:215], v[72:75]\n"
    "s_waitcnt lgkmcnt(7)\n"
    "ds_read_b64_tr_b16 a[144:145], v20 offset:57344\n"
    "v_mfma_f32_16x16x32_bf16 v[144:147], v[220:223], v[196:199], 0\n"
    "buffer_load_dwordx4 v3, s[12:15], s24 offen lds\n"
    "v_mfma_f32_16x16x32_bf16 v[80:83], v[48:51], a[232:235], v[80:83]\n"
    "s_waitcnt lgkmcnt(7)\n"
    "ds_read_b64_tr_b16 a[150:151], v20 offset:57856\n"
    "v_cvt_pk_bf16_f32 v52, v176, v177\n"
    "v_mfma_f32_16x16x32_bf16 v[148:151], v[222:225], v[252:255], 0\n"
    "s_waitcnt lgkmcnt(7)\n"
    "ds_read_b64_tr_b16 a[56:57], v20 offset:57344\n"
    "v_cvt_pk_bf16_f32 v53, v178, v179\n"
    "v_mfma_f32_16x16x32_bf16 v[88:91], v[48:51], a[204:207], v[88:91]\n"
    "s_waitcnt lgkmcnt(7)\n"
    "ds_read_b64_tr_b16 a[58:59], v20 offset:57856\n"
    "v_cvt_pk_bf16_f32 v54, v180, v181\n"
    "v_mfma_f32_16x16x32_bf16 v[152:155], v[224:227], v[196:199], 0\n"
    "s_add_u32 m0, s37, 4096\n"
    "v_cvt_pk_bf16_f32 v55, v182, v183\n"
    "v_cvt_pk_bf16_f32 v144, v144, v145\n"
    "v_mfma_f32_16x16x32_bf16 v[96:99], a[170:173], a[216:219], v[96:99]\n"
    "s_waitcnt lgkmcnt(7)\n"
    "s_mov_b32 s24, 2129920\n"
    "v_cvt_pk_bf16_f32 v145, v146, v147\n"
    "v_mfma_f32_16x16x32_bf16 v[156:159], v[226:229], v[252:255], 0\n"
    "buffer_load_dwordx4 v3, s[12:15], s24 offen lds\n"
    "v_mfma_f32_16x16x32_bf16 v[100:103], a[170:173], a[192:195], v[100:103]\n"
    "s_add_u32 m0, s37, 8192\n"
    "ds_write_b64 v61, v[144:145] offset:41472\n"
    "v_cvt_pk_bf16_f32 v148, v148, v149\n"
    "v_mfma_f32_16x16x32_bf16 v[104:107], a[170:173], a[220:223], v[104:107]\n"
    "s_mov_b32 s24, 2392064\n"
    "ds_read_b128 a[208:211], v12 offset:41856\n"
    "v_cvt_pk_bf16_f32 v149, v150, v151\n"
    "v_mfma_f32_16x16x32_bf16 v[108:111], a[170:173], a[228:231], v[108:111]\n"
    "ds_write_b64 v61, v[148:149] offset:41600\n"
    "v_cvt_pk_bf16_f32 v152, v152, v153\n"
    "v_mfma_f32_16x16x32_bf16 v[112:115], a[170:173], a[184:187], v[112:115]\n"
    "buffer_load_dwordx4 v3, s[12:15], s24 offen lds\n"
    "v_mfma_f32_16x16x32_bf16 v[116:119], a[170:173], a[180:183], v[116:119]\n"
    "s_add_u32 m0, s37, 12288\n"
    "v_cvt_pk_bf16_f32 v153, v154, v155\n"
    "v_add_f32 v40, v40, v176\n"
    "v_mfma_f32_16x16x32_bf16 v[120:123], a[170:173], a[236:239], v[120:123]\n"
    "s_waitcnt lgkmcnt(9)\n"
    "s_mov_b32 s24, 2654208\n"
    "ds_read_b128 a[216:219], v12 offset:21120\n"
    "v_mfma_f32_16x16x32_bf16 v[124:127], a[170:173], a[244:247], v[124:127]\n"
    "s_waitcnt lgkmcnt(7)\n"
    "ds_read_b128 a[248:251], v12 offset:384\n"
    "v_add_f32 v41, v41, v177\n"
    "v_mfma_f32_16x16x32_bf16 v[184:187], v[36:39], a[144:147], 0\n"
    "s_waitcnt lgkmcnt(7)\n"
    "ds_read_b64_tr_b16 a[154:155], v20 offset:61440\n"
    "v_add_f32 v42, v42, v178\n"
    "v_mfma_f32_16x16x32_bf16 v[188:191], v[36:39], a[148:151], 0\n"
    "buffer_load_dwordx4 v3, s[12:15], s24 offen lds\n"
    "v_mfma_f32_16x16x32_bf16 v[68:71], v[52:55], a[188:191], v[68:71]\n"
    "s_add_u32 m0, s37, 16384\n"
    "ds_read_b64_tr_b16 a[156:157], v20 offset:61952\n"
    "v_add_f32 v43, v43, v179\n"
    "v_mfma_f32_16x16x32_bf16 v[76:79], v[52:55], a[176:179], v[76:79]\n"
    "s_mov_b32 s24, 2916352\n"
    "ds_read_b64_tr_b16 a[152:153], v21 offset:0\n"
    "v_add_f32 v44, v44, v180\n"
    "v_mfma_f32_16x16x32_bf16 v[84:87], v[52:55], a[196:199], v[84:87]\n"
    "s_waitcnt lgkmcnt(6)\n"
    "ds_read_b64_tr_b16 a[158:159], v21 offset:512\n"
    "v_add_f32 v45, v45, v181\n"
    "v_mfma_f32_16x16x32_bf16 v[92:95], v[52:55], a[208:211], v[92:95]\n"
    "ds_read_b64_tr_b16 a[60:61], v21 offset:0\n"
    "v_add_f32 v46, v46, v182\n"
    "v_mfma_f32_16x16x32_bf16 v[96:99], a[130:133], a[192:195], v[96:99]\n"
    "buffer_load_dwordx4 v3, s[12:15], s24 offen lds\n"
    "v_mfma_f32_16x16x32_bf16 v[100:103], a[130:133], a[220:223], v[100:103]\n"
    "s_add_u32 m0, s37, 20480\n"
    "ds_read_b64_tr_b16 a[62:63], v21 offset:512\n"
    "v_add_f32 v47, v47, v183\n"
    "v_mfma_f32_16x16x32_bf16 v[104:107], a[130:133], a[228:231], v[104:107]\n"
    "s_mov_b32 s24, 3178496\n"
    "ds_write_b64 v61, v[152:153] offset:41728\n"
    "v_cvt_pk_bf16_f32 v156, v156, v157\n"
    "v_mfma_f32_16x16x32_bf16 v[108:111], a[130:133], a[184:187], v[108:111]\n"
    "v_cvt_pk_bf16_f32 v157, v158, v159\n"
    "v_cvt_pk_bf16_f32 v192, v40, v41\n"
    "v_mfma_f32_16x16x32_bf16 v[112:115], a[130:133], a[180:183], v[112:115]\n"
    "buffer_load_dwordx4 v3, s[12:15], s24 offen lds\n"
    "v_mfma_f32_16x16x32_bf16 v[116:119], a[130:133], a[236:239], v[116:119]\n"
    "s_add_u32 m0, s37, 24576\n"
    "ds_write_b64 v61, v[156:157] offset:41856\n"
    "v_cvt_pk_bf16_f32 v193, v42, v43\n"
    "v_mfma_f32_16x16x32_bf16 v[120:123], a[130:133], a[244:247], v[120:123]\n"
    "s_waitcnt lgkmcnt(9)\n"
    "s_mov_b32 s24, 3440640\n"
    "ds_read_b128 a[188:191], v12 offset:41984\n"
    "v_mfma_f32_16x16x32_bf16 v[124:127], a[130:133], a[216:219], v[124:127]\n"
    "ds_read_b128 a[192:195], v12 offset:21248\n"
    "v_cvt_pk_bf16_f32 v194, v44, v45\n"
    "v_cvt_pk_bf16_f32 v195, v46, v47\n"
    "s_nop 1\n"
    "v_mfma_f32_16x16x32_bf16 v[160:163], v[192:195], a[224:227], v[160:163]\n"
    "ds_read_b128 a[224:227], v12 offset:42112\n"
    "v_cvt_pk_bf16_f32 v48, v184, v185\n"
    "v_mfma_f32_16x16x32_bf16 v[164:167], v[192:195], a[200:203], v[164:167]\n"
    "ds_read_b128 a[252:255], v12 offset:21376\n"
    "v_cvt_pk_bf16_f32 v49, v186, v187\n"
    "v_mfma_f32_16x16x32_bf16 v[168:171], v[192:195], a[240:243], v[168:171]\n"
    "s_waitcnt lgkmcnt(12)\n"
    "v_cvt_pk_bf16_f32 v50, v188, v189\n"
    "v_cvt_pk_bf16_f32 v51, v190, v191\n"
    "v_mfma_f32_16x16x32_bf16 v[172:175], v[192:195], a[248:251], v[172:175]\n"
    "s_waitcnt lgkmcnt(9)\n"
    "ds_read_b128 a[124:127], v12 offset:640\n"
    "v_mfma_f32_16x16x32_bf16 v[40:43], v[36:39], a[152:155], 0\n"
    "s_waitcnt lgkmcnt(9)\n"
    "ds_read_b64_tr_b16 a[170:171], v21 offset:8192\n"
    "v_mfma_f32_16x16x32_bf16 v[44:47], v[36:39], a[156:159], 0\n"
    "ds_read_b64_tr_b16 a[172:173], v21 offset:8704\n"
    "v_mfma_f32_16x16x32_bf16 v[64:67], v[48:51], a[212:215], v[64:67]\n"
    "ds_read_b64_tr_b16 a[168:169], v21 offset:12288\n"
    "v_mfma_f32_16x16x32_bf16 v[72:75], v[48:51], a[232:235], v[72:75]\n"
    "buffer_load_dwordx4 v3, s[12:15], s24 offen lds\n"
    "v_mfma_f32_16x16x32_bf16 v[80:83], v[48:51], a[204:207], v[80:83]\n"
    "s_waitcnt lgkmcnt(7)\n"
    "ds_read_b64_tr_b16 a[174:175], v21 offset:12800\n"
    "v_cvt_pk_bf16_f32 v52, v40, v41\n"
    "v_mfma_f32_16x16x32_bf16 v[88:91], v[48:51], a[188:191], v[88:91]\n"
    "s_add_u32 m0, s37, 28672\n"
    "ds_read_b64_tr_b16 a[68:69], v21 offset:12288\n"
    "v_cvt_pk_bf16_f32 v53, v42, v43\n"
    "v_mfma_f32_16x16x32_bf16 v[96:99], a[138:141], a[220:223], v[96:99]\n"
    "s_mov_b32 s24, 3702784\n"
    "ds_read_b64_tr_b16 a[70:71], v21 offset:12800\n"
    "v_cvt_pk_bf16_f32 v54, v44, v45\n"
    "v_mfma_f32_16x16x32_bf16 v[100:103], a[138:141], a[228:231], v[100:103]\n"
    "ds_read_b64_tr_b16 a[64:65], v21 offset:4096\n"
    "v_cvt_pk_bf16_f32 v55, v46, v47\n"
    "v_mfma_f32_16x16x32_bf16 v[104:107], a[138:141], a[184:187], v[104:107]\n"
    "buffer_load_dwordx4 v3, s[12:15], s24 offen lds\n"
    "v_mfma_f32_16x16x32_bf16 v[108:111], a[138:141], a[180:183], v[108:111]\n"
    "s_add_u32 m0, s37, 32768\n"
    "ds_read_b64_tr_b16 a[66:67], v21 offset:4608\n"
    "v_add_f32 v184, v184, v40\n"
    "v_mfma_f32_16x16x32_bf16 v[112:115], a[138:141], a[236:239], v[112:115]\n"
    "s_waitcnt vmcnt(5) lgkmcnt(6)\n"
    "s_barrier\n"
    "ds_read_b128 a[188:191], v13 offset:41472\n"
    "v_mfma_f32_16x16x32_bf16 v[116:119], a[138:141], a[244:247], v[116:119]\n"
    "s_mov_b32 s24, 3964928\n"
    "ds_read_b128 a[204:207], v13 offset:41728\n"
    "v_add_f32 v185, v185, v41\n"
    "v_mfma_f32_16x16x32_bf16 v[120:123], a[138:141], a[216:219], v[120:123]\n"
    "ds_read_b128 a[212:215], v13 offset:41984\n"
    "v_add_f32 v186, v186, v42\n"
    "v_mfma_f32_16x16x32_bf16 v[124:127], a[138:141], a[192:195], v[124:127]\n"
    "buffer_load_dwordx4 v3, s[12:15], s24 offen lds\n"
    "v_mfma_f32_16x16x32_bf16 v[68:71], v[52:55], a[176:179], v[68:71]\n"
    "s_add_u32 m0, s37, 36864\n"
    "ds_read_b128 a[176:179], v13 offset:42240\n"
    "v_add_f32 v187, v187, v43\n"
    "v_mfma_f32_16x16x32_bf16 v[76:79], v[52:55], a[196:199], v[76:79]\n"
    "s_mov_b32 s24, 4227072\n"
    "ds_read_b128 a[196:199], v12 offset:21504\n"
    "v_add_f32 v188, v188, v44\n"
    "v_mfma_f32_16x16x32_bf16 v[84:87], v[52:55], a[208:211], v[84:87]\n"
    "ds_read_b64_tr_b16 a[130:131], v21 offset:16384\n"
    "v_add_f32 v189, v189, v45\n"
    "v_mfma_f32_16x16x32_bf16 v[92:95], v[52:55], a[224:227], v[92:95]\n"
    "ds_read_b64_tr_b16 a[132:133], v21 offset:16896\n"
    "v_add_f32 v190, v190, v46\n"
    "v_mfma_f32_16x16x32_bf16 v[96:99], a[146:149], a[228:231], v[96:99]\n"
    "buffer_load_dwordx4 v3, s[12:15], s24 offen lds\n"
    "v_mfma_f32_16x16x32_bf16 v[100:103], a[146:149], a[184:187], v[100:103]\n"
    "s_add_u32 m0, s37, 40960\n"
    "ds_read_b64_tr_b16 a[128:129], v21 offset:20480\n"
    "v_add_f32 v191, v191, v47\n"
    "v_mfma_f32_16x16x32_bf16 v[104:107], a[146:149], a[180:183], v[104:107]\n"
    "s_mov_b32 s24, 4489216\n"
    "ds_read_b64_tr_b16 a[134:135], v21 offset:20992\n"
    "v_cvt_pk_bf16_f32 v192, v184, v185\n"
    "v_mfma_f32_16x16x32_bf16 v[108:111], a[146:149], a[236:239], v[108:111]\n"
    "s_waitcnt lgkmcnt(10)\n"
    "ds_read_b64_tr_b16 a[72:73], v21 offset:20480\n"
    "v_cvt_pk_bf16_f32 v193, v186, v187\n"
    "v_mfma_f32_16x16x32_bf16 v[112:115], a[146:149], a[244:247], v[112:115]\n"
    "ds_read_b64_tr_b16 a[74:75], v21 offset:20992\n"
    "v_cvt_pk_bf16_f32 v194, v188, v189\n"
    "v_mfma_f32_16x16x32_bf16 v[116:119], a[146:149], a[216:219], v[116:119]\n"
    "buffer_load_dwordx4 v3, s[12:15], s24 offen lds\n"
    "v_mfma_f32_16x16x32_bf16 v[120:123], a[146:149], a[192:195], v[120:123]\n"
    "s_add_u32 m0, s37, 45056\n"
    "ds_read_b128 a[208:211], v13 offset:41600\n"
    "v_cvt_pk_bf16_f32 v195, v190, v191\n"
    "v_mfma_f32_16x16x32_bf16 v[124:127], a[146:149], a[252:255], v[124:127]\n"
    "s_mov_b32 s24, 4751360\n"
    "ds_read_b128 a[220:223], v13 offset:41856\n"
    "v_mfma_f32_16x16x32_bf16 v[160:163], v[192:195], a[200:203], v[160:163]\n"
    "ds_read_b128 a[200:203], v13 offset:42112\n"
    "v_mfma_f32_16x16x32_bf16 v[164:167], v[192:195], a[240:243], v[164:167]\n"
    "s_waitcnt lgkmcnt(10)\n"
    "ds_read_b128 a[224:227], v13 offset:42368\n"
    "v_mfma_f32_16x16x32_bf16 v[168:171], v[192:195], a[248:251], v[168:171]\n"
    "buffer_load_dwordx4 v3, s[12:15], s24 offen lds\n"
    "v_mfma_f32_16x16x32_bf16 v[172:175], v[192:195], a[124:127], v[172:175]\n"
    "s_add_u32 m0, s37, 49152\n"
    "ds_read_b128 a[228:231], v13 offset:128\n"
    "v_mfma_f32_16x16x32_bf16 v[184:187], v[36:39], a[168:171], 0\n"
    "s_mov_b32 s24, 5013504\n"
    "ds_read_b128 a[232:235], v13 offset:384\n"
    "v_mfma_f32_16x16x32_bf16 v[188:191], v[36:39], a[172:175], 0\n"
    "ds_read_b128 a[240:243], v13 offset:640\n"
    "v_mfma_f32_16x16x32_bf16 v[176:179], v[236:239], a[64:67], v[184:187]\n"
    "ds_read_b128 a[248:251], v13 offset:896\n"
    "v_mfma_f32_16x16x32_bf16 v[180:183], v[234:237], a[64:67], v[188:191]\n"
    "buffer_load_dwordx4 v3, s[12:15], s24 offen lds\n"
    "v_mfma_f32_16x16x32_bf16 v[64:67], a[64:67], a[188:191], v[64:67]\n"
    "s_add_u32 m0, s37, 53248\n"
    "s_waitcnt lgkmcnt(10)\n"
    "ds_read_b64_tr_b16 a[138:139], v21 offset:24576\n"
    "v_mfma_f32_16x16x32_bf16 v[72:75], a[64:67], a[204:207], v[72:75]\n"
    "s_mov_b32 s24, 5275648\n"
    "ds_read_b64_tr_b16 a[140:141], v21 offset:25088\n"
    "v_mfma_f32_16x16x32_bf16 v[80:83], a[64:67], a[212:215], v[80:83]\n"
    "ds_read_b64_tr_b16 a[136:137], v21 offset:28672\n"
    "v_cvt_pk_bf16_f32 v52, v184, v185\n"
    "v_mfma_f32_16x16x32_bf16 v[88:91], a[64:67], a[176:179], v[88:91]\n"
    "ds_read_b64_tr_b16 a[142:143], v21 offset:29184\n"
    "v_cvt_pk_bf16_f32 v53, v186, v187\n"
    "v_mfma_f32_16x16x32_bf16 v[96:99], a[154:157], a[184:187], v[96:99]\n"
    "buffer_load_dwordx4 v3, s[12:15], s24 offen lds\n"
    "v_mfma_f32_16x16x32_bf16 v[100:103], a[154:157], a[180:183], v[100:103]\n"
    "s_add_u32 m0, s37, 57344\n"
    "ds_read_b64_tr_b16 a[76:77], v21 offset:28672\n"
    "v_cvt_pk_bf16_f32 v54, v188, v189\n"
    "v_mfma_f32_16x16x32_bf16 v[104:107], a[154:157], a[236:239], v[104:107]\n"
    "s_mov_b32 s24, 5537792\n"
    "s_waitcnt lgkmcnt(10)\n"
    "ds_read_b64_tr_b16 a[78:79], v21 offset:29184\n"
    "v_cvt_pk_bf16_f32 v55, v190, v191\n"
    "v_mfma_f32_16x16x32_bf16 v[108:111], a[154:157], a[244:247], v[108:111]\n"
    "ds_read_b128 a[180:183], v14 offset:41472\n"
    "v_cvt_pk_bf16_f32 v192, v176, v177\n"
    "v_mfma_f32_16x16x32_bf16 v[112:115], a[154:157], a[216:219], v[112:115]\n"
    "ds_read_b128 a[184:187], v13 offset:20864\n"
    "v_cvt_pk_bf16_f32 v193, v178, v179\n"
    "v_mfma_f32_16x16x32_bf16 v[116:119], a[154:157], a[192:195], v[116:119]\n"
    "buffer_load_dwordx4 v3, s[12:15], s24 offen lds\n"
    "v_mfma_f32_16x16x32_bf16 v[120:123], a[154:157], a[252:255], v[120:123]\n"
    "s_add_u32 m0, s37, 61440\n"
    "ds_read_b128 a[188:191], v13 offset:20992\n"
    "v_cvt_pk_bf16_f32 v194, v180, v181\n"
    "v_mfma_f32_16x16x32_bf16 v[124:127], a[154:157], a[196:199], v[124:127]\n"
    "s_mov_b32 s24, 5799936\n"
    "ds_read_b128 a[192:195], v13 offset:21120\n"
    "v_cvt_pk_bf16_f32 v195, v182, v183\n"
    "v_mfma_f32_16x16x32_bf16 v[40:43], v[36:39], a[128:131], 0\n"
    "s_waitcnt lgkmcnt(10)\n"
    "ds_read_b128 a[196:199], v13 offset:21248\n"
    "v_mfma_f32_16x16x32_bf16 v[44:47], v[36:39], a[132:135], 0\n"
    "ds_read_b128 a[216:219], v13 offset:21376\n"
    "v_mfma_f32_16x16x32_bf16 v[68:71], v[52:55], a[208:211], v[68:71]\n"
    "buffer_load_dwordx4 v3, s[12:15], s24 offen lds\n"
    "v_mfma_f32_16x16x32_bf16 v[76:79], v[52:55], a[220:223], v[76:79]\n"
    "s_add_u32 m0, s37, 65536\n"
    "ds_read_b128 a[208:211], v13 offset:21504\n"
    "v_mfma_f32_16x16x32_bf16 v[84:87], v[52:55], a[200:203], v[84:87]\n"
    "s_waitcnt vmcnt(6) lgkmcnt(7)\n"
    "s_barrier\n"
    "ds_read_b128 a[236:239], v13 offset:21632\n"
    "v_cvt_pk_bf16_f32 v48, v40, v41\n"
    "v_mfma_f32_16x16x32_bf16 v[92:95], v[52:55], a[224:227], v[92:95]\n"
    "s_mov_b32 s24, 6062080\n"
    "ds_read_b128 a[244:247], v14 offset:20736\n"
    "v_cvt_pk_bf16_f32 v49, v42, v43\n"
    "v_mfma_f32_16x16x32_bf16 v[160:163], v[192:195], a[228:231], v[160:163]\n"
    "ds_read_b64_tr_b16 a[146:147], v20 offset:0\n"
    "v_cvt_pk_bf16_f32 v50, v44, v45\n"
    "v_mfma_f32_16x16x32_bf16 v[164:167], v[192:195], a[232:235], v[164:167]\n"
    "buffer_load_dwordx4 v3, s[12:15], s24 offen lds\n"
    "v_mfma_f32_16x16x32_bf16 v[168:171], v[192:195], a[240:243], v[168:171]\n"
    "s_add_u32 m0, s37, 69632\n"
    "ds_read_b64_tr_b16 a[148:149], v20 offset:512\n"
    "v_cvt_pk_bf16_f32 v51, v46, v47\n"
    "v_mfma_f32_16x16x32_bf16 v[172:175], v[192:195], a[248:251], v[172:175]\n"
    "s_mov_b32 s24, 6324224\n"
    "ds_read_b64_tr_b16 a[144:145], v20 offset:4096\n"
    "v_mfma_f32_16x16x32_bf16 v[176:179], v[36:39], a[136:139], 0\n"
    "ds_read_b64_tr_b16 a[150:151], v20 offset:4608\n"
    "v_mfma_f32_16x16x32_bf16 v[180:183], v[36:39], a[140:143], 0\n"
    "ds_read_b64_tr_b16 a[80:81], v20 offset:4096\n"
    "v_mfma_f32_16x16x32_bf16 v[64:67], v[48:51], a[204:207], v[64:67]\n"
    "buffer_load_dwordx4 v3, s[12:15], s24 offen lds\n"
    "v_mfma_f32_16x16x32_bf16 v[72:75], v[48:51], a[212:215], v[72:75]\n"
    "s_add_u32 m0, s37, 73728\n"
    "ds_read_b64_tr_b16 a[82:83], v20 offset:4608\n"
    "v_mfma_f32_16x16x32_bf16 v[80:83], v[48:51], a[176:179], v[80:83]\n"
    "s_waitcnt lgkmcnt(14)\n"
    "s_mov_b32 s24, 6586368\n"
    "v_cvt_pk_bf16_f32 v52, v176, v177\n"
    "v_mfma_f32_16x16x32_bf16 v[88:91], v[48:51], a[180:183], v[88:91]\n"
    "s_waitcnt lgkmcnt(13)\n"
    "v_cvt_pk_bf16_f32 v53, v178, v179\n"
    "v_cvt_pk_bf16_f32 v54, v180, v181\n"
    "v_mfma_f32_16x16x32_bf16 v[96:99], a[170:173], a[184:187], v[96:99]\n"
    "s_waitcnt lgkmcnt(12)\n"
    "v_cvt_pk_bf16_f32 v55, v182, v183\n"
    "v_add_f32 v40, v40, v176\n"
    "v_mfma_f32_16x16x32_bf16 v[100:103], a[170:173], a[188:191], v[100:103]\n"
    "s_waitcnt lgkmcnt(11)\n"
    "buffer_load_dwordx4 v3, s[12:15], s24 offen lds\n"
    "v_mfma_f32_16x16x32_bf16 v[104:107], a[170:173], a[192:195], v[104:107]\n"
    "s_waitcnt lgkmcnt(10)\n"
    "s_add_u32 m0, s37, 77824\n"
    "ds_read_b128 a[184:187], v14 offset:41600\n"
    "v_mfma_f32_16x16x32_bf16 v[108:111], a[170:173], a[196:199], v[108:111]\n"
    "s_waitcnt lgkmcnt(10)\n"
    "s_mov_b32 s24, 6848512\n"
    "v_add_f32 v41, v41, v177\n"
    "v_mfma_f32_16x16x32_bf16 v[112:115], a[170:173], a[216:219], v[112:115]\n"
    "s_waitcnt lgkmcnt(9)\n"
    "v_add_f32 v42, v42, v178\n"
    "v_add_f32 v43, v43, v179\n"
    "v_mfma_f32_16x16x32_bf16 v[116:119], a[170:173], a[208:211], v[116:119]\n"
    "s_waitcnt lgkmcnt(8)\n"
    "buffer_load_dwordx4 v3, s[12:15], s24 offen lds\n"
    "v_mfma_f32_16x16x32_bf16 v[120:123], a[170:173], a[236:239], v[120:123]\n"
    "s_waitcnt lgkmcnt(7)\n"
    "s_add_u32 m0, s37, 81920\n"
    "v_add_f32 v44, v44, v180\n"
    "v_mfma_f32_16x16x32_bf16 v[124:127], a[170:173], a[244:247], v[124:127]\n"
    "s_waitcnt lgkmcnt(4)\n"
    "s_mov_b32 s24, 7110656\n"
    "ds_read_b128 a[204:207], v14 offset:20864\n"
    "v_mfma_f32_16x16x32_bf16 v[184:187], v[36:39], a[144:147], 0\n"
    "s_waitcnt lgkmcnt(4)\n"
    "ds_read_b128 a[228:231], v14 offset:128\n"
    "v_add_f32 v45, v45, v181\n"
    "v_mfma_f32_16x16x32_bf16 v[188:191], v[36:39], a[148:151], 0\n"
    "ds_read_b64_tr_b16 a[154:155], v20 offset:8192\n"
    "v_add_f32 v46, v46, v182\n"
    "v_mfma_f32_16x16x32_bf16 v[68:71], v[52:55], a[220:223], v[68:71]\n"
    "ds_read_b64_tr_b16 a[156:157], v20 offset:8704\n"
    "v_add_f32 v47, v47, v183\n"
    "v_mfma_f32_16x16x32_bf16 v[76:79], v[52:55], a[200:203], v[76:79]\n"
    "buffer_load_dwordx4 v3, s[12:15], s24 offen lds\n"
    "v_mfma_f32_16x16x32_bf16 v[84:87], v[52:55], a[224:227], v[84:87]\n"
    "s_waitcnt lgkmcnt(4)\n"
    "s_add_u32 m0, s37, 86016\n"
    "ds_read_b64_tr_b16 a[152:153], v20 offset:12288\n"
    "v_mfma_f32_16x16x32_bf16 v[92:95], v[52:55], a[184:187], v[92:95]\n"
    "s_mov_b32 s24, 7372800\n"
    "ds_read_b64_tr_b16 a[158:159], v20 offset:12800\n"
    "v_cvt_pk_bf16_f32 v192, v40, v41\n"
    "v_mfma_f32_16x16x32_bf16 v[96:99], a[130:133], a[188:191], v[96:99]\n"
    "ds_read_b64_tr_b16 a[84:85], v20 offset:12288\n"
    "v_cvt_pk_bf16_f32 v193, v42, v43\n"
    "v_mfma_f32_16x16x32_bf16 v[100:103], a[130:133], a[192:195], v[100:103]\n"
    "ds_read_b64_tr_b16 a[86:87], v20 offset:12800\n"
    "v_cvt_pk_bf16_f32 v194, v44, v45\n"
    "v_mfma_f32_16x16x32_bf16 v[104:107], a[130:133], a[196:199], v[104:107]\n"
    "buffer_load_dwordx4 v3, s[12:15], s24 offen lds\n"
    "v_mfma_f32_16x16x32_bf16 v[108:111], a[130:133], a[216:219], v[108:111]\n"
    "s_add_u32 m0, s37, 90112\n"
    "v_cvt_pk_bf16_f32 v195, v46, v47\n"
    "v_cvt_pk_bf16_f32 v48, v184, v185\n"
    "v_mfma_f32_16x16x32_bf16 v[112:115], a[130:133], a[208:211], v[112:115]\n"
    "s_mov_b32 s24, 7634944\n"
    "v_cvt_pk_bf16_f32 v49, v186, v187\n"
    "v_cvt_pk_bf16_f32 v50, v188, v189\n"
    "v_mfma_f32_16x16x32_bf16 v[116:119], a[130:133], a[236:239], v[116:119]\n"
    "v_cvt_pk_bf16_f32 v51, v190, v191\n"
    "v_mfma_f32_16x16x32_bf16 v[120:123], a[130:133], a[244:247], v[120:123]\n"
    "s_waitcnt lgkmcnt(7)\n"
    "ds_read_b128 a[188:191], v14 offset:41728\n"
    "v_mfma_f32_16x16x32_bf16 v[124:127], a[130:133], a[204:207], v[124:127]\n"
    "buffer_load_dwordx4 v3, s[12:15], s24 offen lds\n"
    "v_mfma_f32_16x16x32_bf16 v[160:163], v[192:195], a[232:235], v[160:163]\n"
    "s_add_u32 m0, s37, 94208\n"
    "ds_read_b128 a[220:223], v14 offset:20992\n"
    "v_mfma_f32_16x16x32_bf16 v[164:167], v[192:195], a[240:243], v[164:167]\n"
    "s_mov_b32 s24, 7897088\n"
    "ds_read_b64_tr_b16 a[162:163], v20 offset:16384\n"
    "v_mfma_f32_16x16x32_bf16 v[168:171], v[192:195], a[248:251], v[168:171]\n"
    "s_waitcnt lgkmcnt(9)\n"
    "ds_read_b64_tr_b16 a[164:165], v20 offset:16896\n"
    "v_mfma_f32_16x16x32_bf16 v[172:175], v[192:195], a[228:231], v[172:175]\n"
    "s_waitcnt lgkmcnt(7)\n"
    "ds_read_b64_tr_b16 a[160:161], v20 offset:20480\n"
    "v_mfma_f32_16x16x32_bf16 v[40:43], v[36:39], a[152:155], 0\n"
    "s_waitcnt lgkmcnt(7)\n"
    "buffer_load_dwordx4 v3, s[12:15], s24 offen lds\n"
    "v_mfma_f32_16x16x32_bf16 v[44:47], v[36:39], a[156:159], 0\n"
    "ds_read_b64_tr_b16 a[166:167], v20 offset:20992\n"
    "v_mfma_f32_16x16x32_bf16 v[64:67], v[48:51], a[212:215], v[64:67]\n"
    "ds_read_b64_tr_b16 a[88:89], v20 offset:20480\n"
    "v_mfma_f32_16x16x32_bf16 v[72:75], v[48:51], a[176:179], v[72:75]\n"
    "ds_read_b64_tr_b16 a[90:91], v20 offset:20992\n"
    "v_mfma_f32_16x16x32_bf16 v[80:83], v[48:51], a[180:183], v[80:83]\n"
    "s_waitcnt lgkmcnt(7)\n"
    "v_cvt_pk_bf16_f32 v52, v40, v41\n"
    "v_cvt_pk_bf16_f32 v53, v42, v43\n"
    "v_mfma_f32_16x16x32_bf16 v[88:91], v[48:51], a[188:191], v[88:91]\n"
    "buffer_store_dwordx4 v[32:35], v22, s[16:19], s25 offen sc1\n"
    "v_mfma_f32_16x16x32_bf16 v[96:99], a[138:141], a[192:195], v[96:99]\n"
    "v_cvt_pk_bf16_f32 v54, v44, v45\n"
    "v_cvt_pk_bf16_f32 v55, v46, v47\n"
    "v_mfma_f32_16x16x32_bf16 v[100:103], a[138:141], a[196:199], v[100:103]\n"
    "v_add_f32 v184, v184, v40\n"
    "v_add_f32 v185, v185, v41\n"
    "v_mfma_f32_16x16x32_bf16 v[104:107], a[138:141], a[216:219], v[104:107]\n"
    "ds_read_b128 a[192:195], v14 offset:41856\n"
    "v_add_f32 v186, v186, v42\n"
    "v_mfma_f32_16x16x32_bf16 v[108:111], a[138:141], a[208:211], v[108:111]\n"
    "buffer_store_dwordx4 v[32:35], v22, s[16:19], s26 offen sc1\n"
    "v_mfma_f32_16x16x32_bf16 v[112:115], a[138:141], a[236:239], v[112:115]\n"
    "v_add_f32 v187, v187, v43\n"
    "v_add_f32 v188, v188, v44\n"
    "v_mfma_f32_16x16x32_bf16 v[116:119], a[138:141], a[244:247], v[116:119]\n"
    "v_add_f32 v189, v189, v45\n"
    "v_add_f32 v190, v190, v46\n"
    "v_mfma_f32_16x16x32_bf16 v[120:123], a[138:141], a[204:207], v[120:123]\n"
    "s_waitcnt vmcnt(6) lgkmcnt(1)\n"
    "s_barrier\n"
    "ds_read_b128 a[212:215], v14 offset:21120\n"
    "v_mfma_f32_16x16x32_bf16 v[124:127], a[138:141], a[220:223], v[124:127]\n"
    "s_add_u32 m0, s38, 62208\n"
    "ds_read_b128 a[232:235], v14 offset:384\n"
    "v_add_f32 v191, v191, v47\n"
    "v_mfma_f32_16x16x32_bf16 v[176:179], v[36:39], a[160:163], 0\n"
    "ds_read_b64_tr_b16 a[170:171], v20 offset:24576\n"
    "v_add_u32 v5, 2048, v5\n"
    "v_mfma_f32_16x16x32_bf16 v[180:183], v[36:39], a[164:167], 0\n"
    "buffer_load_dwordx4 v5, s[8:11], 0 offen lds\n"
    "v_mfma_f32_16x16x32_bf16 v[68:71], v[52:55], a[200:203], v[68:71]\n"
    "s_add_u32 m0, s38, 66816\n"
    "ds_read_b64_tr_b16 a[172:173], v20 offset:25088\n"
    "v_cvt_pk_bf16_f32 v192, v184, v185\n"
    "v_mfma_f32_16x16x32_bf16 v[76:79], v[52:55], a[224:227], v[76:79]\n"
    "ds_read_b64_tr_b16 a[168:169], v20 offset:28672\n"
    "v_add_u32 v6, 2048, v6\n"
    "v_mfma_f32_16x16x32_bf16 v[84:87], v[52:55], a[184:187], v[84:87]\n"
    "s_waitcnt lgkmcnt(5)\n"
    "ds_read_b64_tr_b16 a[174:175], v20 offset:29184\n"
    "v_cvt_pk_bf16_f32 v193, v186, v187\n"
    "v_mfma_f32_16x16x32_bf16 v[92:95], v[52:55], a[192:195], v[92:95]\n"
    "ds_read_b64_tr_b16 a[92:93], v20 offset:28672\n"
    "v_cvt_pk_bf16_f32 v194, v188, v189\n"
    "v_mfma_f32_16x16x32_bf16 v[96:99], a[146:149], a[196:199], v[96:99]\n"
    "buffer_load_dwordx4 v6, s[8:11], 0 offen lds\n"
    "v_mfma_f32_16x16x32_bf16 v[100:103], a[146:149], a[216:219], v[100:103]\n"
    "s_add_u32 m0, s38, 71424\n"
    "ds_read_b64_tr_b16 a[94:95], v20 offset:29184\n"
    "v_cvt_pk_bf16_f32 v195, v190, v191\n"
    "v_mfma_f32_16x16x32_bf16 v[104:107], a[146:149], a[208:211], v[104:107]\n"
    "v_cvt_pk_bf16_f32 v48, v176, v177\n"
    "v_add_u32 v7, 2048, v7\n"
    "v_mfma_f32_16x16x32_bf16 v[108:111], a[146:149], a[236:239], v[108:111]\n"
    "v_cvt_pk_bf16_f32 v49, v178, v179\n"
    "v_cvt_pk_bf16_f32 v50, v180, v181\n"
    "v_mfma_f32_16x16x32_bf16 v[112:115], a[146:149], a[244:247], v[112:115]\n"
    "v_cvt_pk_bf16_f32 v51, v182, v183\n"
    "v_mfma_f32_16x16x32_bf16 v[116:119], a[146:149], a[204:207], v[116:119]\n"
    "buffer_load_dwordx4 v7, s[8:11], 0 offen lds\n"
    "v_mfma_f32_16x16x32_bf16 v[120:123], a[146:149], a[220:223], v[120:123]\n"
    "s_waitcnt lgkmcnt(7)\n"
    "s_add_u32 m0, s38, 76032\n"
    "ds_read_b128 a[196:199], v14 offset:41984\n"
    "v_mfma_f32_16x16x32_bf16 v[124:127], a[146:149], a[212:215], v[124:127]\n"
    "ds_read_b128 a[200:203], v14 offset:21248\n"
    "v_add_u32 v8, 2048, v8\n"
    "v_mfma_f32_16x16x32_bf16 v[160:163], v[192:195], a[240:243], v[160:163]\n"
    "ds_read_b64_tr_b16 a[130:131], v20 offset:32768\n"
    "v_mfma_f32_16x16x32_bf16 v[164:167], v[192:195], a[248:251], v[164:167]\n"
    "ds_read_b64_tr_b16 a[132:133], v20 offset:33280\n"
    "v_mfma_f32_16x16x32_bf16 v[168:171], v[192:195], a[228:231], v[168:171]\n"
    "s_waitcnt lgkmcnt(10)\n"
    "buffer_load_dwordx4 v8, s[8:11], 0 offen lds\n"
    "v_mfma_f32_16x16x32_bf16 v[172:175], v[192:195], a[232:235], v[172:175]\n"
    "s_waitcnt lgkmcnt(7)\n"
    "s_add_u32 m0, s7, 62208\n"
    "ds_read_b64_tr_b16 a[128:129], v20 offset:36864\n"
    "v_mfma_f32_16x16x32_bf16 v[184:187], v[36:39], a[168:171], 0\n"
    "s_waitcnt lgkmcnt(7)\n"
    "ds_read_b64_tr_b16 a[134:135], v20 offset:37376\n"
    "v_add_u32 v9, 2048, v9\n"
    "v_mfma_f32_16x16x32_bf16 v[188:191], v[36:39], a[172:175], 0\n"
    "ds_read_b64_tr_b16 a[96:97], v20 offset:36864\n"
    "v_mfma_f32_16x16x32_bf16 v[64:67], v[48:51], a[176:179], v[64:67]\n"
    "ds_read_b64_tr_b16 a[98:99], v20 offset:37376\n"
    "v_mfma_f32_16x16x32_bf16 v[72:75], v[48:51], a[180:183], v[72:75]\n"
    "buffer_load_dwordx4 v9, s[8:11], 0 offen lds\n"
    "v_mfma_f32_16x16x32_bf16 v[80:83], v[48:51], a[188:191], v[80:83]\n"
    "s_waitcnt lgkmcnt(7)\n"
    "v_cvt_pk_bf16_f32 v52, v184, v185\n"
    "v_cvt_pk_bf16_f32 v53, v186, v187\n"
    "v_mfma_f32_16x16x32_bf16 v[88:91], v[48:51], a[196:199], v[88:91]\n"
    "v_cvt_pk_bf16_f32 v54, v188, v189\n"
    "v_cvt_pk_bf16_f32 v55, v190, v191\n"
    "v_mfma_f32_16x16x32_bf16 v[96:99], a[154:157], a[216:219], v[96:99]\n"
    "v_add_f32 v176, v176, v184\n"
    "v_add_f32 v177, v177, v185\n"
    "v_mfma_f32_16x16x32_bf16 v[100:103], a[154:157], a[208:211], v[100:103]\n"
    "ds_read_b128 a[176:179], v14 offset:42112\n"
    "v_add_f32 v178, v178, v186\n"
    "v_mfma_f32_16x16x32_bf16 v[104:107], a[154:157], a[236:239], v[104:107]\n"
    "buffer_store_dwordx4 v[32:35], v22, s[16:19], s27 offen sc1\n"
    "v_mfma_f32_16x16x32_bf16 v[108:111], a[154:157], a[244:247], v[108:111]\n"
    "v_add_f32 v179, v179, v187\n"
    "v_add_f32 v180, v180, v188\n"
    "v_mfma_f32_16x16x32_bf16 v[112:115], a[154:157], a[204:207], v[112:115]\n"
    "v_add_f32 v181, v181, v189\n"
    "v_add_f32 v182, v182, v190\n"
    "v_mfma_f32_16x16x32_bf16 v[116:119], a[154:157], a[220:223], v[116:119]\n"
    "v_add_f32 v183, v183, v191\n"
    "v_cvt_pk_bf16_f32 v192, v176, v177\n"
    "v_mfma_f32_16x16x32_bf16 v[120:123], a[154:157], a[212:215], v[120:123]\n"
    "s_waitcnt lgkmcnt(7)\n"
    "ds_read_b128 a[216:219], v14 offset:21376\n"
    "v_cvt_pk_bf16_f32 v193, v178, v179\n"
    "v_mfma_f32_16x16x32_bf16 v[124:127], a[154:157], a[200:203], v[124:127]\n"
    "s_waitcnt lgkmcnt(5)\n"
    "buffer_store_dwordx4 v[32:35], v22, s[16:19], s28 offen sc1\n"
    "v_mfma_f32_16x16x32_bf16 v[40:43], v[36:39], a[128:131], 0\n"
    "s_waitcnt lgkmcnt(4)\n"
    "ds_read_b128 a[240:243], v14 offset:640\n"
    "v_cvt_pk_bf16_f32 v194, v180, v181\n"
    "v_mfma_f32_16x16x32_bf16 v[44:47], v[36:39], a[132:135], 0\n"
    "ds_read_b64_tr_b16 a[138:139], v20 offset:40960\n"
    "v_cvt_pk_bf16_f32 v195, v182, v183\n"
    "v_mfma_f32_16x16x32_bf16 v[68:71], v[52:55], a[224:227], v[68:71]\n"
    "ds_read_b64_tr_b16 a[140:141], v20 offset:41472\n"
    "v_mfma_f32_16x16x32_bf16 v[76:79], v[52:55], a[184:187], v[76:79]\n"
    "ds_read_b64_tr_b16 a[136:137], v20 offset:45056\n"
    "v_mfma_f32_16x16x32_bf16 v[84:87], v[52:55], a[192:195], v[84:87]\n"
    "s_waitcnt lgkmcnt(5)\n"
    "buffer_store_dwordx4 v[32:35], v22, s[16:19], s29 offen sc1\n"
    "v_mfma_f32_16x16x32_bf16 v[92:95], v[52:55], a[176:179], v[92:95]\n"
    "ds_read_b64_tr_b16 a[142:143], v20 offset:45568\n"
    "v_cvt_pk_bf16_f32 v48, v40, v41\n"
    "v_mfma_f32_16x16x32_bf16 v[96:99], a[162:165], a[208:211], v[96:99]\n"
    "ds_read_b64_tr_b16 a[100:101], v20 offset:45056\n"
    "v_cvt_pk_bf16_f32 v49, v42, v43\n"
    "v_mfma_f32_16x16x32_bf16 v[100:103], a[162:165], a[236:239], v[100:103]\n"
    "ds_read_b64_tr_b16 a[102:103], v20 offset:45568\n"
    "v_cvt_pk_bf16_f32 v50, v44, v45\n"
    "v_mfma_f32_16x16x32_bf16 v[104:107], a[162:165], a[244:247], v[104:107]\n"
    "v_cvt_pk_bf16_f32 v51, v46, v47\n"
    "v_mfma_f32_16x16x32_bf16 v[108:111], a[162:165], a[204:207], v[108:111]\n"
    "buffer_store_dwordx4 v[32:35], v22, s[16:19], s30 offen sc1\n"
    "v_mfma_f32_16x16x32_bf16 v[112:115], a[162:165], a[220:223], v[112:115]\n"
    "v_mfma_f32_16x16x32_bf16 v[116:119], a[162:165], a[212:215], v[116:119]\n"
    "v_mfma_f32_16x16x32_bf16 v[120:123], a[162:165], a[200:203], v[120:123]\n"
    "s_waitcnt lgkmcnt(7)\n"
    "ds_read_b128 a[208:211], v14 offset:42240\n"
    "v_mfma_f32_16x16x32_bf16 v[124:127], a[162:165], a[216:219], v[124:127]\n"
    "ds_read_b128 a[224:227], v14 offset:21504\n"
    "v_mfma_f32_16x16x32_bf16 v[160:163], v[192:195], a[248:251], v[160:163]\n"
    "buffer_store_dwordx4 v[32:35], v22, s[16:19], s31 offen sc1\n"
    "v_mfma_f32_16x16x32_bf16 v[164:167], v[192:195], a[228:231], v[164:167]\n"
    "ds_read_b64_tr_b16 a[146:147], v20 offset:49152\n"
    "v_mfma_f32_16x16x32_bf16 v[168:171], v[192:195], a[232:235], v[168:171]\n"
    "s_waitcnt lgkmcnt(9)\n"
    "ds_read_b64_tr_b16 a[148:149], v20 offset:49664\n"
    "v_mfma_f32_16x16x32_bf16 v[172:175], v[192:195], a[240:243], v[172:175]\n"
    "s_waitcnt lgkmcnt(7)\n"
    "ds_read_b64_tr_b16 a[144:145], v20 offset:53248\n"
    "v_mfma_f32_16x16x32_bf16 v[176:179], v[36:39], a[136:139], 0\n"
    "s_waitcnt lgkmcnt(7)\n"
    "ds_read_b64_tr_b16 a[150:151], v20 offset:53760\n"
    "v_mfma_f32_16x16x32_bf16 v[180:183], v[36:39], a[140:143], 0\n"
    "v_mfma_f32_16x16x32_bf16 v[64:67], v[48:51], a[180:183], v[64:67]\n"
    "ds_read_b64_tr_b16 a[104:105], v20 offset:53248\n"
    "v_mfma_f32_16x16x32_bf16 v[72:75], v[48:51], a[188:191], v[72:75]\n"
    "ds_read_b64_tr_b16 a[106:107], v20 offset:53760\n"
    "v_mfma_f32_16x16x32_bf16 v[80:83], v[48:51], a[196:199], v[80:83]\n"
    "s_waitcnt lgkmcnt(7)\n"
    "v_cvt_pk_bf16_f32 v52, v176, v177\n"
    "v_cvt_pk_bf16_f32 v53, v178, v179\n"
    "v_mfma_f32_16x16x32_bf16 v[88:91], v[48:51], a[208:211], v[88:91]\n"
    "v_cvt_pk_bf16_f32 v54, v180, v181\n"
    "v_cvt_pk_bf16_f32 v55, v182, v183\n"
    "v_mfma_f32_16x16x32_bf16 v[96:99], a[170:173], a[236:239], v[96:99]\n"
    "v_add_f32 v40, v40, v176\n"
    "v_add_f32 v41, v41, v177\n"
    "v_mfma_f32_16x16x32_bf16 v[100:103], a[170:173], a[244:247], v[100:103]\n"
    "ds_read_b128 a[180:183], v14 offset:42368\n"
    "v_add_f32 v42, v42, v178\n"
    "v_mfma_f32_16x16x32_bf16 v[104:107], a[170:173], a[204:207], v[104:107]\n"
    "ds_read_b128 a[236:239], v14 offset:21632\n"
    "v_add_f32 v43, v43, v179\n"
    "v_mfma_f32_16x16x32_bf16 v[108:111], a[170:173], a[220:223], v[108:111]\n"
    "ds_read_b128 a[248:251], v14 offset:896\n"
    "v_add_f32 v44, v44, v180\n"
    "v_mfma_f32_16x16x32_bf16 v[112:115], a[170:173], a[212:215], v[112:115]\n"
    "ds_read_b64_tr_b16 a[154:155], v20 offset:57344\n"
    "v_add_f32 v45, v45, v181\n"
    "v_mfma_f32_16x16x32_bf16 v[116:119], a[170:173], a[200:203], v[116:119]\n"
    "v_add_f32 v46, v46, v182\n"
    "v_add_f32 v47, v47, v183\n"
    "v_mfma_f32_16x16x32_bf16 v[120:123], a[170:173], a[216:219], v[120:123]\n"
    "s_waitcnt lgkmcnt(10)\n"
    "ds_read_b64_tr_b16 a[156:157], v20 offset:57856\n"
    "v_cvt_pk_bf16_f32 v192, v40, v41\n"
    "v_mfma_f32_16x16x32_bf16 v[124:127], a[170:173], a[224:227], v[124:127]\n"
    "s_waitcnt lgkmcnt(8)\n"
    "ds_read_b64_tr_b16 a[152:153], v20 offset:61440\n"
    "v_cvt_pk_bf16_f32 v193, v42, v43\n"
    "v_mfma_f32_16x16x32_bf16 v[184:187], v[36:39], a[144:147], 0\n"
    "s_waitcnt lgkmcnt(8)\n"
    "ds_read_b64_tr_b16 a[158:159], v20 offset:61952\n"
    "v_cvt_pk_bf16_f32 v194, v44, v45\n"
    "v_mfma_f32_16x16x32_bf16 v[188:191], v[36:39], a[148:151], 0\n"
    "ds_read_b64_tr_b16 a[108:109], v20 offset:61440\n"
    "v_cvt_pk_bf16_f32 v195, v46, v47\n"
    "v_mfma_f32_16x16x32_bf16 v[68:71], v[52:55], a[184:187], v[68:71]\n"
    "v_mfma_f32_16x16x32_bf16 v[76:79], v[52:55], a[192:195], v[76:79]\n"
    "ds_read_b64_tr_b16 a[110:111], v20 offset:61952\n"
    "v_mfma_f32_16x16x32_bf16 v[84:87], v[52:55], a[176:179], v[84:87]\n"
    "s_waitcnt vmcnt(5) lgkmcnt(8)\n"
    "s_barrier\n"
    "ds_read_b64_tr_b16 v[200:201], v18 offset:62208\n"
    "v_cvt_pk_bf16_f32 v48, v184, v185\n"
    "v_mfma_f32_16x16x32_bf16 v[92:95], v[52:55], a[180:183], v[92:95]\n"
    "ds_read_b64_tr_b16 v[202:203], v18 offset:0\n"
    "v_cvt_pk_bf16_f32 v49, v186, v187\n"
    "v_mfma_f32_16x16x32_bf16 v[96:99], a[130:133], a[244:247], v[96:99]\n"
    "ds_read_b64 v[216:217], v62 offset:62208\n"
    "v_cvt_pk_bf16_f32 v50, v188, v189\n"
    "v_mfma_f32_16x16x32_bf16 v[100:103], a[130:133], a[204:207], v[100:103]\n"
    "v_cvt_pk_bf16_f32 v51, v190, v191\n"
    "v_mfma_f32_16x16x32_bf16 v[104:107], a[130:133], a[220:223], v[104:107]\n"
    "ds_read_b64 v[218:219], v62\n"
    "v_mfma_f32_16x16x32_bf16 v[108:111], a[130:133], a[212:215], v[108:111]\n"
    "ds_read_b64_tr_b16 v[204:205], v18 offset:62336\n"
    "v_mfma_f32_16x16x32_bf16 v[112:115], a[130:133], a[200:203], v[112:115]\n"
    "ds_read_b64_tr_b16 v[206:207], v18 offset:128\n"
    "v_mfma_f32_16x16x32_bf16 v[116:119], a[130:133], a[216:219], v[116:119]\n"
    "ds_read_b128 a[184:187], v15 offset:41472\n"
    "v_mfma_f32_16x16x32_bf16 v[120:123], a[130:133], a[224:227], v[120:123]\n"
    "s_waitcnt lgkmcnt(14)\n"
    "v_mfma_f32_16x16x32_bf16 v[124:127], a[130:133], a[236:239], v[124:127]\n"
    "ds_read_b64_tr_b16 v[208:209], v18 offset:62464\n"
    "v_mfma_f32_16x16x32_bf16 v[160:163], v[192:195], a[228:231], v[160:163]\n"
    "s_waitcnt lgkmcnt(10)\n"
    "ds_read_b64_tr_b16 v[210:211], v18 offset:256\n"
    "v_mfma_f32_16x16x32_bf16 v[164:167], v[192:195], a[232:235], v[164:167]\n"
    "ds_read_b64_tr_b16 v[212:213], v18 offset:62592\n"
    "v_mfma_f32_16x16x32_bf16 v[168:171], v[192:195], a[240:243], v[168:171]\n"
    "ds_read_b64_tr_b16 v[214:215], v18 offset:384\n"
    "v_mfma_f32_16x16x32_bf16 v[172:175], v[192:195], a[248:251], v[172:175]\n"
    "v_mfma_f32_16x16x32_bf16 v[40:43], v[36:39], a[152:155], 0\n"
    "ds_read_b128 a[228:231], v15 offset:20736\n"
    "v_mfma_f32_16x16x32_bf16 v[44:47], v[36:39], a[156:159], 0\n"
    "ds_read_b64_tr_b16 a[162:163], v21 offset:0\n"
    "v_mfma_f32_16x16x32_bf16 v[64:67], v[48:51], a[188:191], v[64:67]\n"
    "s_waitcnt lgkmcnt(10)\n"
    "ds_read_b64_tr_b16 a[164:165], v21 offset:512\n"
    "v_mfma_f32_16x16x32_bf16 v[72:75], v[48:51], a[196:199], v[72:75]\n"
    "ds_read_b64_tr_b16 a[160:161], v21 offset:4096\n"
    "v_mfma_f32_16x16x32_bf16 v[128:131], v[200:203], v[196:199], 0\n"
    "s_waitcnt lgkmcnt(11)\n"
    "v_lshlrev_b32 v240, 16, v216\n"
    "v_and_b32 v241, s48, v216\n"
    "v_mfma_f32_16x16x32_bf16 v[80:83], v[48:51], a[208:211], v[80:83]\n"
    "s_waitcnt lgkmcnt(9)\n"
    "ds_read_b64_tr_b16 a[166:167], v21 offset:4608\n"
    "v_cvt_pk_bf16_f32 v52, v40, v41\n"
    "v_mfma_f32_16x16x32_bf16 v[132:135], v[204:207], v[196:199], 0\n"
    "s_waitcnt lgkmcnt(9)\n"
    "ds_read_b64_tr_b16 a[112:113], v21 offset:4096\n"
    "v_cvt_pk_bf16_f32 v53, v42, v43\n"
    "v_mfma_f32_16x16x32_bf16 v[88:91], v[48:51], a[184:187], v[88:91]\n"
    "s_waitcnt lgkmcnt(8)\n"
    "ds_read_b64_tr_b16 a[114:115], v21 offset:4608\n"
    "v_cvt_pk_bf16_f32 v54, v44, v45\n"
    "v_mfma_f32_16x16x32_bf16 v[136:139], v[208:211], v[196:199], 0\n"
    "v_cvt_pk_bf16_f32 v55, v46, v47\n"
    "v_lshlrev_b32 v242, 16, v217\n"
    "v_mfma_f32_16x16x32_bf16 v[96:99], a[138:141], a[204:207], v[96:99]\n"
    "s_waitcnt lgkmcnt(7)\n"
    "v_and_b32 v243, s48, v217\n"
    "v_lshlrev_b32 v244, 16, v218\n"
    "v_mfma_f32_16x16x32_bf16 v[140:143], v[212:215], v[196:199], 0\n"
    "v_and_b32 v245, s48, v218\n"
    "v_lshlrev_b32 v246, 16, v219\n"
    "v_mfma_f32_16x16x32_bf16 v[100:103], a[138:141], a[220:223], v[100:103]\n"
    "v_and_b32 v247, s48, v219\n"
    "v_sub_f32 v240, v240, v244\n"
    "v_mfma_f32_16x16x32_bf16 v[104:107], a[138:141], a[212:215], v[104:107]\n"
    "ds_read_b128 a[188:191], v15 offset:41600\n"
    "v_sub_f32 v241, v241, v245\n"
    "v_mfma_f32_16x16x32_bf16 v[108:111], a[138:141], a[200:203], v[108:111]\n"
    "v_sub_f32 v242, v242, v246\n"
    "v_sub_f32 v243, v243, v247\n"
    "v_mfma_f32_16x16x32_bf16 v[112:115], a[138:141], a[216:219], v[112:115]\n"
    "v_cvt_pk_bf16_f32 v240, v240, v241\n"
    "v_cvt_pk_bf16_f32 v241, v242, v243\n"
    "v_mfma_f32_16x16x32_bf16 v[116:119], a[138:141], a[224:227], v[116:119]\n"
    "ds_write_b64 v62, v[240:241] offset:62208\n"
    "v_cvt_pk_bf16_f32 v128, v128, v129\n"
    "v_mfma_f32_16x16x32_bf16 v[120:123], a[138:141], a[236:239], v[120:123]\n"
    "s_waitcnt lgkmcnt(8)\n"
    "ds_read_b128 a[204:207], v15 offset:20864\n"
    "v_cvt_pk_bf16_f32 v129, v130, v131\n"
    "v_mfma_f32_16x16x32_bf16 v[124:127], a[138:141], a[228:231], v[124:127]\n"
    "s_waitcnt lgkmcnt(6)\n"
    "ds_read_b128 a[244:247], v15 offset:128\n"
    "v_add_f32 v184, v184, v40\n"
    "v_mfma_f32_16x16x32_bf16 v[176:179], v[36:39], a[160:163], 0\n"
    "s_waitcnt lgkmcnt(6)\n"
    "ds_read_b64_tr_b16 a[170:171], v21 offset:8192\n"
    "v_add_f32 v185, v185, v41\n"
    "v_mfma_f32_16x16x32_bf16 v[180:183], v[36:39], a[164:167], 0\n"
    "ds_write_b64 v60, v[128:129] offset:62208\n"
    "v_cvt_pk_bf16_f32 v132, v132, v133\n"
    "v_mfma_f32_16x16x32_bf16 v[68:71], v[52:55], a[192:195], v[68:71]\n"
    "ds_read_b64_tr_b16 a[172:173], v21 offset:8704\n"
    "v_cvt_pk_bf16_f32 v133, v134, v135\n"
    "v_mfma_f32_16x16x32_bf16 v[76:79], v[52:55], a[176:179], v[76:79]\n"
    "ds_read_b64_tr_b16 a[168:169], v21 offset:12288\n"
    "v_add_f32 v186, v186, v42\n"
    "v_mfma_f32_16x16x32_bf16 v[84:87], v[52:55], a[180:183], v[84:87]\n"
    "s_waitcnt lgkmcnt(7)\n"
    "ds_read_b64_tr_b16 a[174:175], v21 offset:12800\n"
    "v_add_f32 v187, v187, v43\n"
    "v_mfma_f32_16x16x32_bf16 v[92:95], v[52:55], a[188:191], v[92:95]\n"
    "ds_read_b64_tr_b16 a[116:117], v21 offset:12288\n"
    "v_add_f32 v188, v188, v44\n"
    "v_mfma_f32_16x16x32_bf16 v[96:99], a[146:149], a[220:223], v[96:99]\n"
    "ds_write_b64 v60, v[132:133] offset:62336\n"
    "v_cvt_pk_bf16_f32 v136, v136, v137\n"
    "v_mfma_f32_16x16x32_bf16 v[100:103], a[146:149], a[212:215], v[100:103]\n"
    "ds_read_b64_tr_b16 a[118:119], v21 offset:12800\n"
    "v_cvt_pk_bf16_f32 v137, v138, v139\n"
    "v_mfma_f32_16x16x32_bf16 v[104:107], a[146:149], a[200:203], v[104:107]\n"
    "ds_write_b64 v60, v[136:137] offset:62464\n"
    "v_cvt_pk_bf16_f32 v140, v140, v141\n"
    "v_mfma_f32_16x16x32_bf16 v[108:111], a[146:149], a[216:219], v[108:111]\n"
    "v_cvt_pk_bf16_f32 v141, v142, v143\n"
    "v_add_f32 v189, v189, v45\n"
    "v_mfma_f32_16x16x32_bf16 v[112:115], a[146:149], a[224:227], v[112:115]\n"
    "ds_read_b128 a[192:195], v15 offset:41728\n"
    "v_add_f32 v190, v190, v46\n"
    "v_mfma_f32_16x16x32_bf16 v[116:119], a[146:149], a[236:239], v[116:119]\n"
    "ds_write_b64 v60, v[140:141] offset:62592\n"
    "v_add_f32 v191, v191, v47\n"
    "v_mfma_f32_16x16x32_bf16 v[120:123], a[146:149], a[228:231], v[120:123]\n"
    "s_waitcnt lgkmcnt(12)\n"
    "ds_read_b128 a[220:223], v15 offset:20992\n"
    "v_cvt_pk_bf16_f32 v192, v184, v185\n"
    "v_mfma_f32_16x16x32_bf16 v[124:127], a[146:149], a[204:207], v[124:127]\n"
    "ds_read_b64_tr_b16 a[130:131], v21 offset:16384\n"
    "v_cvt_pk_bf16_f32 v193, v186, v187\n"
    "v_cvt_pk_bf16_f32 v194, v188, v189\n"
    "v_cvt_pk_bf16_f32 v195, v190, v191\n"
    "s_nop 1\n"
    "v_mfma_f32_16x16x32_bf16 v[160:163], v[192:195], a[232:235], v[160:163]\n"
    "ds_read_b64_tr_b16 a[132:133], v21 offset:16896\n"
    "v_cvt_pk_bf16_f32 v48, v176, v177\n"
    "v_mfma_f32_16x16x32_bf16 v[164:167], v[192:195], a[240:243], v[164:167]\n"
    "s_waitcnt lgkmcnt(10)\n"
    "ds_read_b64_tr_b16 a[128:129], v21 offset:20480\n"
    "v_cvt_pk_bf16_f32 v49, v178, v179\n"
    "v_mfma_f32_16x16x32_bf16 v[168:171], v[192:195], a[248:251], v[168:171]\n"
    "v_cvt_pk_bf16_f32 v50, v180, v181\n"
    "v_cvt_pk_bf16_f32 v51, v182, v183\n"
    "v_mfma_f32_16x16x32_bf16 v[172:175], v[192:195], a[244:247], v[172:175]\n"
    "ds_read_b64_tr_b16 a[134:135], v21 offset:20992\n"
    "v_mfma_f32_16x16x32_bf16 v[184:187], v[36:39], a[168:171], 0\n"
    "s_waitcnt lgkmcnt(11)\n"
    "ds_read_b64_tr_b16 a[120:121], v21 offset:20480\n"
    "v_mfma_f32_16x16x32_bf16 v[188:191], v[36:39], a[172:175], 0\n"
    "ds_read_b64_tr_b16 a[122:123], v21 offset:20992\n"
    "v_mfma_f32_16x16x32_bf16 v[64:67], v[48:51], a[196:199], v[64:67]\n"
    "ds_read_b64_tr_b16 v[200:201], v19 offset:62208\n"
    "v_mfma_f32_16x16x32_bf16 v[72:75], v[48:51], a[208:211], v[72:75]\n"
    "v_mfma_f32_16x16x32_bf16 v[80:83], v[48:51], a[184:187], v[80:83]\n"
    "s_waitcnt lgkmcnt(9)\n"
    "ds_read_b64_tr_b16 v[202:203], v19 offset:0\n"
    "v_cvt_pk_bf16_f32 v52, v184, v185\n"
    "v_mfma_f32_16x16x32_bf16 v[88:91], v[48:51], a[192:195], v[88:91]\n"
    "ds_read_b64_tr_b16 v[204:205], v19 offset:62336\n"
    "v_cvt_pk_bf16_f32 v53, v186, v187\n"
    "v_mfma_f32_16x16x32_bf16 v[96:99], a[154:157], a[212:215], v[96:99]\n"
    "ds_read_b64_tr_b16 v[206:207], v19 offset:128\n"
    "v_cvt_pk_bf16_f32 v54, v188, v189\n"
    "v_mfma_f32_16x16x32_bf16 v[100:103], a[154:157], a[200:203], v[100:103]\n"
    "ds_read_b128 a[196:199], v15 offset:41856\n"
    "v_cvt_pk_bf16_f32 v55, v190, v191\n"
    "v_mfma_f32_16x16x32_bf16 v[104:107], a[154:157], a[216:219], v[104:107]\n"
    "v_add_f32 v176, v176, v184\n"
    "v_add_f32 v177, v177, v185\n"
    "v_mfma_f32_16x16x32_bf16 v[108:111], a[154:157], a[224:227], v[108:111]\n"
    "ds_read_b64_tr_b16 v[208:209], v19 offset:62464\n"
    "v_add_f32 v178, v178, v186\n"
    "v_mfma_f32_16x16x32_bf16 v[112:115], a[154:157], a[236:239], v[112:115]\n"
    "s_waitcnt vmcnt(0)\n"
    "ds_read_b64_tr_b16 v[210:211], v19 offset:256\n"
    "v_lshl_add_u64 v[26:27], v[26:27], 0, 1\n"
    "v_mfma_f32_16x16x32_bf16 v[116:119], a[154:157], a[228:231], v[116:119]\n"
    "s_waitcnt lgkmcnt(10)\n"
    "ds_read_b64_tr_b16 v[212:213], v19 offset:62592\n"
    "v_add_f32 v179, v179, v187\n"
    "v_mfma_f32_16x16x32_bf16 v[120:123], a[154:157], a[204:207], v[120:123]\n"
    "ds_read_b64_tr_b16 v[214:215], v19 offset:384\n"
    "v_add_f32 v180, v180, v188\n"
    "v_mfma_f32_16x16x32_bf16 v[124:127], a[154:157], a[220:223], v[124:127]\n"
    "buffer_store_dwordx2 v[26:27], v24, s[16:19], s40 offen sc1\n"
    "v_mfma_f32_16x16x32_bf16 v[40:43], v[36:39], a[128:131], 0\n"
    "s_waitcnt lgkmcnt(11)\n"
    "v_add_f32 v181, v181, v189\n"
    "v_add_f32 v182, v182, v190\n"
    "v_mfma_f32_16x16x32_bf16 v[44:47], v[36:39], a[132:135], 0\n"
    "v_add_f32 v183, v183, v191\n"
    "v_cvt_pk_bf16_f32 v192, v176, v177\n"
    "v_mfma_f32_16x16x32_bf16 v[68:71], v[52:55], a[176:179], v[68:71]\n"
    "ds_read_b128 a[176:179], v15 offset:21120\n"
    "v_cvt_pk_bf16_f32 v193, v178, v179\n"
    "v_mfma_f32_16x16x32_bf16 v[76:79], v[52:55], a[180:183], v[76:79]\n"
    "s_waitcnt lgkmcnt(8)\n"
    "ds_read_b128 a[212:215], v15 offset:384\n"
    "v_cvt_pk_bf16_f32 v194, v180, v181\n"
    "v_mfma_f32_16x16x32_bf16 v[128:131], v[200:203], v[196:199], 0\n"
    "v_cvt_pk_bf16_f32 v195, v182, v183\n"
    "v_cvt_pk_bf16_f32 v48, v40, v41\n"
    "v_mfma_f32_16x16x32_bf16 v[84:87], v[52:55], a[188:191], v[84:87]\n"
    "s_waitcnt lgkmcnt(7)\n"
    "ds_read_b64_tr_b16 a[138:139], v21 offset:24576\n"
    "v_cvt_pk_bf16_f32 v49, v42, v43\n"
    "v_mfma_f32_16x16x32_bf16 v[132:135], v[204:207], v[196:199], 0\n"
    "s_waitcnt lgkmcnt(7)\n"
    "ds_read_b64_tr_b16 a[140:141], v21 offset:25088\n"
    "v_cvt_pk_bf16_f32 v50, v44, v45\n"
    "v_mfma_f32_16x16x32_bf16 v[92:95], v[52:55], a[196:199], v[92:95]\n"
    "s_waitcnt lgkmcnt(6)\n"
    "ds_read_b64_tr_b16 a[136:137], v21 offset:28672\n"
    "v_cvt_pk_bf16_f32 v51, v46, v47\n"
    "v_mfma_f32_16x16x32_bf16 v[136:139], v[208:211], v[196:199], 0\n"
    "ds_read_b64_tr_b16 a[142:143], v21 offset:29184\n"
    "v_cvt_pk_bf16_f32 v128, v128, v129\n"
    "v_mfma_f32_16x16x32_bf16 v[96:99], a[162:165], a[200:203], v[96:99]\n"
    "s_waitcnt lgkmcnt(6)\n"
    "v_cvt_pk_bf16_f32 v129, v130, v131\n"
    "v_mfma_f32_16x16x32_bf16 v[140:143], v[212:215], v[196:199], 0\n"
    "ds_read_b64_tr_b16 a[124:125], v21 offset:28672\n"
    "v_mfma_f32_16x16x32_bf16 v[100:103], a[162:165], a[216:219], v[100:103]\n"
    "ds_read_b64_tr_b16 a[126:127], v21 offset:29184\n"
    "v_mfma_f32_16x16x32_bf16 v[104:107], a[162:165], a[224:227], v[104:107]\n"
    "ds_write_b64 v61, v[128:129] offset:62208\n"
    "v_cvt_pk_bf16_f32 v132, v132, v133\n"
    "v_mfma_f32_16x16x32_bf16 v[108:111], a[162:165], a[236:239], v[108:111]\n"
    "v_cvt_pk_bf16_f32 v133, v134, v135\n"
    "v_mfma_f32_16x16x32_bf16 v[112:115], a[162:165], a[228:231], v[112:115]\n"
    "ds_write_b64 v61, v[132:133] offset:62336\n"
    "v_cvt_pk_bf16_f32 v136, v136, v137\n"
    "v_mfma_f32_16x16x32_bf16 v[116:119], a[162:165], a[204:207], v[116:119]\n"
    "v_cvt_pk_bf16_f32 v137, v138, v139\n"
    "v_mfma_f32_16x16x32_bf16 v[120:123], a[162:165], a[220:223], v[120:123]\n"
    "s_waitcnt lgkmcnt(9)\n"
    "ds_read_b128 a[200:203], v15 offset:41984\n"
    "v_mfma_f32_16x16x32_bf16 v[124:127], a[162:165], a[176:179], v[124:127]\n"
    "ds_write_b64 v61, v[136:137] offset:62464\n"
    "v_cvt_pk_bf16_f32 v140, v140, v141\n"
    "v_mfma_f32_16x16x32_bf16 v[160:163], v[192:195], a[240:243], v[160:163]\n"
    "v_cvt_pk_bf16_f32 v141, v142, v143\n"
    "v_mfma_f32_16x16x32_bf16 v[164:167], v[192:195], a[248:251], v[164:167]\n"
    "ds_write_b64 v61, v[140:141] offset:62592\n"
    "v_mfma_f32_16x16x32_bf16 v[168:171], v[192:195], a[244:247], v[168:171]\n"
    "s_waitcnt lgkmcnt(11)\n"
    "v_mfma_f32_16x16x32_bf16 v[172:175], v[192:195], a[212:215], v[172:175]\n"
    "s_waitcnt lgkmcnt(8)\n"
    "v_mfma_f32_16x16x32_bf16 v[176:179], v[36:39], a[136:139], 0\n"
    "s_waitcnt lgkmcnt(7)\n"
    "v_mfma_f32_16x16x32_bf16 v[180:183], v[36:39], a[140:143], 0\n"
    "ds_read_b128 a[232:235], v15 offset:21248\n"
    "v_mfma_f32_16x16x32_bf16 v[64:67], v[48:51], a[208:211], v[64:67]\n"
    "v_mfma_f32_16x16x32_bf16 v[72:75], v[48:51], a[184:187], v[72:75]\n"
    "v_mfma_f32_16x16x32_bf16 v[80:83], v[48:51], a[192:195], v[80:83]\n"
    "s_waitcnt lgkmcnt(3)\n"
    "s_nop 0\n"
    "v_cvt_pk_bf16_f32 v52, v176, v177\n"
    "v_cvt_pk_bf16_f32 v53, v178, v179\n"
    "v_mfma_f32_16x16x32_bf16 v[88:91], v[48:51], a[200:203], v[88:91]\n"
    "v_cvt_pk_bf16_f32 v54, v180, v181\n"
    "v_cvt_pk_bf16_f32 v55, v182, v183\n"
    "v_mfma_f32_16x16x32_bf16 v[96:99], a[170:173], a[216:219], v[96:99]\n"
    "ds_read_b128 a[184:187], v15 offset:42112\n"
    "v_add_f32 v40, v40, v176\n"
    "v_mfma_f32_16x16x32_bf16 v[100:103], a[170:173], a[224:227], v[100:103]\n"
    "v_add_f32 v41, v41, v177\n"
    "v_add_f32 v42, v42, v178\n"
    "v_mfma_f32_16x16x32_bf16 v[104:107], a[170:173], a[236:239], v[104:107]\n"
    "v_add_f32 v43, v43, v179\n"
    "v_add_f32 v44, v44, v180\n"
    "v_mfma_f32_16x16x32_bf16 v[108:111], a[170:173], a[228:231], v[108:111]\n"
    "v_add_f32 v45, v45, v181\n"
    "v_add_f32 v46, v46, v182\n"
    "v_mfma_f32_16x16x32_bf16 v[112:115], a[170:173], a[204:207], v[112:115]\n"
    "v_add_f32 v47, v47, v183\n"
    "v_cvt_pk_bf16_f32 v192, v40, v41\n"
    "v_mfma_f32_16x16x32_bf16 v[116:119], a[170:173], a[220:223], v[116:119]\n"
    "v_cvt_pk_bf16_f32 v193, v42, v43\n"
    "v_cvt_pk_bf16_f32 v194, v44, v45\n"
    "v_mfma_f32_16x16x32_bf16 v[120:123], a[170:173], a[176:179], v[120:123]\n"
    "s_waitcnt lgkmcnt(1)\n"
    "v_cvt_pk_bf16_f32 v195, v46, v47\n"
    "v_mfma_f32_16x16x32_bf16 v[124:127], a[170:173], a[232:235], v[124:127]\n"
    "v_mfma_f32_16x16x32_bf16 v[68:71], v[52:55], a[180:183], v[68:71]\n"
    "ds_read_b128 a[180:183], v15 offset:21376\n"
    "v_mfma_f32_16x16x32_bf16 v[76:79], v[52:55], a[188:191], v[76:79]\n"
    "v_mfma_f32_16x16x32_bf16 v[84:87], v[52:55], a[196:199], v[84:87]\n"
    "s_waitcnt lgkmcnt(1)\n"
    "v_mfma_f32_16x16x32_bf16 v[92:95], v[52:55], a[184:187], v[92:95]\n"
    "v_mfma_f32_16x16x32_bf16 v[96:99], a[130:133], a[224:227], v[96:99]\n"
    "ds_read_b128 a[184:187], v15 offset:640\n"
    "v_mfma_f32_16x16x32_bf16 v[100:103], a[130:133], a[236:239], v[100:103]\n"
    "v_mfma_f32_16x16x32_bf16 v[104:107], a[130:133], a[228:231], v[104:107]\n"
    "v_mfma_f32_16x16x32_bf16 v[108:111], a[130:133], a[204:207], v[108:111]\n"
    "v_mfma_f32_16x16x32_bf16 v[112:115], a[130:133], a[220:223], v[112:115]\n"
    "v_mfma_f32_16x16x32_bf16 v[116:119], a[130:133], a[176:179], v[116:119]\n"
    "v_mfma_f32_16x16x32_bf16 v[120:123], a[130:133], a[232:235], v[120:123]\n"
    "s_waitcnt lgkmcnt(1)\n"
    "buffer_load_dwordx2 v[28:29], v23, s[16:19], s40 offen offset:0 sc1\n"
    "v_mfma_f32_16x16x32_bf16 v[124:127], a[130:133], a[180:183], v[124:127]\n"
    "v_mfma_f32_16x16x32_bf16 v[160:163], v[192:195], a[248:251], v[160:163]\n"
    "ds_read_b128 a[188:191], v15 offset:21504\n"
    "v_mfma_f32_16x16x32_bf16 v[164:167], v[192:195], a[244:247], v[164:167]\n"
    "s_barrier\n"
    "s_setpc_b64 s[52:53]\n"
    ".fill (LP0_%=+24576-.)/4, 4, 0xbf800000\n"
    "LV0_%=:\n"
    "s_nop 3\n"
    "v_add_f32 v80, v80, v168\n"
    "v_add_f32 v81, v81, v169\n"
    "v_mfma_f32_16x16x32_bf16 v[104:107], a[130:133], a[232:235], v[104:107]\n"
    "v_add_f32 v82, v82, v170\n"
    "v_add_f32 v83, v83, v171\n"
    "v_mfma_f32_16x16x32_bf16 v[108:111], a[130:133], a[212:215], v[108:111]\n"
    "v_add_f32 v84, v84, v168\n"
    "v_add_f32 v85, v85, v169\n"
    "v_mfma_f32_16x16x32_bf16 v[112:115], a[130:133], a[192:195], v[112:115]\n"
    "ds_read_b128 a[176:179], v15 offset:21504\n"
    "v_add_f32 v86, v86, v170\n"
    "v_mfma_f32_16x16x32_bf16 v[116:119], a[130:133], a[188:191], v[116:119]\n"
    "s_barrier\n"
    "ds_read_b128 a[184:187], v10 offset:62848\n"
    "v_add_f32 v87, v87, v171\n"
    "v_mfma_f32_16x16x32_bf16 v[120:123], a[130:133], a[196:199], v[120:123]\n"
    "s_waitcnt lgkmcnt(2)\n"
    "ds_read_b128 a[200:203], v10 offset:62976\n"
    "v_add_f32 v88, v88, v172\n"
    "v_mfma_f32_16x16x32_bf16 v[124:127], a[130:133], a[180:183], v[124:127]\n"
    "ds_read_b128 a[204:207], v10 offset:63104\n"
    "v_add_f32 v89, v89, v173\n"
    "v_mfma_f32_16x16x32_bf16 v[96:99], a[138:141], a[228:231], v[96:99]\n"
    "ds_read_b128 a[208:211], v11 offset:62208\n"
    "v_add_f32 v90, v90, v174\n"
    "v_mfma_f32_16x16x32_bf16 v[100:103], a[138:141], a[232:235], v[100:103]\n"
    "v_add_f32 v91, v91, v175\n"
    "v_add_f32 v92, v92, v172\n"
    "v_mfma_f32_16x16x32_bf16 v[104:107], a[138:141], a[212:215], v[104:107]\n"
    "v_add_f32 v93, v93, v173\n"
    "v_add_f32 v94, v94, v174\n"
    "v_mfma_f32_16x16x32_bf16 v[108:111], a[138:141], a[192:195], v[108:111]\n"
    "v_add_f32 v95, v95, v175\n"
    "v_add_f32 v64, v64, v160\n"
    "v_mfma_f32_16x16x32_bf16 v[112:115], a[138:141], a[188:191], v[112:115]\n"
    "ds_read_b128 a[188:191], v11 offset:62336\n"
    "v_add_f32 v65, v65, v161\n"
    "v_mfma_f32_16x16x32_bf16 v[116:119], a[138:141], a[196:199], v[116:119]\n"
    "v_add_f32 v66, v66, v162\n"
    "v_add_f32 v67, v67, v163\n"
    "v_mfma_f32_16x16x32_bf16 v[120:123], a[138:141], a[180:183], v[120:123]\n"
    "s_waitcnt lgkmcnt(5)\n"
    "v_add_f32 v68, v68, v160\n"
    "v_add_f32 v69, v69, v161\n"
    "v_mfma_f32_16x16x32_bf16 v[124:127], a[138:141], a[176:179], v[124:127]\n"
    "s_waitcnt lgkmcnt(4)\n"
    "v_add_f32 v70, v70, v162\n"
    "v_add_f32 v71, v71, v163\n"
    "v_mfma_f32_16x16x32_bf16 v[144:147], a[4:7], a[184:187], v[80:83]\n"
    "s_waitcnt lgkmcnt(3)\n"
    "ds_read_b128 a[176:179], v11 offset:62464\n"
    "v_add_f32 v72, v72, v164\n"
    "v_mfma_f32_16x16x32_bf16 v[148:151], a[4:7], a[200:203], v[84:87]\n"
    "s_waitcnt lgkmcnt(3)\n"
    "v_add_f32 v73, v73, v165\n"
    "v_add_f32 v74, v74, v166\n"
    "v_mfma_f32_16x16x32_bf16 v[152:155], a[4:7], a[204:207], v[88:91]\n"
    "s_waitcnt lgkmcnt(2)\n"
    "v_add_f32 v75, v75, v167\n"
    "v_add_f32 v76, v76, v164\n"
    "v_mfma_f32_16x16x32_bf16 v[156:159], a[4:7], a[208:211], v[92:95]\n"
    "v_add_f32 v77, v77, v165\n"
    "v_add_f32 v78, v78, v166\n"
    "v_mfma_f32_16x16x32_bf16 v[144:147], a[8:11], a[200:203], v[144:147]\n"
    "ds_read_b128 a[180:183], v11 offset:62592\n"
    "v_add_f32 v79, v79, v167\n"
    "v_mfma_f32_16x16x32_bf16 v[148:151], a[8:11], a[204:207], v[148:151]\n"
    "v_add_f32 v96, v96, v64\n"
    "v_add_f32 v97, v97, v65\n"
    "v_mfma_f32_16x16x32_bf16 v[152:155], a[8:11], a[208:211], v[152:155]\n"
    "s_waitcnt lgkmcnt(2)\n"
    "v_add_f32 v98, v98, v66\n"
    "v_add_f32 v99, v99, v67\n"
    "v_mfma_f32_16x16x32_bf16 v[156:159], a[8:11], a[188:191], v[156:159]\n"
    "v_add_f32 v100, v100, v68\n"
    "v_add_f32 v101, v101, v69\n"
    "v_mfma_f32_16x16x32_bf16 v[144:147], a[12:15], a[204:207], v[144:147]\n"
    "ds_read_b128 a[184:187], v11 offset:62720\n"
    "v_add_f32 v102, v102, v70\n"
    "v_mfma_f32_16x16x32_bf16 v[148:151], a[12:15], a[208:211], v[148:151]\n"
    "v_add_f32 v103, v103, v71\n"
    "v_cvt_pk_bf16_f32 v200, v96, v97\n"
    "v_mfma_f32_16x16x32_bf16 v[152:155], a[12:15], a[188:191], v[152:155]\n"
    "s_waitcnt lgkmcnt(2)\n"
    "v_cvt_pk_bf16_f32 v201, v98, v99\n"
    "v_cvt_pk_bf16_f32 v202, v100, v101\n"
    "v_mfma_f32_16x16x32_bf16 v[156:159], a[12:15], a[176:179], v[156:159]\n"
    "v_cvt_pk_bf16_f32 v203, v102, v103\n"
    "v_mfma_f32_16x16x32_bf16 v[144:147], a[16:19], a[208:211], v[144:147]\n"
    "s_xor_b32 s36, s39, 131072\n"
    "ds_read_b128 a[192:195], v11 offset:62848\n"
    "v_min_u32 v200, -2, v200\n"
    "v_mfma_f32_16x16x32_bf16 v[148:151], a[16:19], a[188:191], v[148:151]\n"
    "v_min_u32 v201, -2, v201\n"
    "v_min_u32 v202, -2, v202\n"
    "v_mfma_f32_16x16x32_bf16 v[152:155], a[16:19], a[176:179], v[152:155]\n"
    "s_waitcnt lgkmcnt(2)\n"
    "v_min_u32 v203, -2, v203\n"
    "v_mfma_f32_16x16x32_bf16 v[156:159], a[16:19], a[180:183], v[156:159]\n"
    "s_waitcnt vmcnt(0)\n"
    "v_cmp_ne_u64 vcc, v[28:29], v[26:27]\n"
    "s_cbranch_vccz LF0ok_%=\n"
    "s_mov_b32 s43, 0x100000\n"
    "LF0lp_%=:\n"
    "buffer_load_dwordx2 v[28:29], v23, s[16:19], s40 offen offset:0 sc1\n"
    "s_waitcnt vmcnt(0)\n"
    "s_sub_u32 s43, s43, 1\n"
    "s_cbranch_scc1 LF0ok_%=\n"
    "v_cmp_ne_u64 vcc, v[28:29], v[26:27]\n"
    "s_cbranch_vccnz LF0lp_%=\n"
    "LF0ok_%=:\n"
    "v_mfma_f32_16x16x32_bf16 v[144:147], a[20:23], a[188:191], v[144:147]\n"
    "ds_read_b128 a[188:191], v11 offset:62976\n"
    "v_mfma_f32_16x16x32_bf16 v[148:151], a[20:23], a[176:179], v[148:151]\n"
    "buffer_store_dwordx4 v[200:203], v22, s[16:19], s36 offen sc1\n"
    "v_mfma_f32_16x16x32_bf16 v[152:155], a[20:23], a[180:183], v[152:155]\n"
    "s_waitcnt lgkmcnt(2)\n"
    "v_add_f32 v104, v104, v72\n"
    "v_add_f32 v105, v105, v73\n"
    "v_mfma_f32_16x16x32_bf16 v[156:159], a[20:23], a[184:187], v[156:159]\n"
    "v_add_f32 v106, v106, v74\n"
    "v_add_f32 v107, v107, v75\n"
    "v_mfma_f32_16x16x32_bf16 v[144:147], a[24:27], a[176:179], v[144:147]\n"
    "ds_read_b128 a[176:179], v11 offset:63104\n"
    "v_add_f32 v108, v108, v76\n"
    "v_mfma_f32_16x16x32_bf16 v[148:151], a[24:27], a[180:183], v[148:151]\n"
    "v_add_f32 v109, v109, v77\n"
    "v_add_f32 v110, v110, v78\n"
    "v_mfma_f32_16x16x32_bf16 v[152:155], a[24:27], a[184:187], v[152:155]\n"
    "s_waitcnt lgkmcnt(2)\n"
    "v_add_f32 v111, v111, v79\n"
    "v_cvt_pk_bf16_f32 v204, v104, v105\n"
    "v_mfma_f32_16x16x32_bf16 v[156:159], a[24:27], a[192:195], v[156:159]\n"
    "v_cvt_pk_bf16_f32 v205, v106, v107\n"
    "v_cvt_pk_bf16_f32 v206, v108, v109\n"
    "v_mfma_f32_16x16x32_bf16 v[144:147], a[28:31], a[180:183], v[144:147]\n"
    "ds_read_b128 a[180:183], v12 offset:62208\n"
    "v_cvt_pk_bf16_f32 v207, v110, v111\n"
    "v_mfma_f32_16x16x32_bf16 v[148:151], a[28:31], a[184:187], v[148:151]\n"
    "s_xor_b32 s36, s39, 163840\n"
    "v_min_u32 v204, -2, v204\n"
    "v_min_u32 v205, -2, v205\n"
    "v_mfma_f32_16x16x32_bf16 v[152:155], a[28:31], a[192:195], v[152:155]\n"
    "s_waitcnt lgkmcnt(2)\n"
    "v_min_u32 v206, -2, v206\n"
    "v_min_u32 v207, -2, v207\n"
    "v_mfma_f32_16x16x32_bf16 v[156:159], a[28:31], a[188:191], v[156:159]\n"
    "buffer_store_dwordx4 v[204:207], v22, s[16:19], s36 offen sc1\n"
    "v_mfma_f32_16x16x32_bf16 v[144:147], a[32:35], a[184:187], v[144:147]\n"
    "ds_read_b128 a[184:187], v12 offset:62336\n"
    "v_add_f32 v112, v112, v80\n"
    "v_mfma_f32_16x16x32_bf16 v[148:151], a[32:35], a[192:195], v[148:151]\n"
    "v_add_f32 v113, v113, v81\n"
    "v_add_f32 v114, v114, v82\n"
    "v_mfma_f32_16x16x32_bf16 v[152:155], a[32:35], a[188:191], v[152:155]\n"
    "s_waitcnt lgkmcnt(2)\n"
    "v_add_f32 v115, v115, v83\n"
    "v_add_f32 v116, v116, v84\n"
    "v_mfma_f32_16x16x32_bf16 v[156:159], a[32:35], a[176:179], v[156:159]\n"
    "v_add_f32 v117, v117, v85\n"
    "v_add_f32 v118, v118, v86\n"
    "v_mfma_f32_16x16x32_bf16 v[144:147], a[36:39], a[192:195], v[144:147]\n"
    "ds_read_b128 a[192:195], v12 offset:62464\n"
    "v_add_f32 v119, v119, v87\n"
    "v_mfma_f32_16x16x32_bf16 v[148:151], a[36:39], a[188:191], v[148:151]\n"
    "v_cvt_pk_bf16_f32 v208, v112, v113\n"
    "v_cvt_pk_bf16_f32 v209, v114, v115\n"
    "v_mfma_f32_16x16x32_bf16 v[152:155], a[36:39], a[176:179], v[152:155]\n"
    "s_waitcnt lgkmcnt(2)\n"
    "v_cvt_pk_bf16_f32 v210, v116, v117\n"
    "v_cvt_pk_bf16_f32 v211, v118, v119\n"
    "v_mfma_f32_16x16x32_bf16 v[156:159], a[36:39], a[180:183], v[156:159]\n"
    "s_xor_b32 s36, s39, 196608\n"
    "v_min_u32 v208, -2, v208\n"
    "v_min_u32 v209, -2, v209\n"
    "v_mfma_f32_16x16x32_bf16 v[144:147], a[40:43], a[188:191], v[144:147]\n"
    "ds_read_b128 a[188:191], v12 offset:62592\n"
    "v_min_u32 v210, -2, v210\n"
    "v_mfma_f32_16x16x32_bf16 v[148:151], a[40:43], a[176:179], v[148:151]\n"
    "v_min_u32 v211, -2, v211\n"
    "v_mfma_f32_16x16x32_bf16 v[152:155], a[40:43], a[180:183], v[152:155]\n"
    "s_waitcnt lgkmcnt(2)\n"
    "buffer_store_dwordx4 v[208:211], v22, s[16:19], s36 offen sc1\n"
    "v_mfma_f32_16x16x32_bf16 v[156:159], a[40:43], a[184:187], v[156:159]\n"
    "v_add_f32 v120, v120, v88\n"
    "v_add_f32 v121, v121, v89\n"
    "v_mfma_f32_16x16x32_bf16 v[144:147], a[44:47], a[176:179], v[144:147]\n"
    "ds_read_b128 a[176:179], v12 offset:62720\n"
    "v_add_f32 v122, v122, v90\n"
    "v_mfma_f32_16x16x32_bf16 v[148:151], a[44:47], a[180:183], v[148:151]\n"
    "v_add_f32 v123, v123, v91\n"
    "v_add_f32 v124, v124, v92\n"
    "v_mfma_f32_16x16x32_bf16 v[152:155], a[44:47], a[184:187], v[152:155]\n"
    "s_waitcnt lgkmcnt(2)\n"
    "v_add_f32 v125, v125, v93\n"
    "v_add_f32 v126, v126, v94\n"
    "v_mfma_f32_16x16x32_bf16 v[156:159], a[44:47], a[192:195], v[156:159]\n"
    "v_add_f32 v127, v127, v95\n"
    "v_cvt_pk_bf16_f32 v212, v120, v121\n"
    "v_mfma_f32_16x16x32_bf16 v[144:147], a[48:51], a[180:183], v[144:147]\n"
    "ds_read_b128 a[180:183], v12 offset:62848\n"
    "v_cvt_pk_bf16_f32 v213, v122, v123\n"
    "v_mfma_f32_16x16x32_bf16 v[148:151], a[48:51], a[184:187], v[148:151]\n"
    "v_cvt_pk_bf16_f32 v214, v124, v125\n"
    "v_cvt_pk_bf16_f32 v215, v126, v127\n"
    "v_mfma_f32_16x16x32_bf16 v[152:155], a[48:51], a[192:195], v[152:155]\n"
    "s_waitcnt lgkmcnt(2)\n"
    "s_xor_b32 s36, s39, 229376\n"
    "v_min_u32 v212, -2, v212\n"
    "v_mfma_f32_16x16x32_bf16 v[156:159], a[48:51], a[188:191], v[156:159]\n"
    "v_min_u32 v213, -2, v213\n"
    "v_min_u32 v214, -2, v214\n"
    "v_mfma_f32_16x16x32_bf16 v[144:147], a[52:55], a[184:187], v[144:147]\n"
    "ds_read_b128 a[184:187], v12 offset:62976\n"
    "v_min_u32 v215, -2, v215\n"
    "v_mfma_f32_16x16x32_bf16 v[148:151], a[52:55], a[192:195], v[148:151]\n"
    "ds_read_b128 a[196:199], v13 offset:62848\n"
    "v_mfma_f32_16x16x32_bf16 v[152:155], a[52:55], a[188:191], v[152:155]\n"
    "s_waitcnt lgkmcnt(3)\n"
    "ds_read_b128 a[200:203], v13 offset:62976\n"
    "v_mfma_f32_16x16x32_bf16 v[156:159], a[52:55], a[176:179], v[156:159]\n"
    "ds_read_b128 a[204:207], v13 offset:63104\n"
    "v_mfma_f32_16x16x32_bf16 v[144:147], a[56:59], a[192:195], v[144:147]\n"
    "ds_read_b128 a[192:195], v14 offset:62208\n"
    "v_mfma_f32_16x16x32_bf16 v[148:151], a[56:59], a[188:191], v[148:151]\n"
    "buffer_store_dwordx4 v[212:215], v22, s[16:19], s36 offen sc1\n"
    "v_mfma_f32_16x16x32_bf16 v[152:155], a[56:59], a[176:179], v[152:155]\n"
    "s_waitcnt lgkmcnt(5)\n"
    "buffer_store_dwordx2 v[26:27], v24, s[16:19], s41 offen sc1\n"
    "v_mfma_f32_16x16x32_bf16 v[156:159], a[56:59], a[180:183], v[156:159]\n"
    "v_mfma_f32_16x16x32_bf16 v[144:147], a[60:63], a[188:191], v[144:147]\n"
    "ds_read_b128 a[188:191], v14 offset:62336\n"
    "v_mfma_f32_16x16x32_bf16 v[148:151], a[60:63], a[176:179], v[148:151]\n"
    "v_mfma_f32_16x16x32_bf16 v[152:155], a[60:63], a[180:183], v[152:155]\n"
    "s_waitcnt lgkmcnt(5)\n"
    "v_mfma_f32_16x16x32_bf16 v[156:159], a[60:63], a[184:187], v[156:159]\n"
    "s_waitcnt lgkmcnt(4)\n"
    "v_mfma_f32_16x16x32_bf16 v[144:147], a[68:71], a[196:199], v[144:147]\n"
    "s_waitcnt lgkmcnt(3)\n"
    "ds_read_b128 a[176:179], v14 offset:62464\n"
    "v_mfma_f32_16x16x32_bf16 v[148:151], a[68:71], a[200:203], v[148:151]\n"
    "s_waitcnt lgkmcnt(3)\n"
    "v_mfma_f32_16x16x32_bf16 v[152:155], a[68:71], a[204:207], v[152:155]\n"
    "s_waitcnt lgkmcnt(2)\n"
    "v_mfma_f32_16x16x32_bf16 v[156:159], a[68:71], a[192:195], v[156:159]\n"
    "v_mfma_f32_16x16x32_bf16 v[144:147], a[72:75], a[200:203], v[144:147]\n"
    "ds_read_b128 a[180:183], v14 offset:62592\n"
    "v_mfma_f32_16x16x32_bf16 v[148:151], a[72:75], a[204:207], v[148:151]\n"
    "v_mfma_f32_16x16x32_bf16 v[152:155], a[72:75], a[192:195], v[152:155]\n"
    "s_waitcnt lgkmcnt(2)\n"
    "v_mfma_f32_16x16x32_bf16 v[156:159], a[72:75], a[188:191], v[156:159]\n"
    "v_mfma_f32_16x16x32_bf16 v[144:147], a[76:79], a[204:207], v[144:147]\n"
    "ds_read_b128 a[184:187], v14 offset:62720\n"
    "v_mfma_f32_16x16x32_bf16 v[148:151], a[76:79], a[192:195], v[148:151]\n"
    "v_mfma_f32_16x16x32_bf16 v[152:155], a[76:79], a[188:191], v[152:155]\n"
    "s_waitcnt lgkmcnt(2)\n"
    "v_mfma_f32_16x16x32_bf16 v[156:159], a[76:79], a[176:179], v[156:159]\n"
    "v_mfma_f32_16x16x32_bf16 v[144:147], a[80:83], a[192:195], v[144:147]\n"
    "ds_read_b128 a[192:195], v14 offset:62848\n"
    "v_mfma_f32_16x16x32_bf16 v[148:151], a[80:83], a[188:191], v[148:151]\n"
    "v_mfma_f32_16x16x32_bf16 v[152:155], a[80:83], a[176:179], v[152:155]\n"
    "s_waitcnt lgkmcnt(2)\n"
    "v_mfma_f32_16x16x32_bf16 v[156:159], a[80:83], a[180:183], v[156:159]\n"
    "v_mfma_f32_16x16x32_bf16 v[144:147], a[84:87], a[188:191], v[144:147]\n"
    "ds_read_b128 a[188:191], v14 offset:62976\n"
    "v_mfma_f32_16x16x32_bf16 v[148:151], a[84:87], a[176:179], v[148:151]\n"
    "v_mfma_f32_16x16x32_bf16 v[152:155], a[84:87], a[180:183], v[152:155]\n"
    "s_waitcnt lgkmcnt(2)\n"
    "v_mfma_f32_16x16x32_bf16 v[156:159], a[84:87], a[184:187], v[156:159]\n"
    "v_mfma_f32_16x16x32_bf16 v[144:147], a[88:91], a[176:179], v[144:147]\n"
    "ds_read_b128 a[176:179], v14 offset:63104\n"
    "v_mfma_f32_16x16x32_bf16 v[148:151], a[88:91], a[180:183], v[148:151]\n"
    "v_mfma_f32_16x16x32_bf16 v[152:155], a[88:91], a[184:187], v[152:155]\n"
    "s_waitcnt lgkmcnt(2)\n"
    "v_mfma_f32_16x16x32_bf16 v[156:159], a[88:91], a[192:195], v[156:159]\n"
    "v_mfma_f32_16x16x32_bf16 v[144:147], a[92:95], a[180:183], v[144:147]\n"
    "ds_read_b128 a[180:183], v15 offset:62208\n"
    "v_mfma_f32_16x16x32_bf16 v[148:151], a[92:95], a[184:187], v[148:151]\n"
    "v_mfma_f32_16x16x32_bf16 v[152:155], a[92:95], a[192:195], v[152:155]\n"
    "s_waitcnt lgkmcnt(2)\n"
    "v_mfma_f32_16x16x32_bf16 v[156:159], a[92:95], a[188:191], v[156:159]\n"
    "v_mfma_f32_16x16x32_bf16 v[144:147], a[96:99], a[184:187], v[144:147]\n"
    "ds_read_b128 a[184:187], v15 offset:62336\n"
    "v_mfma_f32_16x16x32_bf16 v[148:151], a[96:99], a[192:195], v[148:151]\n"
    "v_mfma_f32_16x16x32_bf16 v[152:155], a[96:99], a[188:191], v[152:155]\n"
    "s_waitcnt lgkmcnt(2)\n"
    "v_mfma_f32_16x16x32_bf16 v[156:159], a[96:99], a[176:179], v[156:159]\n"
    "v_mfma_f32_16x16x32_bf16 v[144:147], a[100:103], a[192:195], v[144:147]\n"
    "ds_read_b128 a[192:195], v15 offset:62464\n"
    "v_mfma_f32_16x16x32_bf16 v[148:151], a[100:103], a[188:191], v[148:151]\n"
    "v_mfma_f32_16x16x32_bf16 v[152:155], a[100:103], a[176:179], v[152:155]\n"
    "s_waitcnt lgkmcnt(2)\n"
    "v_mfma_f32_16x16x32_bf16 v[156:159], a[100:103], a[180:183], v[156:159]\n"
    "v_mfma_f32_16x16x32_bf16 v[144:147], a[104:107], a[188:191], v[144:147]\n"
    "ds_read_b128 a[188:191], v15 offset:62592\n"
    "v_mfma_f32_16x16x32_bf16 v[148:151], a[104:107], a[176:179], v[148:151]\n"
    "v_mfma_f32_16x16x32_bf16 v[152:155], a[104:107], a[180:183], v[152:155]\n"
    "s_waitcnt lgkmcnt(2)\n"
    "v_mfma_f32_16x16x32_bf16 v[156:159], a[104:107], a[184:187], v[156:159]\n"
    "v_mfma_f32_16x16x32_bf16 v[144:147], a[108:111], a[176:179], v[144:147]\n"
    "ds_read_b128 a[176:179], v15 offset:62720\n"
    "v_mfma_f32_16x16x32_bf16 v[148:151], a[108:111], a[180:183], v[148:151]\n"
    "v_mfma_f32_16x16x32_bf16 v[152:155], a[108:111], a[184:187], v[152:155]\n"
    "s_waitcnt lgkmcnt(2)\n"
    "v_mfma_f32_16x16x32_bf16 v[156:159], a[108:111], a[192:195], v[156:159]\n"
    "v_mfma_f32_16x16x32_bf16 v[144:147], a[112:115], a[180:183], v[144:147]\n"
    "ds_read_b128 a[180:183], v15 offset:62848\n"
    "v_mfma_f32_16x16x32_bf16 v[148:151], a[112:115], a[184:187], v[148:151]\n"
    "buffer_load_dwordx4 v[160:163], v22, s[16:19], s28 offen sc1\n"
    "v_mfma_f32_16x16x32_bf16 v[152:155], a[112:115], a[192:195], v[152:155]\n"
    "s_waitcnt lgkmcnt(2)\n"
    "v_mfma_f32_16x16x32_bf16 v[156:159], a[112:115], a[188:191], v[156:159]\n"
    "v_mfma_f32_16x16x32_bf16 v[144:147], a[116:119], a[184:187], v[144:147]\n"
    "ds_read_b128 a[184:187], v15 offset:62976\n"
    "v_mfma_f32_16x16x32_bf16 v[148:151], a[116:119], a[192:195], v[148:151]\n"
    "ds_read_b128 a[196:199], v10 offset:62592\n"
    "v_mfma_f32_16x16x32_bf16 v[152:155], a[116:119], a[188:191], v[152:155]\n"
    "s_waitcnt lgkmcnt(3)\n"
    "ds_read_b128 a[200:203], v10 offset:62720\n"
    "v_mfma_f32_16x16x32_bf16 v[156:159], a[116:119], a[176:179], v[156:159]\n"
    "buffer_load_dwordx4 v[164:167], v22, s[16:19], s30 offen sc1\n"
    "v_mfma_f32_16x16x32_bf16 v[144:147], a[120:123], a[192:195], v[144:147]\n"
    "ds_read_b128 a[192:195], v10 offset:62848\n"
    "v_mfma_f32_16x16x32_bf16 v[148:151], a[120:123], a[188:191], v[148:151]\n"
    "v_mfma_f32_16x16x32_bf16 v[152:155], a[120:123], a[176:179], v[152:155]\n"
    "s_waitcnt lgkmcnt(4)\n"
    "ds_read_b128 a[204:207], v10 offset:62976\n"
    "v_mfma_f32_16x16x32_bf16 v[156:159], a[120:123], a[180:183], v[156:159]\n"
    "buffer_load_dwordx4 v[168:171], v22, s[16:19], s29 offen sc1\n"
    "v_mfma_f32_16x16x32_bf16 v[144:147], a[124:127], a[188:191], v[144:147]\n"
    "ds_read_b128 a[188:191], v10 offset:63104\n"
    "v_mfma_f32_16x16x32_bf16 v[148:151], a[124:127], a[176:179], v[148:151]\n"
    "v_mfma_f32_16x16x32_bf16 v[152:155], a[124:127], a[180:183], v[152:155]\n"
    "s_waitcnt lgkmcnt(5)\n"
    "ds_read_b128 a[176:179], v11 offset:62208\n"
    "v_mfma_f32_16x16x32_bf16 v[156:159], a[124:127], a[184:187], v[156:159]\n"
    "s_waitcnt lgkmcnt(5)\n"
    "buffer_load_dwordx4 v[172:175], v22, s[16:19], s31 offen sc1\n"
    "v_mfma_f32_16x16x32_bf16 v[136:139], a[4:7], a[196:199], v[72:75]\n"
    "s_waitcnt lgkmcnt(4)\n"
    "ds_read_b128 a[180:183], v11 offset:62336\n"
    "v_mfma_f32_16x16x32_bf16 v[140:143], a[4:7], a[200:203], v[76:79]\n"
    "v_mfma_f32_16x16x32_bf16 v[136:139], a[8:11], a[200:203], v[136:139]\n"
    "s_waitcnt lgkmcnt(4)\n"
    "ds_read_b128 a[184:187], v11 offset:62464\n"
    "v_mfma_f32_16x16x32_bf16 v[140:143], a[8:11], a[192:195], v[140:143]\n"
    "v_cvt_pk_bf16_f32 v216, v144, v145\n"
    "v_cvt_pk_bf16_f32 v217, v146, v147\n"
    "v_mfma_f32_16x16x32_bf16 v[136:139], a[12:15], a[192:195], v[136:139]\n"
    "s_waitcnt lgkmcnt(4)\n"
    "ds_read_b128 a[192:195], v11 offset:62592\n"
    "v_cvt_pk_bf16_f32 v218, v148, v149\n"
    "v_mfma_f32_16x16x32_bf16 v[140:143], a[12:15], a[204:207], v[140:143]\n"
    "v_cvt_pk_bf16_f32 v219, v150, v151\n"
    "v_mfma_f32_16x16x32_bf16 v[136:139], a[16:19], a[204:207], v[136:139]\n"
    "s_waitcnt lgkmcnt(4)\n"
    "s_xor_b32 s24, s39, 65536\n"
    "ds_read_b128 a[196:199], v11 offset:62720\n"
    "v_mfma_f32_16x16x32_bf16 v[140:143], a[16:19], a[188:191], v[140:143]\n"
    "v_min_u32 v216, -2, v216\n"
    "v_min_u32 v217, -2, v217\n"
    "v_mfma_f32_16x16x32_bf16 v[136:139], a[20:23], a[188:191], v[136:139]\n"
    "s_waitcnt lgkmcnt(4)\n"
    "ds_read_b128 a[188:191], v11 offset:62848\n"
    "v_min_u32 v218, -2, v218\n"
    "v_mfma_f32_16x16x32_bf16 v[140:143], a[20:23], a[176:179], v[140:143]\n"
    "v_min_u32 v219, -2, v219\n"
    "v_mfma_f32_16x16x32_bf16 v[136:139], a[24:27], a[176:179], v[136:139]\n"
    "s_waitcnt lgkmcnt(4)\n"
    "ds_read_b128 a[176:179], v11 offset:62976\n"
    "v_mfma_f32_16x16x32_bf16 v[140:143], a[24:27], a[180:183], v[140:143]\n"
    "buffer_store_dwordx4 v[216:219], v22, s[16:19], s24 offen sc1\n"
    "v_mfma_f32_16x16x32_bf16 v[136:139], a[28:31], a[180:183], v[136:139]\n"
    "s_waitcnt lgkmcnt(4)\n"
    "ds_read_b128 a[180:183], v11 offset:63104\n"
    "v_cvt_pk_bf16_f32 v220, v152, v153\n"
    "v_mfma_f32_16x16x32_bf16 v[140:143], a[28:31], a[184:187], v[140:143]\n"
    "v_cvt_pk_bf16_f32 v221, v154, v155\n"
    "v_cvt_pk_bf16_f32 v222, v156, v157\n"
    "v_mfma_f32_16x16x32_bf16 v[136:139], a[32:35], a[184:187], v[136:139]\n"
    "s_waitcnt lgkmcnt(4)\n"
    "ds_read_b128 a[184:187], v12 offset:62208\n"
    "v_cvt_pk_bf16_f32 v223, v158, v159\n"
    "v_mfma_f32_16x16x32_bf16 v[140:143], a[32:35], a[192:195], v[140:143]\n"
    "s_xor_b32 s24, s39, 98304\n"
    "v_min_u32 v220, -2, v220\n"
    "v_min_u32 v221, -2, v221\n"
    "v_mfma_f32_16x16x32_bf16 v[136:139], a[36:39], a[192:195], v[136:139]\n"
    "s_waitcnt lgkmcnt(4)\n"
    "ds_read_b128 a[192:195], v12 offset:62336\n"
    "v_min_u32 v222, -2, v222\n"
    "v_mfma_f32_16x16x32_bf16 v[140:143], a[36:39], a[196:199], v[140:143]\n"
    "v_min_u32 v223, -2, v223\n"
    "v_mfma_f32_16x16x32_bf16 v[136:139], a[40:43], a[196:199], v[136:139]\n"
    "s_waitcnt lgkmcnt(4)\n"
    "ds_read_b128 a[196:199], v12 offset:62464\n"
    "v_mfma_f32_16x16x32_bf16 v[140:143], a[40:43], a[188:191], v[140:143]\n"
    "ds_read_b128 a[200:203], v13 offset:62592\n"
    "v_mfma_f32_16x16x32_bf16 v[136:139], a[44:47], a[188:191], v[136:139]\n"
    "s_waitcnt lgkmcnt(5)\n"
    "ds_read_b128 a[188:191], v13 offset:62720\n"
    "v_mfma_f32_16x16x32_bf16 v[140:143], a[44:47], a[176:179], v[140:143]\n"
    "buffer_store_dwordx4 v[220:223], v22, s[16:19], s24 offen sc1\n"
    "v_mfma_f32_16x16x32_bf16 v[136:139], a[48:51], a[176:179], v[136:139]\n"
    "s_waitcnt lgkmcnt(5)\n"
    "ds_read_b128 a[176:179], v13 offset:62848\n"
    "v_mfma_f32_16x16x32_bf16 v[140:143], a[48:51], a[180:183], v[140:143]\n"
    "v_mfma_f32_16x16x32_bf16 v[136:139], a[52:55], a[180:183], v[136:139]\n"
    "s_waitcnt lgkmcnt(5)\n"
    "ds_read_b128 a[180:183], v13 offset:62976\n"
    "v_mfma_f32_16x16x32_bf16 v[140:143], a[52:55], a[184:187], v[140:143]\n"
    "v_mfma_f32_16x16x32_bf16 v[136:139], a[56:59], a[184:187], v[136:139]\n"
    "s_waitcnt lgkmcnt(5)\n"
    "ds_read_b128 a[184:187], v13 offset:63104\n"
    "v_mfma_f32_16x16x32_bf16 v[140:143], a[56:59], a[192:195], v[140:143]\n"
    "v_mfma_f32_16x16x32_bf16 v[136:139], a[60:63], a[192:195], v[136:139]\n"
    "s_waitcnt lgkmcnt(5)\n"
    "ds_read_b128 a[192:195], v14 offset:62208\n"
    "v_mfma_f32_16x16x32_bf16 v[140:143], a[60:63], a[196:199], v[140:143]\n"
    "s_waitcnt lgkmcnt(5)\n"
    "v_mfma_f32_16x16x32_bf16 v[136:139], a[68:71], a[200:203], v[136:139]\n"
    "s_waitcnt lgkmcnt(4)\n"
    "ds_read_b128 a[196:199], v14 offset:62336\n"
    "v_mfma_f32_16x16x32_bf16 v[140:143], a[68:71], a[188:191], v[140:143]\n"
    "v_mfma_f32_16x16x32_bf16 v[136:139], a[72:75], a[188:191], v[136:139]\n"
    "s_waitcnt lgkmcnt(4)\n"
    "ds_read_b128 a[188:191], v14 offset:62464\n"
    "v_mfma_f32_16x16x32_bf16 v[140:143], a[72:75], a[176:179], v[140:143]\n"
    "v_mfma_f32_16x16x32_bf16 v[136:139], a[76:79], a[176:179], v[136:139]\n"
    "s_waitcnt lgkmcnt(4)\n"
    "ds_read_b128 a[176:179], v14 offset:62592\n"
    "v_mfma_f32_16x16x32_bf16 v[140:143], a[76:79], a[180:183], v[140:143]\n"
    "v_mfma_f32_16x16x32_bf16 v[136:139], a[80:83], a[180:183], v[136:139]\n"
    "s_waitcnt lgkmcnt(4)\n"
    "ds_read_b128 a[180:183], v14 offset:62720\n"
    "v_mfma_f32_16x16x32_bf16 v[140:143], a[80:83], a[184:187], v[140:143]\n"
    "v_mfma_f32_16x16x32_bf16 v[136:139], a[84:87], a[184:187], v[136:139]\n"
    "s_waitcnt lgkmcnt(4)\n"
    "ds_read_b128 a[184:187], v14 offset:62848\n"
    "v_mfma_f32_16x16x32_bf16 v[140:143], a[84:87], a[192:195], v[140:143]\n"
    "v_mfma_f32_16x16x32_bf16 v[136:139], a[88:91], a[192:195], v[136:139]\n"
    "s_waitcnt lgkmcnt(4)\n"
    "ds_read_b128 a[192:195], v14 offset:62976\n"
    "v_mfma_f32_16x16x32_bf16 v[140:143], a[88:91], a[196:199], v[140:143]\n"
    "v_mfma_f32_16x16x32_bf16 v[136:139], a[92:95], a[196:199], v[136:139]\n"
    "s_waitcnt lgkmcnt(4)\n"
    "ds_read_b128 a[196:199], v14 offset:63104\n"
    "v_mfma_f32_16x16x32_bf16 v[140:143], a[92:95], a[188:191], v[140:143]\n"
    "v_mfma_f32_16x16x32_bf16 v[136:139], a[96:99], a[188:191], v[136:139]\n"
    "s_waitcnt lgkmcnt(4)\n"
    "ds_read_b128 a[188:191], v15 offset:62208\n"
    "v_mfma_f32_16x16x32_bf16 v[140:143], a[96:99], a[176:179], v[140:143]\n"
    "v_mfma_f32_16x16x32_bf16 v[136:139], a[100:103], a[176:179], v[136:139]\n"
    "s_waitcnt lgkmcnt(4)\n"
    "ds_read_b128 a[176:179], v15 offset:62336\n"
    "v_mfma_f32_16x16x32_bf16 v[140:143], a[100:103], a[180:183], v[140:143]\n"
    "v_mfma_f32_16x16x32_bf16 v[136:139], a[104:107], a[180:183], v[136:139]\n"
    "s_waitcnt lgkmcnt(4)\n"
    "ds_read_b128 a[180:183], v15 offset:62464\n"
    "v_mfma_f32_16x16x32_bf16 v[140:143], a[104:107], a[184:187], v[140:143]\n"
    "ds_read_b128 a[200:203], v10 offset:62336\n"
    "v_mfma_f32_16x16x32_bf16 v[136:139], a[108:111], a[184:187], v[136:139]\n"
    "s_waitcnt lgkmcnt(5)\n"
    "ds_read_b128 a[184:187], v10 offset:62464\n"
    "v_mfma_f32_16x16x32_bf16 v[140:143], a[108:111], a[192:195], v[140:143]\n"
    "v_mfma_f32_16x16x32_bf16 v[136:139], a[112:115], a[192:195], v[136:139]\n"
    "s_waitcnt lgkmcnt(5)\n"
    "ds_read_b128 a[192:195], v10 offset:62592\n"
    "v_mfma_f32_16x16x32_bf16 v[140:143], a[112:115], a[196:199], v[140:143]\n"
    "v_mfma_f32_16x16x32_bf16 v[136:139], a[116:119], a[196:199], v[136:139]\n"
    "s_waitcnt lgkmcnt(5)\n"
    "ds_read_b128 a[196:199], v10 offset:62720\n"
    "v_mfma_f32_16x16x32_bf16 v[140:143], a[116:119], a[188:191], v[140:143]\n"
    "v_mfma_f32_16x16x32_bf16 v[136:139], a[120:123], a[188:191], v[136:139]\n"
    "s_waitcnt lgkmcnt(5)\n"
    "ds_read_b128 a[188:191], v10 offset:62848\n"
    "v_mfma_f32_16x16x32_bf16 v[140:143], a[120:123], a[176:179], v[140:143]\n"
    "v_mfma_f32_16x16x32_bf16 v[136:139], a[124:127], a[176:179], v[136:139]\n"
    "s_waitcnt lgkmcnt(5)\n"
    "ds_read_b128 a[176:179], v10 offset:62976\n"
    "v_mfma_f32_16x16x32_bf16 v[140:143], a[124:127], a[180:183], v[140:143]\n"
    "s_waitcnt lgkmcnt(5)\n"
    "v_mfma_f32_16x16x32_bf16 v[128:131], a[4:7], a[200:203], v[64:67]\n"
    "s_waitcnt lgkmcnt(4)\n"
    "ds_read_b128 a[180:183], v10 offset:63104\n"
    "v_mfma_f32_16x16x32_bf16 v[132:135], a[4:7], a[184:187], v[68:71]\n"
    "v_mfma_f32_16x16x32_bf16 v[128:131], a[8:11], a[184:187], v[128:131]\n"
    "s_waitcnt lgkmcnt(4)\n"
    "ds_read_b128 a[184:187], v11 offset:62208\n"
    "v_mfma_f32_16x16x32_bf16 v[132:135], a[8:11], a[192:195], v[132:135]\n"
    "v_cvt_pk_bf16_f32 v224, v136, v137\n"
    "v_cvt_pk_bf16_f32 v225, v138, v139\n"
    "v_mfma_f32_16x16x32_bf16 v[128:131], a[12:15], a[192:195], v[128:131]\n"
    "s_waitcnt lgkmcnt(4)\n"
    "ds_read_b128 a[192:195], v11 offset:62336\n"
    "v_cvt_pk_bf16_f32 v226, v140, v141\n"
    "v_mfma_f32_16x16x32_bf16 v[132:135], a[12:15], a[196:199], v[132:135]\n"
    "v_cvt_pk_bf16_f32 v227, v142, v143\n"
    "v_mfma_f32_16x16x32_bf16 v[128:131], a[16:19], a[196:199], v[128:131]\n"
    "s_waitcnt lgkmcnt(4)\n"
    "s_xor_b32 s24, s39, 32768\n"
    "ds_read_b128 a[196:199], v11 offset:62464\n"
    "v_mfma_f32_16x16x32_bf16 v[132:135], a[16:19], a[188:191], v[132:135]\n"
    "v_min_u32 v224, -2, v224\n"
    "v_min_u32 v225, -2, v225\n"
    "v_mfma_f32_16x16x32_bf16 v[128:131], a[20:23], a[188:191], v[128:131]\n"
    "s_waitcnt lgkmcnt(4)\n"
    "ds_read_b128 a[188:191], v11 offset:62592\n"
    "v_min_u32 v226, -2, v226\n"
    "v_mfma_f32_16x16x32_bf16 v[132:135], a[20:23], a[176:179], v[132:135]\n"
    "v_min_u32 v227, -2, v227\n"
    "v_mfma_f32_16x16x32_bf16 v[128:131], a[24:27], a[176:179], v[128:131]\n"
    "s_waitcnt lgkmcnt(4)\n"
    "ds_read_b128 a[176:179], v11 offset:62720\n"
    "v_mfma_f32_16x16x32_bf16 v[132:135], a[24:27], a[180:183], v[132:135]\n"
    "buffer_store_dwordx4 v[224:227], v22, s[16:19], s24 offen sc1\n"
    "v_mfma_f32_16x16x32_bf16 v[128:131], a[28:31], a[180:183], v[128:131]\n"
    "s_waitcnt lgkmcnt(4)\n"
    "ds_read_b128 a[180:183], v11 offset:62848\n"
    "v_mfma_f32_16x16x32_bf16 v[132:135], a[28:31], a[184:187], v[132:135]\n"
    "v_mfma_f32_16x16x32_bf16 v[128:131], a[32:35], a[184:187], v[128:131]\n"
    "s_waitcnt lgkmcnt(4)\n"
    "ds_read_b128 a[184:187], v11 offset:62976\n"
    "v_mfma_f32_16x16x32_bf16 v[132:135], a[32:35], a[192:195], v[132:135]\n"
    "v_mfma_f32_16x16x32_bf16 v[128:131], a[36:39], a[192:195], v[128:131]\n"
    "s_waitcnt lgkmcnt(4)\n"
    "ds_read_b128 a[192:195], v11 offset:63104\n"
    "v_mfma_f32_16x16x32_bf16 v[132:135], a[36:39], a[196:199], v[132:135]\n"
    "v_mfma_f32_16x16x32_bf16 v[128:131], a[40:43], a[196:199], v[128:131]\n"
    "s_waitcnt lgkmcnt(4)\n"
    "ds_read_b128 a[196:199], v12 offset:62208\n"
    "v_mfma_f32_16x16x32_bf16 v[132:135], a[40:43], a[188:191], v[132:135]\n"
    "ds_read_b128 a[200:203], v13 offset:62336\n"
    "v_mfma_f32_16x16x32_bf16 v[128:131], a[44:47], a[188:191], v[128:131]\n"
    "s_waitcnt lgkmcnt(5)\n"
    "s_waitcnt vmcnt(3)\n"
    "ds_read_b128 a[188:191], v13 offset:62464\n"
    "v_mfma_f32_16x16x32_bf16 v[132:135], a[44:47], a[176:179], v[132:135]\n"
    "v_max3_u32 v240, v160, v161, v162\n"
    "v_max3_u32 v240, v240, v163, v164\n"
    "v_mfma_f32_16x16x32_bf16 v[128:131], a[48:51], a[176:179], v[128:131]\n"
    "s_waitcnt lgkmcnt(5)\n"
    "ds_read_b128 a[176:179], v13 offset:62592\n"
    "v_max3_u32 v240, v240, v165, v166\n"
    "v_mfma_f32_16x16x32_bf16 v[132:135], a[48:51], a[180:183], v[132:135]\n"
    "buffer_load_dwordx4 v[176:179], v22, s[16:19], s26 offen sc1\n"
    "v_mfma_f32_16x16x32_bf16 v[128:131], a[52:55], a[180:183], v[128:131]\n"
    "s_waitcnt lgkmcnt(5)\n"
    "ds_read_b128 a[180:183], v13 offset:62720\n"
    "v_max3_u32 v240, v240, v167, v168\n"
    "v_mfma_f32_16x16x32_bf16 v[132:135], a[52:55], a[184:187], v[132:135]\n"
    "v_max3_u32 v240, v240, v169, v170\n"
    "v_max3_u32 v240, v240, v171, v172\n"
    "v_mfma_f32_16x16x32_bf16 v[128:131], a[56:59], a[184:187], v[128:131]\n"
    "s_waitcnt lgkmcnt(5)\n"
    "ds_read_b128 a[184:187], v13 offset:62848\n"
    "v_max3_u32 v240, v240, v173, v174\n"
    "v_mfma_f32_16x16x32_bf16 v[132:135], a[56:59], a[192:195], v[132:135]\n"
    "buffer_load_dwordx4 v[180:183], v22, s[16:19], s27 offen sc1\n"
    "v_mfma_f32_16x16x32_bf16 v[128:131], a[60:63], a[192:195], v[128:131]\n"
    "s_waitcnt lgkmcnt(5)\n"
    "ds_read_b128 a[192:195], v13 offset:62976\n"
    "v_max_u32 v240, v240, v175\n"
    "v_mfma_f32_16x16x32_bf16 v[132:135], a[60:63], a[196:199], v[132:135]\n"
    "s_waitcnt lgkmcnt(5)\n"
    "v_cmp_eq_u32 vcc, -1, v240\n"
    "s_cbranch_vccz LE0ok_%=\n"
    "s_mov_b32 s43, 0x100000\n"
    "LE0lp_%=:\n"
    "buffer_load_dwordx4 v[160:163], v22, s[16:19], s28 offen sc1\n"
    "buffer_load_dwordx4 v[164:167], v22, s[16:19], s30 offen sc1\n"
    "buffer_load_dwordx4 v[168:171], v22, s[16:19], s29 offen sc1\n"
    "buffer_load_dwordx4 v[172:175], v22, s[16:19], s31 offen sc1\n"
    "s_waitcnt vmcnt(0)\n"
    "s_sub_u32 s43, s43, 1\n"
    "s_cbranch_scc1 LE0ok_%=\n"
    "v_max3_u32 v240, v160, v161, v162\n"
    "v_max3_u32 v240, v240, v163, v164\n"
    "v_max3_u32 v240, v240, v165, v166\n"
    "v_max3_u32 v240, v240, v167, v168\n"
    "v_max3_u32 v240, v240, v169, v170\n"
    "v_max3_u32 v240, v240, v171, v172\n"
    "v_max3_u32 v240, v240, v173, v174\n"
    "v_max_u32 v240, v240, v175\n"
    "v_cmp_eq_u32 vcc, -1, v240\n"
    "s_cbranch_vccnz LE0lp_%=\n"
    "LE0ok_%=:\n"
    "v_mfma_f32_16x16x32_bf16 v[128:131], a[68:71], a[200:203], v[128:131]\n"
    "s_waitcnt lgkmcnt(4)\n"
    "ds_read_b128 a[196:199], v13 offset:63104\n"
    "v_lshlrev_b32 v188, 16, v160\n"
    "v_mfma_f32_16x16x32_bf16 v[132:135], a[68:71], a[188:191], v[132:135]\n"
    "v_and_b32 v189, s48, v160\n"
    "v_lshlrev_b32 v190, 16, v161\n"
    "v_mfma_f32_16x16x32_bf16 v[128:131], a[72:75], a[188:191], v[128:131]\n"
    "s_waitcnt lgkmcnt(4)\n"
    "ds_read_b128 a[188:191], v14 offset:62208\n"
    "v_and_b32 v191, s48, v161\n"
    "v_mfma_f32_16x16x32_bf16 v[132:135], a[72:75], a[176:179], v[132:135]\n"
    "v_lshlrev_b32 v192, 16, v162\n"
    "v_and_b32 v193, s48, v162\n"
    "v_mfma_f32_16x16x32_bf16 v[128:131], a[76:79], a[176:179], v[128:131]\n"
    "s_waitcnt lgkmcnt(4)\n"
    "ds_read_b128 a[176:179], v14 offset:62336\n"
    "v_lshlrev_b32 v194, 16, v163\n"
    "v_mfma_f32_16x16x32_bf16 v[132:135], a[76:79], a[180:183], v[132:135]\n"
    "v_and_b32 v195, s48, v163\n"
    "v_lshlrev_b32 v240, 16, v164\n"
    "v_mfma_f32_16x16x32_bf16 v[128:131], a[80:83], a[180:183], v[128:131]\n"
    "s_waitcnt lgkmcnt(4)\n"
    "ds_read_b128 a[180:183], v14 offset:62464\n"
    "v_and_b32 v241, s48, v164\n"
    "v_mfma_f32_16x16x32_bf16 v[132:135], a[80:83], a[184:187], v[132:135]\n"
    "v_lshlrev_b32 v242, 16, v165\n"
    "v_and_b32 v243, s48, v165\n"
    "v_mfma_f32_16x16x32_bf16 v[128:131], a[84:87], a[184:187], v[128:131]\n"
    "s_waitcnt lgkmcnt(4)\n"
    "ds_read_b128 a[184:187], v14 offset:62592\n"
    "v_lshlrev_b32 v244, 16, v166\n"
    "v_mfma_f32_16x16x32_bf16 v[132:135], a[84:87], a[192:195], v[132:135]\n"
    "v_and_b32 v245, s48, v166\n"
    "v_lshlrev_b32 v246, 16, v167\n"
    "v_mfma_f32_16x16x32_bf16 v[128:131], a[88:91], a[192:195], v[128:131]\n"
    "s_waitcnt lgkmcnt(4)\n"
    "ds_read_b128 a[192:195], v14 offset:62720\n"
    "v_and_b32 v247, s48, v167\n"
    "v_mfma_f32_16x16x32_bf16 v[132:135], a[88:91], a[196:199], v[132:135]\n"
    "v_add_f32 v188, v188, v240\n"
    "v_add_f32 v189, v189, v241\n"
    "v_mfma_f32_16x16x32_bf16 v[128:131], a[92:95], a[196:199], v[128:131]\n"
    "s_waitcnt lgkmcnt(4)\n"
    "ds_read_b128 a[196:199], v14 offset:62848\n"
    "v_add_f32 v190, v190, v242\n"
    "v_mfma_f32_16x16x32_bf16 v[132:135], a[92:95], a[188:191], v[132:135]\n"
    "v_add_f32 v191, v191, v243\n"
    "v_add_f32 v192, v192, v244\n"
    "v_mfma_f32_16x16x32_bf16 v[128:131], a[96:99], a[188:191], v[128:131]\n"
    "s_waitcnt lgkmcnt(4)\n"
    "ds_read_b128 a[188:191], v14 offset:62976\n"
    "v_add_f32 v193, v193, v245\n"
    "v_mfma_f32_16x16x32_bf16 v[132:135], a[96:99], a[176:179], v[132:135]\n"
    "v_add_f32 v194, v194, v246\n"
    "v_add_f32 v195, v195, v247\n"
    "v_mfma_f32_16x16x32_bf16 v[128:131], a[100:103], a[176:179], v[128:131]\n"
    "s_waitcnt lgkmcnt(4)\n"
    "ds_read_b128 a[176:179], v14 offset:63104\n"
    "v_lshlrev_b32 v40, 16, v168\n"
    "v_mfma_f32_16x16x32_bf16 v[132:135], a[100:103], a[180:183], v[132:135]\n"
    "v_and_b32 v41, s48, v168\n"
    "v_lshlrev_b32 v42, 16, v169\n"
    "v_mfma_f32_16x16x32_bf16 v[128:131], a[104:107], a[180:183], v[128:131]\n"
    "s_waitcnt lgkmcnt(4)\n"
    "ds_read_b128 a[180:183], v15 offset:62208\n"
    "v_and_b32 v43, s48, v169\n"
    "v_mfma_f32_16x16x32_bf16 v[132:135], a[104:107], a[184:187], v[132:135]\n"
    "v_lshlrev_b32 v44, 16, v170\n"
    "v_and_b32 v45, s48, v170\n"
    "v_mfma_f32_16x16x32_bf16 v[128:131], a[108:111], a[184:187], v[128:131]\n"
    "s_waitcnt lgkmcnt(4)\n"
    "buffer_load_dwordx4 v[184:187], v22, s[16:19], s25 offen sc1\n"
    "v_mfma_f32_16x16x32_bf16 v[132:135], a[108:111], a[192:195], v[132:135]\n"
    "v_lshlrev_b32 v46, 16, v171\n"
    "v_and_b32 v47, s48, v171\n"
    "v_mfma_f32_16x16x32_bf16 v[128:131], a[112:115], a[192:195], v[128:131]\n"
    "s_waitcnt lgkmcnt(3)\n"
    "v_lshlrev_b32 v240, 16, v172\n"
    "v_and_b32 v241, s48, v172\n"
    "v_mfma_f32_16x16x32_bf16 v[132:135], a[112:115], a[196:199], v[132:135]\n"
    "v_lshlrev_b32 v242, 16, v173\n"
    "v_and_b32 v243, s48, v173\n"
    "v_mfma_f32_16x16x32_bf16 v[128:131], a[116:119], a[196:199], v[128:131]\n"
    "s_waitcnt lgkmcnt(2)\n"
    "v_lshlrev_b32 v244, 16, v174\n"
    "v_and_b32 v245, s48, v174\n"
    "v_mfma_f32_16x16x32_bf16 v[132:135], a[116:119], a[188:191], v[132:135]\n"
    "v_lshlrev_b32 v246, 16, v175\n"
    "v_and_b32 v247, s48, v175\n"
    "v_mfma_f32_16x16x32_bf16 v[128:131], a[120:123], a[188:191], v[128:131]\n"
    "s_waitcnt lgkmcnt(1)\n"
    "v_add_f32 v40, v40, v240\n"
    "v_add_f32 v41, v41, v241\n"
    "v_mfma_f32_16x16x32_bf16 v[132:135], a[120:123], a[176:179], v[132:135]\n"
    "v_add_f32 v42, v42, v242\n"
    "v_add_f32 v43, v43, v243\n"
    "v_mfma_f32_16x16x32_bf16 v[128:131], a[124:127], a[176:179], v[128:131]\n"
    "s_waitcnt lgkmcnt(0)\n"
    "v_add_f32 v44, v44, v244\n"
    "v_add_f32 v45, v45, v245\n"
    "v_mfma_f32_16x16x32_bf16 v[132:135], a[124:127], a[180:183], v[132:135]\n"
    "v_add_f32 v46, v46, v246\n"
    "v_add_f32 v47, v47, v247\n"
    "s_waitcnt vmcnt(1)\n"
    "v_max3_u32 v240, v176, v177, v178\n"
    "v_max3_u32 v240, v240, v179, v180\n"
    "v_max3_u32 v240, v240, v181, v182\n"
    "v_max_u32 v240, v240, v183\n"
    "v_cmp_eq_u32 vcc, -1, v240\n"
    "s_cbranch_vccz LM0ok_%=\n"
    "s_mov_b32 s43, 0x100000\n"
    "LM0lp_%=:\n"
    "buffer_load_dwordx4 v[176:179], v22, s[16:19], s26 offen sc1\n"
    "buffer_load_dwordx4 v[180:183], v22, s[16:19], s27 offen sc1\n"
    "s_waitcnt vmcnt(0)\n"
    "s_sub_u32 s43, s43, 1\n"
    "s_cbranch_scc1 LM0ok_%=\n"
    "v_max3_u32 v240, v176, v177, v178\n"
    "v_max3_u32 v240, v240, v179, v180\n"
    "v_max3_u32 v240, v240, v181, v182\n"
    "v_max_u32 v240, v240, v183\n"
    "v_cmp_eq_u32 vcc, -1, v240\n"
    "s_cbranch_vccnz LM0lp_%=\n"
    "LM0ok_%=:\n"
    "v_mfma_f32_16x16x32_bf16 v[128:131], v[236:239], v[176:179], v[128:131]\n"
    "v_mfma_f32_16x16x32_bf16 v[132:135], v[234:237], v[176:179], v[132:135]\n"
    "v_mfma_f32_16x16x32_bf16 v[40:43], v[236:239], v[180:183], v[40:43]\n"
    "v_mfma_f32_16x16x32_bf16 v[44:47], v[234:237], v[180:183], v[44:47]\n"
    "s_nop 4\n"
    "v_pk_add_f32 v[128:129], v[128:129], v[188:189]\n"
    "v_pk_add_f32 v[130:131], v[130:131], v[190:191]\n"
    "v_pk_add_f32 v[132:133], v[132:133], v[192:193]\n"
    "v_pk_add_f32 v[134:135], v[134:135], v[194:195]\n"
    "v_pk_mul_f32 v[240:241], v[128:129], s[46:47] op_sel_hi:[1,0]\n"
    "v_pk_mul_f32 v[242:243], v[130:131], s[46:47] op_sel_hi:[1,0]\n"
    "v_pk_mul_f32 v[244:245], v[132:133], s[46:47] op_sel_hi:[1,0]\n"
    "v_pk_mul_f32 v[246:247], v[134:135], s[46:47] op_sel_hi:[1,0]\n"
    "v_exp_f32 v240, -|v240|\n"
    "v_fma_f32 v248, -v128, v128, |v128|\n"
    "v_exp_f32 v241, -|v241|\n"
    "v_fma_f32 v249, -v129, v129, |v129|\n"
    "v_exp_f32 v242, -|v242|\n"
    "v_fma_f32 v250, -v130, v130, |v130|\n"
    "v_exp_f32 v243, -|v243|\n"
    "v_fma_f32 v251, -v131, v131, |v131|\n"
    "v_exp_f32 v244, -|v244|\n"
    "v_fma_f32 v252, -v132, v132, |v132|\n"
    "v_exp_f32 v245, -|v245|\n"
    "v_fma_f32 v253, -v133, v133, |v133|\n"
    "v_exp_f32 v246, -|v246|\n"
    "v_fma_f32 v254, -v134, v134, |v134|\n"
    "v_exp_f32 v247, -|v247|\n"
    "v_fma_f32 v255, -v135, v135, |v135|\n"
    "v_pk_fma_f32 v[168:169], v[240:241], 0.5, 0.5 op_sel_hi:[1,0,0] neg_lo:[1,0,0] neg_hi:[1,0,0]\n"
    "v_pk_fma_f32 v[170:171], v[242:243], 0.5, 0.5 op_sel_hi:[1,0,0] neg_lo:[1,0,0] neg_hi:[1,0,0]\n"
    "v_pk_fma_f32 v[172:173], v[244:245], 0.5, 0.5 op_sel_hi:[1,0,0] neg_lo:[1,0,0] neg_hi:[1,0,0]\n"
    "v_pk_fma_f32 v[174:175], v[246:247], 0.5, 0.5 op_sel_hi:[1,0,0] neg_lo:[1,0,0] neg_hi:[1,0,0]\n"
    "v_pk_fma_f32 v[240:241], v[240:241], 0.5, 0.5 op_sel_hi:[1,0,0]\n"
    "v_pk_fma_f32 v[242:243], v[242:243], 0.5, 0.5 op_sel_hi:[1,0,0]\n"
    "v_pk_fma_f32 v[244:245], v[244:245], 0.5, 0.5 op_sel_hi:[1,0,0]\n"
    "v_pk_fma_f32 v[246:247], v[246:247], 0.5, 0.5 op_sel_hi:[1,0,0]\n"
    "v_med3_f32 v248, v168, v248, |v128|\n"
    "v_med3_f32 v249, v169, v249, |v129|\n"
    "v_med3_f32 v250, v170, v250, |v130|\n"
    "v_med3_f32 v251, v171, v251, |v131|\n"
    "v_med3_f32 v252, v172, v252, |v132|\n"
    "v_med3_f32 v253, v173, v253, |v133|\n"
    "v_med3_f32 v254, v174, v254, |v134|\n"
    "v_med3_f32 v255, v175, v255, |v135|\n"
    "v_bfi_b32 v248, s47, v248, v128\n"
    "v_bfi_b32 v249, s47, v249, v129\n"
    "v_bfi_b32 v250, s47, v250, v130\n"
    "v_bfi_b32 v251, s47, v251, v131\n"
    "v_bfi_b32 v252, s47, v252, v132\n"
    "v_bfi_b32 v253, s47, v253, v133\n"
    "v_bfi_b32 v254, s47, v254, v134\n"
    "v_bfi_b32 v255, s47, v255, v135\n"
    "s_waitcnt vmcnt(0)\n"
    "v_max3_u32 v188, v184, v185, v186\n"
    "v_max_u32 v188, v188, v187\n"
    "v_cmp_eq_u32 vcc, -1, v188\n"
    "s_cbranch_vccz LL0ok_%=\n"
    "s_mov_b32 s43, 0x100000\n"
    "LL0lp_%=:\n"
    "buffer_load_dwordx4 v[184:187], v22, s[16:19], s25 offen sc1\n"
    "s_waitcnt vmcnt(0)\n"
    "s_sub_u32 s43, s43, 1\n"
    "s_cbranch_scc1 LL0ok_%=\n"
    "v_max3_u32 v188, v184, v185, v186\n"
    "v_max_u32 v188, v188, v187\n"
    "v_cmp_eq_u32 vcc, -1, v188\n"
    "s_cbranch_vccnz LL0lp_%=\n"
    "LL0ok_%=:\n"
    "v_lshlrev_b32 v188, 16, v184\n"
    "v_and_b32 v189, s48, v184\n"
    "v_lshlrev_b32 v190, 16, v185\n"
    "v_and_b32 v191, s48, v185\n"
    "v_lshlrev_b32 v192, 16, v186\n"
    "v_and_b32 v193, s48, v186\n"
    "v_lshlrev_b32 v194, 16, v187\n"
    "v_and_b32 v195, s48, v187\n"
    "v_pk_add_f32 v[40:41], v[40:41], v[188:189]\n"
    "v_pk_add_f32 v[42:43], v[42:43], v[190:191]\n"
    "v_pk_add_f32 v[44:45], v[44:45], v[192:193]\n"
    "v_pk_add_f32 v[46:47], v[46:47], v[194:195]\n"
    "v_pk_mul_f32 v[200:201], v[40:41], s[44:45] op_sel:[0,1] op_sel_hi:[1,1]\n"
    "v_pk_mul_f32 v[202:203], v[42:43], s[44:45] op_sel:[0,1] op_sel_hi:[1,1]\n"
    "v_pk_mul_f32 v[204:205], v[44:45], s[44:45] op_sel:[0,1] op_sel_hi:[1,1]\n"
    "v_pk_mul_f32 v[206:207], v[46:47], s[44:45] op_sel:[0,1] op_sel_hi:[1,1]\n"
    "v_exp_f32 v200, v200\n"
    "v_exp_f32 v201, v201\n"
    "v_exp_f32 v202, v202\n"
    "v_exp_f32 v203, v203\n"
    "v_exp_f32 v204, v204\n"
    "v_exp_f32 v205, v205\n"
    "v_exp_f32 v206, v206\n"
    "v_exp_f32 v207, v207\n"
    "v_pk_add_f32 v[200:201], v[200:201], 1.0 op_sel_hi:[1,0]\n"
    "v_pk_add_f32 v[202:203], v[202:203], 1.0 op_sel_hi:[1,0]\n"
    "v_pk_add_f32 v[204:205], v[204:205], 1.0 op_sel_hi:[1,0]\n"
    "v_pk_add_f32 v[206:207], v[206:207], 1.0 op_sel_hi:[1,0]\n"
    "v_pk_mul_f32 v[240:241], v[240:241], v[200:201]\n"
    "v_pk_mul_f32 v[242:243], v[242:243], v[202:203]\n"
    "v_pk_mul_f32 v[244:245], v[244:245], v[204:205]\n"
    "v_pk_mul_f32 v[246:247], v[246:247], v[206:207]\n"
    "v_rcp_f32 v240, v240\n"
    "v_rcp_f32 v241, v241\n"
    "v_rcp_f32 v242, v242\n"
    "v_rcp_f32 v243, v243\n"
    "v_rcp_f32 v244, v244\n"
    "v_rcp_f32 v245, v245\n"
    "v_rcp_f32 v246, v246\n"
    "v_rcp_f32 v247, v247\n"
    "v_pk_mul_f32 v[248:249], v[248:249], v[240:241]\n"
    "v_pk_mul_f32 v[250:251], v[250:251], v[242:243]\n"
    "v_pk_mul_f32 v[252:253], v[252:253], v[244:245]\n"
    "v_pk_mul_f32 v[254:255], v[254:255], v[246:247]\n"
    "v_cvt_pk_bf16_f32 v240, v248, v249\n"
    "v_cvt_pk_bf16_f32 v241, v250, v251\n"
    "v_cvt_pk_bf16_f32 v242, v252, v253\n"
    "v_cvt_pk_bf16_f32 v243, v254, v255\n"
    "buffer_store_dwordx2 v[240:241], v25, s[20:23], 0 offen\n"
    "buffer_store_dwordx2 v[242:243], v25, s[20:23], 0 offen offset:2048\n"
    "s_endpgm\n"
    ".fill (LV0_%=+6144-.)/4, 4, 0xbf800000\n"
    "LV1_%=:\n"
    "s_nop 3\n"
    "v_add_f32 v80, v80, v168\n"
    "v_add_f32 v81, v81, v169\n"
    "v_mfma_f32_16x16x32_bf16 v[104:107], a[130:133], a[232:235], v[104:107]\n"
    "v_add_f32 v82, v82, v170\n"
    "v_add_f32 v83, v83, v171\n"
    "v_mfma_f32_16x16x32_bf16 v[108:111], a[130:133], a[212:215], v[108:111]\n"
    "v_add_f32 v84, v84, v168\n"
    "v_add_f32 v85, v85, v169\n"
    "v_mfma_f32_16x16x32_bf16 v[112:115], a[130:133], a[192:195], v[112:115]\n"
    "ds_read_b128 a[176:179], v15 offset:21504\n"
    "v_add_f32 v86, v86, v170\n"
    "v_mfma_f32_16x16x32_bf16 v[116:119], a[130:133], a[188:191], v[116:119]\n"
    "s_barrier\n"
    "ds_read_b128 a[184:187], v10 offset:62848\n"
    "v_add_f32 v87, v87, v171\n"
    "v_mfma_f32_16x16x32_bf16 v[120:123], a[130:133], a[196:199], v[120:123]\n"
    "s_waitcnt lgkmcnt(2)\n"
    "ds_read_b128 a[200:203], v10 offset:62976\n"
    "v_add_f32 v88, v88, v172\n"
    "v_mfma_f32_16x16x32_bf16 v[124:127], a[130:133], a[180:183], v[124:127]\n"
    "ds_read_b128 a[204:207], v10 offset:63104\n"
    "v_add_f32 v89, v89, v173\n"
    "v_mfma_f32_16x16x32_bf16 v[96:99], a[138:141], a[228:231], v[96:99]\n"
    "ds_read_b128 a[208:211], v11 offset:62208\n"
    "v_add_f32 v90, v90, v174\n"
    "v_mfma_f32_16x16x32_bf16 v[100:103], a[138:141], a[232:235], v[100:103]\n"
    "v_add_f32 v91, v91, v175\n"
    "v_add_f32 v92, v92, v172\n"
    "v_mfma_f32_16x16x32_bf16 v[104:107], a[138:141], a[212:215], v[104:107]\n"
    "v_add_f32 v93, v93, v173\n"
    "v_add_f32 v94, v94, v174\n"
    "v_mfma_f32_16x16x32_bf16 v[108:111], a[138:141], a[192:195], v[108:111]\n"
    "v_add_f32 v95, v95, v175\n"
    "v_add_f32 v64, v64, v160\n"
    "v_mfma_f32_16x16x32_bf16 v[112:115], a[138:141], a[188:191], v[112:115]\n"
    "ds_read_b128 a[188:191], v11 offset:62336\n"
    "v_add_f32 v65, v65, v161\n"
    "v_mfma_f32_16x16x32_bf16 v[116:119], a[138:141], a[196:199], v[116:119]\n"
    "v_add_f32 v66, v66, v162\n"
    "v_add_f32 v67, v67, v163\n"
    "v_mfma_f32_16x16x32_bf16 v[120:123], a[138:141], a[180:183], v[120:123]\n"
    "s_waitcnt lgkmcnt(5)\n"
    "v_add_f32 v68, v68, v160\n"
    "v_add_f32 v69, v69, v161\n"
    "v_mfma_f32_16x16x32_bf16 v[124:127], a[138:141], a[176:179], v[124:127]\n"
    "s_waitcnt lgkmcnt(4)\n"
    "v_add_f32 v70, v70, v162\n"
    "v_add_f32 v71, v71, v163\n"
    "v_mfma_f32_16x16x32_bf16 v[144:147], a[4:7], a[184:187], v[80:83]\n"
    "s_waitcnt lgkmcnt(3)\n"
    "ds_read_b128 a[176:179], v11 offset:62464\n"
    "v_add_f32 v72, v72, v164\n"
    "v_mfma_f32_16x16x32_bf16 v[148:151], a[4:7], a[200:203], v[84:87]\n"
    "s_waitcnt lgkmcnt(3)\n"
    "v_add_f32 v73, v73, v165\n"
    "v_add_f32 v74, v74, v166\n"
    "v_mfma_f32_16x16x32_bf16 v[152:155], a[4:7], a[204:207], v[88:91]\n"
    "s_waitcnt lgkmcnt(2)\n"
    "v_add_f32 v75, v75, v167\n"
    "v_add_f32 v76, v76, v164\n"
    "v_mfma_f32_16x16x32_bf16 v[156:159], a[4:7], a[208:211], v[92:95]\n"
    "v_add_f32 v77, v77, v165\n"
    "v_add_f32 v78, v78, v166\n"
    "v_mfma_f32_16x16x32_bf16 v[144:147], a[8:11], a[200:203], v[144:147]\n"
    "ds_read_b128 a[180:183], v11 offset:62592\n"
    "v_add_f32 v79, v79, v167\n"
    "v_mfma_f32_16x16x32_bf16 v[148:151], a[8:11], a[204:207], v[148:151]\n"
    "v_add_f32 v104, v104, v72\n"
    "v_add_f32 v105, v105, v73\n"
    "v_mfma_f32_16x16x32_bf16 v[152:155], a[8:11], a[208:211], v[152:155]\n"
    "s_waitcnt lgkmcnt(2)\n"
    "v_add_f32 v106, v106, v74\n"
    "v_add_f32 v107, v107, v75\n"
    "v_mfma_f32_16x16x32_bf16 v[156:159], a[8:11], a[188:191], v[156:159]\n"
    "v_add_f32 v108, v108, v76\n"
    "v_add_f32 v109, v109, v77\n"
    "v_mfma_f32_16x16x32_bf16 v[144:147], a[12:15], a[204:207], v[144:147]\n"
    "ds_read_b128 a[184:187], v11 offset:62720\n"
    "v_add_f32 v110, v110, v78\n"
    "v_mfma_f32_16x16x32_bf16 v[148:151], a[12:15], a[208:211], v[148:151]\n"
    "v_add_f32 v111, v111, v79\n"
    "v_cvt_pk_bf16_f32 v200, v104, v105\n"
    "v_mfma_f32_16x16x32_bf16 v[152:155], a[12:15], a[188:191], v[152:155]\n"
    "s_waitcnt lgkmcnt(2)\n"
    "v_cvt_pk_bf16_f32 v201, v106, v107\n"
    "v_cvt_pk_bf16_f32 v202, v108, v109\n"
    "v_mfma_f32_16x16x32_bf16 v[156:159], a[12:15], a[176:179], v[156:159]\n"
    "v_cvt_pk_bf16_f32 v203, v110, v111\n"
    "v_mfma_f32_16x16x32_bf16 v[144:147], a[16:19], a[208:211], v[144:147]\n"
    "s_xor_b32 s36, s39, 131072\n"
    "ds_read_b128 a[192:195], v11 offset:62848\n"
    "v_min_u32 v200, -2, v200\n"
    "v_mfma_f32_16x16x32_bf16 v[148:151], a[16:19], a[188:191], v[148:151]\n"
    "v_min_u32 v201, -2, v201\n"
    "v_min_u32 v202, -2, v202\n"
    "v_mfma_f32_16x16x32_bf16 v[152:155], a[16:19], a[176:179], v[152:155]\n"
    "s_waitcnt lgkmcnt(2)\n"
    "v_min_u32 v203, -2, v203\n"
    "v_mfma_f32_16x16x32_bf16 v[156:159], a[16:19], a[180:183], v[156:159]\n"
    "s_waitcnt vmcnt(0)\n"
    "v_cmp_ne_u64 vcc, v[28:29], v[26:27]\n"
    "s_cbranch_vccz LF1ok_%=\n"
    "s_mov_b32 s43, 0x100000\n"
    "LF1lp_%=:\n"
    "buffer_load_dwordx2 v[28:29], v23, s[16:19], s40 offen offset:0 sc1\n"
    "s_waitcnt vmcnt(0)\n"
    "s_sub_u32 s43, s43, 1\n"
    "s_cbranch_scc1 LF1ok_%=\n"
    "v_cmp_ne_u64 vcc, v[28:29], v[26:27]\n"
    "s_cbranch_vccnz LF1lp_%=\n"
    "LF1ok_%=:\n"
    "v_mfma_f32_16x16x32_bf16 v[144:147], a[20:23], a[188:191], v[144:147]\n"
    "ds_read_b128 a[188:191], v11 offset:62976\n"
    "v_mfma_f32_16x16x32_bf16 v[148:151], a[20:23], a[176:179], v[148:151]\n"
    "buffer_store_dwordx4 v[200:203], v22, s[16:19], s36 offen sc1\n"
    "v_mfma_f32_16x16x32_bf16 v[152:155], a[20:23], a[180:183], v[152:155]\n"
    "s_waitcnt lgkmcnt(2)\n"
    "v_add_f32 v96, v96, v64\n"
    "v_add_f32 v97, v97, v65\n"
    "v_mfma_f32_16x16x32_bf16 v[156:159], a[20:23], a[184:187], v[156:159]\n"
    "v_add_f32 v98, v98, v66\n"
    "v_add_f32 v99, v99, v67\n"
    "v_mfma_f32_16x16x32_bf16 v[144:147], a[24:27], a[176:179], v[144:147]\n"
    "ds_read_b128 a[176:179], v11 offset:63104\n"
    "v_add_f32 v100, v100, v68\n"
    "v_mfma_f32_16x16x32_bf16 v[148:151], a[24:27], a[180:183], v[148:151]\n"
    "v_add_f32 v101, v101, v69\n"
    "v_add_f32 v102, v102, v70\n"
    "v_mfma_f32_16x16x32_bf16 v[152:155], a[24:27], a[184:187], v[152:155]\n"
    "s_waitcnt lgkmcnt(2)\n"
    "v_add_f32 v103, v103, v71\n"
    "v_cvt_pk_bf16_f32 v204, v96, v97\n"
    "v_mfma_f32_16x16x32_bf16 v[156:159], a[24:27], a[192:195], v[156:159]\n"
    "v_cvt_pk_bf16_f32 v205, v98, v99\n"
    "v_cvt_pk_bf16_f32 v206, v100, v101\n"
    "v_mfma_f32_16x16x32_bf16 v[144:147], a[28:31], a[180:183], v[144:147]\n"
    "ds_read_b128 a[180:183], v12 offset:62208\n"
    "v_cvt_pk_bf16_f32 v207, v102, v103\n"
    "v_mfma_f32_16x16x32_bf16 v[148:151], a[28:31], a[184:187], v[148:151]\n"
    "s_xor_b32 s36, s39, 163840\n"
    "v_min_u32 v204, -2, v204\n"
    "v_min_u32 v205, -2, v205\n"
    "v_mfma_f32_16x16x32_bf16 v[152:155], a[28:31], a[192:195], v[152:155]\n"
    "s_waitcnt lgkmcnt(2)\n"
    "v_min_u32 v206, -2, v206\n"
    "v_min_u32 v207, -2, v207\n"
    "v_mfma_f32_16x16x32_bf16 v[156:159], a[28:31], a[188:191], v[156:159]\n"
    "buffer_store_dwordx4 v[204:207], v22, s[16:19], s36 offen sc1\n"
    "v_mfma_f32_16x16x32_bf16 v[144:147], a[32:35], a[184:187], v[144:147]\n"
    "ds_read_b128 a[184:187], v12 offset:62336\n"
    "v_add_f32 v120, v120, v88\n"
    "v_mfma_f32_16x16x32_bf16 v[148:151], a[32:35], a[192:195], v[148:151]\n"
    "v_add_f32 v121, v121, v89\n"
    "v_add_f32 v122, v122, v90\n"
    "v_mfma_f32_16x16x32_bf16 v[152:155], a[32:35], a[188:191], v[152:155]\n"
    "s_waitcnt lgkmcnt(2)\n"
    "v_add_f32 v123, v123, v91\n"
    "v_add_f32 v124, v124, v92\n"
    "v_mfma_f32_16x16x32_bf16 v[156:159], a[32:35], a[176:179], v[156:159]\n"
    "v_add_f32 v125, v125, v93\n"
    "v_add_f32 v126, v126, v94\n"
    "v_mfma_f32_16x16x32_bf16 v[144:147], a[36:39], a[192:195], v[144:147]\n"
    "ds_read_b128 a[192:195], v12 offset:62464\n"
    "v_add_f32 v127, v127, v95\n"
    "v_mfma_f32_16x16x32_bf16 v[148:151], a[36:39], a[188:191], v[148:151]\n"
    "v_cvt_pk_bf16_f32 v208, v120, v121\n"
    "v_cvt_pk_bf16_f32 v209, v122, v123\n"
    "v_mfma_f32_16x16x32_bf16 v[152:155], a[36:39], a[176:179], v[152:155]\n"
    "s_waitcnt lgkmcnt(2)\n"
    "v_cvt_pk_bf16_f32 v210, v124, v125\n"
    "v_cvt_pk_bf16_f32 v211, v126, v127\n"
    "v_mfma_f32_16x16x32_bf16 v[156:159], a[36:39], a[180:183], v[156:159]\n"
    "s_xor_b32 s36, s39, 196608\n"
    "v_min_u32 v208, -2, v208\n"
    "v_min_u32 v209, -2, v209\n"
    "v_mfma_f32_16x16x32_bf16 v[144:147], a[40:43], a[188:191], v[144:147]\n"
    "ds_read_b128 a[188:191], v12 offset:62592\n"
    "v_min_u32 v210, -2, v210\n"
    "v_mfma_f32_16x16x32_bf16 v[148:151], a[40:43], a[176:179], v[148:151]\n"
    "v_min_u32 v211, -2, v211\n"
    "v_mfma_f32_16x16x32_bf16 v[152:155], a[40:43], a[180:183], v[152:155]\n"
    "s_waitcnt lgkmcnt(2)\n"
    "buffer_store_dwordx4 v[208:211], v22, s[16:19], s36 offen sc1\n"
    "v_mfma_f32_16x16x32_bf16 v[156:159], a[40:43], a[184:187], v[156:159]\n"
    "v_add_f32 v112, v112, v80\n"
    "v_add_f32 v113, v113, v81\n"
    "v_mfma_f32_16x16x32_bf16 v[144:147], a[44:47], a[176:179], v[144:147]\n"
    "ds_read_b128 a[176:179], v12 offset:62720\n"
    "v_add_f32 v114, v114, v82\n"
    "v_mfma_f32_16x16x32_bf16 v[148:151], a[44:47], a[180:183], v[148:151]\n"
    "v_add_f32 v115, v115, v83\n"
    "v_add_f32 v116, v116, v84\n"
    "v_mfma_f32_16x16x32_bf16 v[152:155], a[44:47], a[184:187], v[152:155]\n"
    "s_waitcnt lgkmcnt(2)\n"
    "v_add_f32 v117, v117, v85\n"
    "v_add_f32 v118, v118, v86\n"
    "v_mfma_f32_16x16x32_bf16 v[156:159], a[44:47], a[192:195], v[156:159]\n"
    "v_add_f32 v119, v119, v87\n"
    "v_cvt_pk_bf16_f32 v212, v112, v113\n"
    "v_mfma_f32_16x16x32_bf16 v[144:147], a[48:51], a[180:183], v[144:147]\n"
    "ds_read_b128 a[180:183], v12 offset:62848\n"
    "v_cvt_pk_bf16_f32 v213, v114, v115\n"
    "v_mfma_f32_16x16x32_bf16 v[148:151], a[48:51], a[184:187], v[148:151]\n"
    "v_cvt_pk_bf16_f32 v214, v116, v117\n"
    "v_cvt_pk_bf16_f32 v215, v118, v119\n"
    "v_mfma_f32_16x16x32_bf16 v[152:155], a[48:51], a[192:195], v[152:155]\n"
    "s_waitcnt lgkmcnt(2)\n"
    "s_xor_b32 s36, s39, 229376\n"
    "v_min_u32 v212, -2, v212\n"
    "v_mfma_f32_16x16x32_bf16 v[156:159], a[48:51], a[188:191], v[156:159]\n"
    "v_min_u32 v213, -2, v213\n"
    "v_min_u32 v214, -2, v214\n"
    "v_mfma_f32_16x16x32_bf16 v[144:147], a[52:55], a[184:187], v[144:147]\n"
    "ds_read_b128 a[184:187], v12 offset:62976\n"
    "v_min_u32 v215, -2, v215\n"
    "v_mfma_f32_16x16x32_bf16 v[148:151], a[52:55], a[192:195], v[148:151]\n"
    "ds_read_b128 a[196:199], v13 offset:62848\n"
    "v_mfma_f32_16x16x32_bf16 v[152:155], a[52:55], a[188:191], v[152:155]\n"
    "s_waitcnt lgkmcnt(3)\n"
    "ds_read_b128 a[200:203], v13 offset:62976\n"
    "v_mfma_f32_16x16x32_bf16 v[156:159], a[52:55], a[176:179], v[156:159]\n"
    "ds_read_b128 a[204:207], v13 offset:63104\n"
    "v_mfma_f32_16x16x32_bf16 v[144:147], a[56:59], a[192:195], v[144:147]\n"
    "ds_read_b128 a[192:195], v14 offset:62208\n"
    "v_mfma_f32_16x16x32_bf16 v[148:151], a[56:59], a[188:191], v[148:151]\n"
    "buffer_store_dwordx4 v[212:215], v22, s[16:19], s36 offen sc1\n"
    "v_mfma_f32_16x16x32_bf16 v[152:155], a[56:59], a[176:179], v[152:155]\n"
    "s_waitcnt lgkmcnt(5)\n"
    "v_mfma_f32_16x16x32_bf16 v[156:159], a[56:59], a[180:183], v[156:159]\n"
    "v_mfma_f32_16x16x32_bf16 v[144:147], a[60:63], a[188:191], v[144:147]\n"
    "ds_read_b128 a[188:191], v14 offset:62336\n"
    "v_mfma_f32_16x16x32_bf16 v[148:151], a[60:63], a[176:179], v[148:151]\n"
    "v_mfma_f32_16x16x32_bf16 v[152:155], a[60:63], a[180:183], v[152:155]\n"
    "s_waitcnt lgkmcnt(5)\n"
    "v_mfma_f32_16x16x32_bf16 v[156:159], a[60:63], a[184:187], v[156:159]\n"
    "s_waitcnt lgkmcnt(4)\n"
    "v_mfma_f32_16x16x32_bf16 v[144:147], a[68:71], a[196:199], v[144:147]\n"
    "s_waitcnt lgkmcnt(3)\n"
    "ds_read_b128 a[176:179], v14 offset:62464\n"
    "v_mfma_f32_16x16x32_bf16 v[148:151], a[68:71], a[200:203], v[148:151]\n"
    "s_waitcnt lgkmcnt(3)\n"
    "v_mfma_f32_16x16x32_bf16 v[152:155], a[68:71], a[204:207], v[152:155]\n"
    "s_waitcnt lgkmcnt(2)\n"
    "v_mfma_f32_16x16x32_bf16 v[156:159], a[68:71], a[192:195], v[156:159]\n"
    "v_mfma_f32_16x16x32_bf16 v[144:147], a[72:75], a[200:203], v[144:147]\n"
    "ds_read_b128 a[180:183], v14 offset:62592\n"
    "v_mfma_f32_16x16x32_bf16 v[148:151], a[72:75], a[204:207], v[148:151]\n"
    "v_mfma_f32_16x16x32_bf16 v[152:155], a[72:75], a[192:195], v[152:155]\n"
    "s_waitcnt lgkmcnt(2)\n"
    "v_mfma_f32_16x16x32_bf16 v[156:159], a[72:75], a[188:191], v[156:159]\n"
    "v_mfma_f32_16x16x32_bf16 v[144:147], a[76:79], a[204:207], v[144:147]\n"
    "ds_read_b128 a[184:187], v14 offset:62720\n"
    "v_mfma_f32_16x16x32_bf16 v[148:151], a[76:79], a[192:195], v[148:151]\n"
    "v_mfma_f32_16x16x32_bf16 v[152:155], a[76:79], a[188:191], v[152:155]\n"
    "s_waitcnt lgkmcnt(2)\n"
    "v_mfma_f32_16x16x32_bf16 v[156:159], a[76:79], a[176:179], v[156:159]\n"
    "v_mfma_f32_16x16x32_bf16 v[144:147], a[80:83], a[192:195], v[144:147]\n"
    "ds_read_b128 a[192:195], v14 offset:62848\n"
    "v_mfma_f32_16x16x32_bf16 v[148:151], a[80:83], a[188:191], v[148:151]\n"
    "v_mfma_f32_16x16x32_bf16 v[152:155], a[80:83], a[176:179], v[152:155]\n"
    "s_waitcnt lgkmcnt(2)\n"
    "v_mfma_f32_16x16x32_bf16 v[156:159], a[80:83], a[180:183], v[156:159]\n"
    "v_mfma_f32_16x16x32_bf16 v[144:147], a[84:87], a[188:191], v[144:147]\n"
    "ds_read_b128 a[188:191], v14 offset:62976\n"
    "v_mfma_f32_16x16x32_bf16 v[148:151], a[84:87], a[176:179], v[148:151]\n"
    "v_mfma_f32_16x16x32_bf16 v[152:155], a[84:87], a[180:183], v[152:155]\n"
    "s_waitcnt lgkmcnt(2)\n"
    "v_mfma_f32_16x16x32_bf16 v[156:159], a[84:87], a[184:187], v[156:159]\n"
    "v_mfma_f32_16x16x32_bf16 v[144:147], a[88:91], a[176:179], v[144:147]\n"
    "ds_read_b128 a[176:179], v14 offset:63104\n"
    "v_mfma_f32_16x16x32_bf16 v[148:151], a[88:91], a[180:183], v[148:151]\n"
    "v_mfma_f32_16x16x32_bf16 v[152:155], a[88:91], a[184:187], v[152:155]\n"
    "s_waitcnt lgkmcnt(2)\n"
    "v_mfma_f32_16x16x32_bf16 v[156:159], a[88:91], a[192:195], v[156:159]\n"
    "v_mfma_f32_16x16x32_bf16 v[144:147], a[92:95], a[180:183], v[144:147]\n"
    "ds_read_b128 a[180:183], v15 offset:62208\n"
    "v_mfma_f32_16x16x32_bf16 v[148:151], a[92:95], a[184:187], v[148:151]\n"
    "v_mfma_f32_16x16x32_bf16 v[152:155], a[92:95], a[192:195], v[152:155]\n"
    "s_waitcnt lgkmcnt(2)\n"
    "v_mfma_f32_16x16x32_bf16 v[156:159], a[92:95], a[188:191], v[156:159]\n"
    "v_mfma_f32_16x16x32_bf16 v[144:147], a[96:99], a[184:187], v[144:147]\n"
    "ds_read_b128 a[184:187], v15 offset:62336\n"
    "v_mfma_f32_16x16x32_bf16 v[148:151], a[96:99], a[192:195], v[148:151]\n"
    "v_mfma_f32_16x16x32_bf16 v[152:155], a[96:99], a[188:191], v[152:155]\n"
    "s_waitcnt lgkmcnt(2)\n"
    "v_mfma_f32_16x16x32_bf16 v[156:159], a[96:99], a[176:179], v[156:159]\n"
    "v_mfma_f32_16x16x32_bf16 v[144:147], a[100:103], a[192:195], v[144:147]\n"
    "ds_read_b128 a[192:195], v15 offset:62464\n"
    "v_mfma_f32_16x16x32_bf16 v[148:151], a[100:103], a[188:191], v[148:151]\n"
    "v_mfma_f32_16x16x32_bf16 v[152:155], a[100:103], a[176:179], v[152:155]\n"
    "s_waitcnt lgkmcnt(2)\n"
    "v_mfma_f32_16x16x32_bf16 v[156:159], a[100:103], a[180:183], v[156:159]\n"
    "v_mfma_f32_16x16x32_bf16 v[144:147], a[104:107], a[188:191], v[144:147]\n"
    "ds_read_b128 a[188:191], v15 offset:62592\n"
    "v_mfma_f32_16x16x32_bf16 v[148:151], a[104:107], a[176:179], v[148:151]\n"
    "v_mfma_f32_16x16x32_bf16 v[152:155], a[104:107], a[180:183], v[152:155]\n"
    "s_waitcnt lgkmcnt(2)\n"
    "v_mfma_f32_16x16x32_bf16 v[156:159], a[104:107], a[184:187], v[156:159]\n"
    "v_mfma_f32_16x16x32_bf16 v[144:147], a[108:111], a[176:179], v[144:147]\n"
    "ds_read_b128 a[176:179], v15 offset:62720\n"
    "v_mfma_f32_16x16x32_bf16 v[148:151], a[108:111], a[180:183], v[148:151]\n"
    "v_mfma_f32_16x16x32_bf16 v[152:155], a[108:111], a[184:187], v[152:155]\n"
    "s_waitcnt lgkmcnt(2)\n"
    "v_mfma_f32_16x16x32_bf16 v[156:159], a[108:111], a[192:195], v[156:159]\n"
    "v_mfma_f32_16x16x32_bf16 v[144:147], a[112:115], a[180:183], v[144:147]\n"
    "ds_read_b128 a[180:183], v15 offset:62848\n"
    "v_mfma_f32_16x16x32_bf16 v[148:151], a[112:115], a[184:187], v[148:151]\n"
    "buffer_load_dwordx4 v[160:163], v22, s[16:19], s28 offen sc1\n"
    "v_mfma_f32_16x16x32_bf16 v[152:155], a[112:115], a[192:195], v[152:155]\n"
    "s_waitcnt lgkmcnt(2)\n"
    "v_mfma_f32_16x16x32_bf16 v[156:159], a[112:115], a[188:191], v[156:159]\n"
    "v_mfma_f32_16x16x32_bf16 v[144:147], a[116:119], a[184:187], v[144:147]\n"
    "ds_read_b128 a[184:187], v15 offset:62976\n"
    "v_mfma_f32_16x16x32_bf16 v[148:151], a[116:119], a[192:195], v[148:151]\n"
    "ds_read_b128 a[196:199], v10 offset:62336\n"
    "v_mfma_f32_16x16x32_bf16 v[152:155], a[116:119], a[188:191], v[152:155]\n"
    "s_waitcnt lgkmcnt(3)\n"
    "ds_read_b128 a[200:203], v10 offset:62464\n"
    "v_mfma_f32_16x16x32_bf16 v[156:159], a[116:119], a[176:179], v[156:159]\n"
    "buffer_load_dwordx4 v[164:167], v22, s[16:19], s30 offen sc1\n"
    "v_mfma_f32_16x16x32_bf16 v[144:147], a[120:123], a[192:195], v[144:147]\n"
    "ds_read_b128 a[192:195], v10 offset:62592\n"
    "v_mfma_f32_16x16x32_bf16 v[148:151], a[120:123], a[188:191], v[148:151]\n"
    "v_mfma_f32_16x16x32_bf16 v[152:155], a[120:123], a[176:179], v[152:155]\n"
    "s_waitcnt lgkmcnt(4)\n"
    "ds_read_b128 a[204:207], v10 offset:62720\n"
    "v_mfma_f32_16x16x32_bf16 v[156:159], a[120:123], a[180:183], v[156:159]\n"
    "buffer_load_dwordx4 v[168:171], v22, s[16:19], s29 offen sc1\n"
    "v_mfma_f32_16x16x32_bf16 v[144:147], a[124:127], a[188:191], v[144:147]\n"
    "ds_read_b128 a[188:191], v10 offset:62848\n"
    "v_mfma_f32_16x16x32_bf16 v[148:151], a[124:127], a[176:179], v[148:151]\n"
    "v_mfma_f32_16x16x32_bf16 v[152:155], a[124:127], a[180:183], v[152:155]\n"
    "s_waitcnt lgkmcnt(5)\n"
    "ds_read_b128 a[176:179], v10 offset:62976\n"
    "v_mfma_f32_16x16x32_bf16 v[156:159], a[124:127], a[184:187], v[156:159]\n"
    "s_waitcnt lgkmcnt(5)\n"
    "buffer_load_dwordx4 v[172:175], v22, s[16:19], s31 offen sc1\n"
    "v_mfma_f32_16x16x32_bf16 v[128:131], a[4:7], a[196:199], v[64:67]\n"
    "s_waitcnt lgkmcnt(4)\n"
    "ds_read_b128 a[180:183], v10 offset:63104\n"
    "v_mfma_f32_16x16x32_bf16 v[132:135], a[4:7], a[200:203], v[68:71]\n"
    "v_mfma_f32_16x16x32_bf16 v[128:131], a[8:11], a[200:203], v[128:131]\n"
    "s_waitcnt lgkmcnt(4)\n"
    "ds_read_b128 a[184:187], v11 offset:62208\n"
    "v_mfma_f32_16x16x32_bf16 v[132:135], a[8:11], a[192:195], v[132:135]\n"
    "v_cvt_pk_bf16_f32 v216, v152, v153\n"
    "v_cvt_pk_bf16_f32 v217, v154, v155\n"
    "v_mfma_f32_16x16x32_bf16 v[128:131], a[12:15], a[192:195], v[128:131]\n"
    "s_waitcnt lgkmcnt(4)\n"
    "ds_read_b128 a[192:195], v11 offset:62336\n"
    "v_cvt_pk_bf16_f32 v218, v156, v157\n"
    "v_mfma_f32_16x16x32_bf16 v[132:135], a[12:15], a[204:207], v[132:135]\n"
    "v_cvt_pk_bf16_f32 v219, v158, v159\n"
    "v_mfma_f32_16x16x32_bf16 v[128:131], a[16:19], a[204:207], v[128:131]\n"
    "s_waitcnt lgkmcnt(4)\n"
    "s_xor_b32 s24, s39, 65536\n"
    "ds_read_b128 a[196:199], v11 offset:62464\n"
    "v_mfma_f32_16x16x32_bf16 v[132:135], a[16:19], a[188:191], v[132:135]\n"
    "v_min_u32 v216, -2, v216\n"
    "v_min_u32 v217, -2, v217\n"
    "v_mfma_f32_16x16x32_bf16 v[128:131], a[20:23], a[188:191], v[128:131]\n"
    "s_waitcnt lgkmcnt(4)\n"
    "ds_read_b128 a[188:191], v11 offset:62592\n"
    "v_min_u32 v218, -2, v218\n"
    "v_mfma_f32_16x16x32_bf16 v[132:135], a[20:23], a[176:179], v[132:135]\n"
    "v_min_u32 v219, -2, v219\n"
    "v_mfma_f32_16x16x32_bf16 v[128:131], a[24:27], a[176:179], v[128:131]\n"
    "s_waitcnt lgkmcnt(4)\n"
    "ds_read_b128 a[176:179], v11 offset:62720\n"
    "v_mfma_f32_16x16x32_bf16 v[132:135], a[24:27], a[180:183], v[132:135]\n"
    "buffer_store_dwordx4 v[216:219], v22, s[16:19], s24 offen sc1\n"
    "v_mfma_f32_16x16x32_bf16 v[128:131], a[28:31], a[180:183], v[128:131]\n"
    "s_waitcnt lgkmcnt(4)\n"
    "ds_read_b128 a[180:183], v11 offset:62848\n"
    "v_cvt_pk_bf16_f32 v220, v144, v145\n"
    "v_mfma_f32_16x16x32_bf16 v[132:135], a[28:31], a[184:187], v[132:135]\n"
    "v_cvt_pk_bf16_f32 v221, v146, v147\n"
    "v_cvt_pk_bf16_f32 v222, v148, v149\n"
    "v_mfma_f32_16x16x32_bf16 v[128:131], a[32:35], a[184:187], v[128:131]\n"
    "s_waitcnt lgkmcnt(4)\n"
    "ds_read_b128 a[184:187], v11 offset:62976\n"
    "v_cvt_pk_bf16_f32 v223, v150, v151\n"
    "v_mfma_f32_16x16x32_bf16 v[132:135], a[32:35], a[192:195], v[132:135]\n"
    "s_xor_b32 s24, s39, 98304\n"
    "v_min_u32 v220, -2, v220\n"
    "v_min_u32 v221, -2, v221\n"
    "v_mfma_f32_16x16x32_bf16 v[128:131], a[36:39], a[192:195], v[128:131]\n"
    "s_waitcnt lgkmcnt(4)\n"
    "ds_read_b128 a[192:195], v11 offset:63104\n"
    "v_min_u32 v222, -2, v222\n"
    "v_mfma_f32_16x16x32_bf16 v[132:135], a[36:39], a[196:199], v[132:135]\n"
    "v_min_u32 v223, -2, v223\n"
    "v_mfma_f32_16x16x32_bf16 v[128:131], a[40:43], a[196:199], v[128:131]\n"
    "s_waitcnt lgkmcnt(4)\n"
    "ds_read_b128 a[196:199], v12 offset:62208\n"
    "v_mfma_f32_16x16x32_bf16 v[132:135], a[40:43], a[188:191], v[132:135]\n"
    "ds_read_b128 a[200:203], v13 offset:62336\n"
    "v_mfma_f32_16x16x32_bf16 v[128:131], a[44:47], a[188:191], v[128:131]\n"
    "s_waitcnt lgkmcnt(5)\n"
    "ds_read_b128 a[188:191], v13 offset:62464\n"
    "v_mfma_f32_16x16x32_bf16 v[132:135], a[44:47], a[176:179], v[132:135]\n"
    "buffer_store_dwordx4 v[220:223], v22, s[16:19], s24 offen sc1\n"
    "v_mfma_f32_16x16x32_bf16 v[128:131], a[48:51], a[176:179], v[128:131]\n"
    "s_waitcnt lgkmcnt(5)\n"
    "ds_read_b128 a[176:179], v13 offset:62592\n"
    "v_mfma_f32_16x16x32_bf16 v[132:135], a[48:51], a[180:183], v[132:135]\n"
    "v_mfma_f32_16x16x32_bf16 v[128:131], a[52:55], a[180:183], v[128:131]\n"
    "s_waitcnt lgkmcnt(5)\n"
    "ds_read_b128 a[180:183], v13 offset:62720\n"
    "v_mfma_f32_16x16x32_bf16 v[132:135], a[52:55], a[184:187], v[132:135]\n"
    "v_mfma_f32_16x16x32_bf16 v[128:131], a[56:59], a[184:187], v[128:131]\n"
    "s_waitcnt lgkmcnt(5)\n"
    "ds_read_b128 a[184:187], v13 offset:62848\n"
    "v_mfma_f32_16x16x32_bf16 v[132:135], a[56:59], a[192:195], v[132:135]\n"
    "v_mfma_f32_16x16x32_bf16 v[128:131], a[60:63], a[192:195], v[128:131]\n"
    "s_waitcnt lgkmcnt(5)\n"
    "ds_read_b128 a[192:195], v13 offset:62976\n"
    "v_mfma_f32_16x16x32_bf16 v[132:135], a[60:63], a[196:199], v[132:135]\n"
    "s_waitcnt lgkmcnt(5)\n"
    "v_mfma_f32_16x16x32_bf16 v[128:131], a[68:71], a[200:203], v[128:131]\n"
    "s_waitcnt lgkmcnt(4)\n"
    "ds_read_b128 a[196:199], v13 offset:63104\n"
    "v_mfma_f32_16x16x32_bf16 v[132:135], a[68:71], a[188:191], v[132:135]\n"
    "v_mfma_f32_16x16x32_bf16 v[128:131], a[72:75], a[188:191], v[128:131]\n"
    "s_waitcnt lgkmcnt(4)\n"
    "ds_read_b128 a[188:191], v14 offset:62208\n"
    "v_mfma_f32_16x16x32_bf16 v[132:135], a[72:75], a[176:179], v[132:135]\n"
    "v_mfma_f32_16x16x32_bf16 v[128:131], a[76:79], a[176:179], v[128:131]\n"
    "s_waitcnt lgkmcnt(4)\n"
    "ds_read_b128 a[176:179], v14 offset:62336\n"
    "v_mfma_f32_16x16x32_bf16 v[132:135], a[76:79], a[180:183], v[132:135]\n"
    "v_mfma_f32_16x16x32_bf16 v[128:131], a[80:83], a[180:183], v[128:131]\n"
    "s_waitcnt lgkmcnt(4)\n"
    "ds_read_b128 a[180:183], v14 offset:62464\n"
    "v_mfma_f32_16x16x32_bf16 v[132:135], a[80:83], a[184:187], v[132:135]\n"
    "v_mfma_f32_16x16x32_bf16 v[128:131], a[84:87], a[184:187], v[128:131]\n"
    "s_waitcnt lgkmcnt(4)\n"
    "ds_read_b128 a[184:187], v14 offset:62592\n"
    "v_mfma_f32_16x16x32_bf16 v[132:135], a[84:87], a[192:195], v[132:135]\n"
    "v_mfma_f32_16x16x32_bf16 v[128:131], a[88:91], a[192:195], v[128:131]\n"
    "s_waitcnt lgkmcnt(4)\n"
    "ds_read_b128 a[192:195], v14 offset:62720\n"
    "v_mfma_f32_16x16x32_bf16 v[132:135], a[88:91], a[196:199], v[132:135]\n"
    "v_mfma_f32_16x16x32_bf16 v[128:131], a[92:95], a[196:199], v[128:131]\n"
    "s_waitcnt lgkmcnt(4)\n"
    "ds_read_b128 a[196:199], v14 offset:62848\n"
    "v_mfma_f32_16x16x32_bf16 v[132:135], a[92:95], a[188:191], v[132:135]\n"
    "v_mfma_f32_16x16x32_bf16 v[128:131], a[96:99], a[188:191], v[128:131]\n"
    "s_waitcnt lgkmcnt(4)\n"
    "ds_read_b128 a[188:191], v14 offset:62976\n"
    "v_mfma_f32_16x16x32_bf16 v[132:135], a[96:99], a[176:179], v[132:135]\n"
    "v_mfma_f32_16x16x32_bf16 v[128:131], a[100:103], a[176:179], v[128:131]\n"
    "s_waitcnt lgkmcnt(4)\n"
    "ds_read_b128 a[176:179], v14 offset:63104\n"
    "v_mfma_f32_16x16x32_bf16 v[132:135], a[100:103], a[180:183], v[132:135]\n"
    "v_mfma_f32_16x16x32_bf16 v[128:131], a[104:107], a[180:183], v[128:131]\n"
    "s_waitcnt lgkmcnt(4)\n"
    "ds_read_b128 a[180:183], v15 offset:62208\n"
    "v_mfma_f32_16x16x32_bf16 v[132:135], a[104:107], a[184:187], v[132:135]\n"
    "ds_read_b128 a[200:203], v10 offset:62592\n"
    "v_mfma_f32_16x16x32_bf16 v[128:131], a[108:111], a[184:187], v[128:131]\n"
    "s_waitcnt lgkmcnt(5)\n"
    "ds_read_b128 a[184:187], v10 offset:62720\n"
    "v_mfma_f32_16x16x32_bf16 v[132:135], a[108:111], a[192:195], v[132:135]\n"
    "v_mfma_f32_16x16x32_bf16 v[128:131], a[112:115], a[192:195], v[128:131]\n"
    "s_waitcnt lgkmcnt(5)\n"
    "ds_read_b128 a[192:195], v10 offset:62848\n"
    "v_mfma_f32_16x16x32_bf16 v[132:135], a[112:115], a[196:199], v[132:135]\n"
    "v_mfma_f32_16x16x32_bf16 v[128:131], a[116:119], a[196:199], v[128:131]\n"
    "s_waitcnt lgkmcnt(5)\n"
    "ds_read_b128 a[196:199], v10 offset:62976\n"
    "v_mfma_f32_16x16x32_bf16 v[132:135], a[116:119], a[188:191], v[132:135]\n"
    "v_mfma_f32_16x16x32_bf16 v[128:131], a[120:123], a[188:191], v[128:131]\n"
    "s_waitcnt lgkmcnt(5)\n"
    "ds_read_b128 a[188:191], v10 offset:63104\n"
    "v_mfma_f32_16x16x32_bf16 v[132:135], a[120:123], a[176:179], v[132:135]\n"
    "v_mfma_f32_16x16x32_bf16 v[128:131], a[124:127], a[176:179], v[128:131]\n"
    "s_waitcnt lgkmcnt(5)\n"
    "ds_read_b128 a[176:179], v11 offset:62208\n"
    "v_mfma_f32_16x16x32_bf16 v[132:135], a[124:127], a[180:183], v[132:135]\n"
    "s_waitcnt lgkmcnt(5)\n"
    "v_mfma_f32_16x16x32_bf16 v[136:139], a[4:7], a[200:203], v[72:75]\n"
    "s_waitcnt lgkmcnt(4)\n"
    "ds_read_b128 a[180:183], v11 offset:62336\n"
    "v_mfma_f32_16x16x32_bf16 v[140:143], a[4:7], a[184:187], v[76:79]\n"
    "v_mfma_f32_16x16x32_bf16 v[136:139], a[8:11], a[184:187], v[136:139]\n"
    "s_waitcnt lgkmcnt(4)\n"
    "ds_read_b128 a[184:187], v11 offset:62464\n"
    "v_mfma_f32_16x16x32_bf16 v[140:143], a[8:11], a[192:195], v[140:143]\n"
    "v_cvt_pk_bf16_f32 v224, v128, v129\n"
    "v_cvt_pk_bf16_f32 v225, v130, v131\n"
    "v_mfma_f32_16x16x32_bf16 v[136:139], a[12:15], a[192:195], v[136:139]\n"
    "s_waitcnt lgkmcnt(4)\n"
    "ds_read_b128 a[192:195], v11 offset:62592\n"
    "v_cvt_pk_bf16_f32 v226, v132, v133\n"
    "v_mfma_f32_16x16x32_bf16 v[140:143], a[12:15], a[196:199], v[140:143]\n"
    "v_cvt_pk_bf16_f32 v227, v134, v135\n"
    "v_mfma_f32_16x16x32_bf16 v[136:139], a[16:19], a[196:199], v[136:139]\n"
    "s_waitcnt lgkmcnt(4)\n"
    "s_xor_b32 s24, s39, 32768\n"
    "ds_read_b128 a[196:199], v11 offset:62720\n"
    "v_mfma_f32_16x16x32_bf16 v[140:143], a[16:19], a[188:191], v[140:143]\n"
    "v_min_u32 v224, -2, v224\n"
    "v_min_u32 v225, -2, v225\n"
    "v_mfma_f32_16x16x32_bf16 v[136:139], a[20:23], a[188:191], v[136:139]\n"
    "s_waitcnt lgkmcnt(4)\n"
    "ds_read_b128 a[188:191], v11 offset:62848\n"
    "v_min_u32 v226, -2, v226\n"
    "v_mfma_f32_16x16x32_bf16 v[140:143], a[20:23], a[176:179], v[140:143]\n"
    "v_min_u32 v227, -2, v227\n"
    "v_mfma_f32_16x16x32_bf16 v[136:139], a[24:27], a[176:179], v[136:139]\n"
    "s_waitcnt lgkmcnt(4)\n"
    "ds_read_b128 a[176:179], v11 offset:62976\n"
    "v_mfma_f32_16x16x32_bf16 v[140:143], a[24:27], a[180:183], v[140:143]\n"
    "buffer_store_dwordx4 v[224:227], v22, s[16:19], s24 offen sc1\n"
    "v_mfma_f32_16x16x32_bf16 v[136:139], a[28:31], a[180:183], v[136:139]\n"
    "s_waitcnt lgkmcnt(4)\n"
    "ds_read_b128 a[180:183], v11 offset:63104\n"
    "v_mfma_f32_16x16x32_bf16 v[140:143], a[28:31], a[184:187], v[140:143]\n"
    "v_mfma_f32_16x16x32_bf16 v[136:139], a[32:35], a[184:187], v[136:139]\n"
    "s_waitcnt lgkmcnt(4)\n"
    "ds_read_b128 a[184:187], v12 offset:62208\n"
    "v_mfma_f32_16x16x32_bf16 v[140:143], a[32:35], a[192:195], v[140:143]\n"
    "v_mfma_f32_16x16x32_bf16 v[136:139], a[36:39], a[192:195], v[136:139]\n"
    "s_waitcnt lgkmcnt(4)\n"
    "ds_read_b128 a[192:195], v12 offset:62336\n"
    "v_mfma_f32_16x16x32_bf16 v[140:143], a[36:39], a[196:199], v[140:143]\n"
    "v_mfma_f32_16x16x32_bf16 v[136:139], a[40:43], a[196:199], v[136:139]\n"
    "s_waitcnt lgkmcnt(4)\n"
    "ds_read_b128 a[196:199], v12 offset:62464\n"
    "v_mfma_f32_16x16x32_bf16 v[140:143], a[40:43], a[188:191], v[140:143]\n"
    "ds_read_b128 a[200:203], v13 offset:62592\n"
    "v_mfma_f32_16x16x32_bf16 v[136:139], a[44:47], a[188:191], v[136:139]\n"
    "s_waitcnt lgkmcnt(5)\n"
    "s_waitcnt vmcnt(3)\n"
    "ds_read_b128 a[188:191], v13 offset:62720\n"
    "v_mfma_f32_16x16x32_bf16 v[140:143], a[44:47], a[176:179], v[140:143]\n"
    "v_max3_u32 v240, v160, v161, v162\n"
    "v_max3_u32 v240, v240, v163, v164\n"
    "v_mfma_f32_16x16x32_bf16 v[136:139], a[48:51], a[176:179], v[136:139]\n"
    "s_waitcnt lgkmcnt(5)\n"
    "ds_read_b128 a[176:179], v13 offset:62848\n"
    "v_max3_u32 v240, v240, v165, v166\n"
    "v_mfma_f32_16x16x32_bf16 v[140:143], a[48:51], a[180:183], v[140:143]\n"
    "buffer_load_dwordx4 v[176:179], v22, s[16:19], s26 offen sc1\n"
    "v_mfma_f32_16x16x32_bf16 v[136:139], a[52:55], a[180:183], v[136:139]\n"
    "s_waitcnt lgkmcnt(5)\n"
    "ds_read_b128 a[180:183], v13 offset:62976\n"
    "v_max3_u32 v240, v240, v167, v168\n"
    "v_mfma_f32_16x16x32_bf16 v[140:143], a[52:55], a[184:187], v[140:143]\n"
    "v_max3_u32 v240, v240, v169, v170\n"
    "v_max3_u32 v240, v240, v171, v172\n"
    "v_mfma_f32_16x16x32_bf16 v[136:139], a[56:59], a[184:187], v[136:139]\n"
    "s_waitcnt lgkmcnt(5)\n"
    "ds_read_b128 a[184:187], v13 offset:63104\n"
    "v_max3_u32 v240, v240, v173, v174\n"
    "v_mfma_f32_16x16x32_bf16 v[140:143], a[56:59], a[192:195], v[140:143]\n"
    "buffer_load_dwordx4 v[180:183], v22, s[16:19], s27 offen sc1\n"
    "v_mfma_f32_16x16x32_bf16 v[136:139], a[60:63], a[192:195], v[136:139]\n"
    "s_waitcnt lgkmcnt(5)\n"
    "ds_read_b128 a[192:195], v14 offset:62208\n"
    "v_max_u32 v240, v240, v175\n"
    "v_mfma_f32_16x16x32_bf16 v[140:143], a[60:63], a[196:199], v[140:143]\n"
    "s_waitcnt lgkmcnt(5)\n"
    "v_cmp_eq_u32 vcc, -1, v240\n"
    "s_cbranch_vccz LE1ok_%=\n"
    "s_mov_b32 s43, 0x100000\n"
    "LE1lp_%=:\n"
    "buffer_load_dwordx4 v[160:163], v22, s[16:19], s28 offen sc1\n"
    "buffer_load_dwordx4 v[164:167], v22, s[16:19], s30 offen sc1\n"
    "buffer_load_dwordx4 v[168:171], v22, s[16:19], s29 offen sc1\n"
    "buffer_load_dwordx4 v[172:175], v22, s[16:19], s31 offen sc1\n"
    "s_waitcnt vmcnt(0)\n"
    "s_sub_u32 s43, s43, 1\n"
    "s_cbranch_scc1 LE1ok_%=\n"
    "v_max3_u32 v240, v160, v161, v162\n"
    "v_max3_u32 v240, v240, v163, v164\n"
    "v_max3_u32 v240, v240, v165, v166\n"
    "v_max3_u32 v240, v240, v167, v168\n"
    "v_max3_u32 v240, v240, v169, v170\n"
    "v_max3_u32 v240, v240, v171, v172\n"
    "v_max3_u32 v240, v240, v173, v174\n"
    "v_max_u32 v240, v240, v175\n"
    "v_cmp_eq_u32 vcc, -1, v240\n"
    "s_cbranch_vccnz LE1lp_%=\n"
    "LE1ok_%=:\n"
    "v_mfma_f32_16x16x32_bf16 v[136:139], a[68:71], a[200:203], v[136:139]\n"
    "s_waitcnt lgkmcnt(4)\n"
    "ds_read_b128 a[196:199], v14 offset:62336\n"
    "v_lshlrev_b32 v188, 16, v160\n"
    "v_mfma_f32_16x16x32_bf16 v[140:143], a[68:71], a[188:191], v[140:143]\n"
    "v_and_b32 v189, s48, v160\n"
    "v_lshlrev_b32 v190, 16, v161\n"
    "v_mfma_f32_16x16x32_bf16 v[136:139], a[72:75], a[188:191], v[136:139]\n"
    "s_waitcnt lgkmcnt(4)\n"
    "ds_read_b128 a[188:191], v14 offset:62464\n"
    "v_and_b32 v191, s48, v161\n"
    "v_mfma_f32_16x16x32_bf16 v[140:143], a[72:75], a[176:179], v[140:143]\n"
    "v_lshlrev_b32 v192, 16, v162\n"
    "v_and_b32 v193, s48, v162\n"
    "v_mfma_f32_16x16x32_bf16 v[136:139], a[76:79], a[176:179], v[136:139]\n"
    "s_waitcnt lgkmcnt(4)\n"
    "ds_read_b128 a[176:179], v14 offset:62592\n"
    "v_lshlrev_b32 v194, 16, v163\n"
    "v_mfma_f32_16x16x32_bf16 v[140:143], a[76:79], a[180:183], v[140:143]\n"
    "v_and_b32 v195, s48, v163\n"
    "v_lshlrev_b32 v240, 16, v164\n"
    "v_mfma_f32_16x16x32_bf16 v[136:139], a[80:83], a[180:183], v[136:139]\n"
    "s_waitcnt lgkmcnt(4)\n"
    "ds_read_b128 a[180:183], v14 offset:62720\n"
    "v_and_b32 v241, s48, v164\n"
    "v_mfma_f32_16x16x32_bf16 v[140:143], a[80:83], a[184:187], v[140:143]\n"
    "v_lshlrev_b32 v242, 16, v165\n"
    "v_and_b32 v243, s48, v165\n"
    "v_mfma_f32_16x16x32_bf16 v[136:139], a[84:87], a[184:187], v[136:139]\n"
    "s_waitcnt lgkmcnt(4)\n"
    "ds_read_b128 a[184:187], v14 offset:62848\n"
    "v_lshlrev_b32 v244, 16, v166\n"
    "v_mfma_f32_16x16x32_bf16 v[140:143], a[84:87], a[192:195], v[140:143]\n"
    "v_and_b32 v245, s48, v166\n"
    "v_lshlrev_b32 v246, 16, v167\n"
    "v_mfma_f32_16x16x32_bf16 v[136:139], a[88:91], a[192:195], v[136:139]\n"
    "s_waitcnt lgkmcnt(4)\n"
    "ds_read_b128 a[192:195], v14 offset:62976\n"
    "v_and_b32 v247, s48, v167\n"
    "v_mfma_f32_16x16x32_bf16 v[140:143], a[88:91], a[196:199], v[140:143]\n"
    "v_add_f32 v188, v188, v240\n"
    "v_add_f32 v189, v189, v241\n"
    "v_mfma_f32_16x16x32_bf16 v[136:139], a[92:95], a[196:199], v[136:139]\n"
    "s_waitcnt lgkmcnt(4)\n"
    "ds_read_b128 a[196:199], v14 offset:63104\n"
    "v_add_f32 v190, v190, v242\n"
    "v_mfma_f32_16x16x32_bf16 v[140:143], a[92:95], a[188:191], v[140:143]\n"
    "v_add_f32 v191, v191, v243\n"
    "v_add_f32 v192, v192, v244\n"
    "v_mfma_f32_16x16x32_bf16 v[136:139], a[96:99], a[188:191], v[136:139]\n"
    "s_waitcnt lgkmcnt(4)\n"
    "ds_read_b128 a[188:191], v15 offset:62208\n"
    "v_add_f32 v193, v193, v245\n"
    "v_mfma_f32_16x16x32_bf16 v[140:143], a[96:99], a[176:179], v[140:143]\n"
    "v_add_f32 v194, v194, v246\n"
    "v_add_f32 v195, v195, v247\n"
    "v_mfma_f32_16x16x32_bf16 v[136:139], a[100:103], a[176:179], v[136:139]\n"
    "s_waitcnt lgkmcnt(4)\n"
    "ds_read_b128 a[176:179], v15 offset:62336\n"
    "v_lshlrev_b32 v40, 16, v168\n"
    "v_mfma_f32_16x16x32_bf16 v[140:143], a[100:103], a[180:183], v[140:143]\n"
    "v_and_b32 v41, s48, v168\n"
    "v_lshlrev_b32 v42, 16, v169\n"
    "v_mfma_f32_16x16x32_bf16 v[136:139], a[104:107], a[180:183], v[136:139]\n"
    "s_waitcnt lgkmcnt(4)\n"
    "ds_read_b128 a[180:183], v15 offset:62464\n"
    "v_and_b32 v43, s48, v169\n"
    "v_mfma_f32_16x16x32_bf16 v[140:143], a[104:107], a[184:187], v[140:143]\n"
    "v_lshlrev_b32 v44, 16, v170\n"
    "v_and_b32 v45, s48, v170\n"
    "v_mfma_f32_16x16x32_bf16 v[136:139], a[108:111], a[184:187], v[136:139]\n"
    "s_waitcnt lgkmcnt(4)\n"
    "buffer_load_dwordx4 v[184:187], v22, s[16:19], s25 offen sc1\n"
    "v_mfma_f32_16x16x32_bf16 v[140:143], a[108:111], a[192:195], v[140:143]\n"
    "v_lshlrev_b32 v46, 16, v171\n"
    "v_and_b32 v47, s48, v171\n"
    "v_mfma_f32_16x16x32_bf16 v[136:139], a[112:115], a[192:195], v[136:139]\n"
    "s_waitcnt lgkmcnt(3)\n"
    "v_lshlrev_b32 v240, 16, v172\n"
    "v_and_b32 v241, s48, v172\n"
    "v_mfma_f32_16x16x32_bf16 v[140:143], a[112:115], a[196:199], v[140:143]\n"
    "v_lshlrev_b32 v242, 16, v173\n"
    "v_and_b32 v243, s48, v173\n"
    "v_mfma_f32_16x16x32_bf16 v[136:139], a[116:119], a[196:199], v[136:139]\n"
    "s_waitcnt lgkmcnt(2)\n"
    "v_lshlrev_b32 v244, 16, v174\n"
    "v_and_b32 v245, s48, v174\n"
    "v_mfma_f32_16x16x32_bf16 v[140:143], a[116:119], a[188:191], v[140:143]\n"
    "v_lshlrev_b32 v246, 16, v175\n"
    "v_and_b32 v247, s48, v175\n"
    "v_mfma_f32_16x16x32_bf16 v[136:139], a[120:123], a[188:191], v[136:139]\n"
    "s_waitcnt lgkmcnt(1)\n"
    "v_add_f32 v40, v40, v240\n"
    "v_add_f32 v41, v41, v241\n"
    "v_mfma_f32_16x16x32_bf16 v[140:143], a[120:123], a[176:179], v[140:143]\n"
    "v_add_f32 v42, v42, v242\n"
    "v_add_f32 v43, v43, v243\n"
    "v_mfma_f32_16x16x32_bf16 v[136:139], a[124:127], a[176:179], v[136:139]\n"
    "s_waitcnt lgkmcnt(0)\n"
    "v_add_f32 v44, v44, v244\n"
    "v_add_f32 v45, v45, v245\n"
    "v_mfma_f32_16x16x32_bf16 v[140:143], a[124:127], a[180:183], v[140:143]\n"
    "v_add_f32 v46, v46, v246\n"
    "v_add_f32 v47, v47, v247\n"
    "s_waitcnt vmcnt(1)\n"
    "v_max3_u32 v240, v176, v177, v178\n"
    "v_max3_u32 v240, v240, v179, v180\n"
    "v_max3_u32 v240, v240, v181, v182\n"
    "v_max_u32 v240, v240, v183\n"
    "v_cmp_eq_u32 vcc, -1, v240\n"
    "s_cbranch_vccz LM1ok_%=\n"
    "s_mov_b32 s43, 0x100000\n"
    "LM1lp_%=:\n"
    "buffer_load_dwordx4 v[176:179], v22, s[16:19], s26 offen sc1\n"
    "buffer_load_dwordx4 v[180:183], v22, s[16:19], s27 offen sc1\n"
    "s_waitcnt vmcnt(0)\n"
    "s_sub_u32 s43, s43, 1\n"
    "s_cbranch_scc1 LM1ok_%=\n"
    "v_max3_u32 v240, v176, v177, v178\n"
    "v_max3_u32 v240, v240, v179, v180\n"
    "v_max3_u32 v240, v240, v181, v182\n"
    "v_max_u32 v240, v240, v183\n"
    "v_cmp_eq_u32 vcc, -1, v240\n"
    "s_cbranch_vccnz LM1lp_%=\n"
    "LM1ok_%=:\n"
    "v_mfma_f32_16x16x32_bf16 v[136:139], v[236:239], v[176:179], v[136:139]\n"
    "v_mfma_f32_16x16x32_bf16 v[140:143], v[234:237], v[176:179], v[140:143]\n"
    "v_mfma_f32_16x16x32_bf16 v[40:43], v[236:239], v[180:183], v[40:43]\n"
    "v_mfma_f32_16x16x32_bf16 v[44:47], v[234:237], v[180:183], v[44:47]\n"
    "s_nop 4\n"
    "v_pk_add_f32 v[136:137], v[136:137], v[188:189]\n"
    "v_pk_add_f32 v[138:139], v[138:139], v[190:191]\n"
    "v_pk_add_f32 v[140:141], v[140:141], v[192:193]\n"
    "v_pk_add_f32 v[142:143], v[142:143], v[194:195]\n"
    "v_pk_mul_f32 v[200:201], v[136:137], s[44:45] op_sel:[0,1] op_sel_hi:[1,1]\n"
    "v_pk_mul_f32 v[202:203], v[138:139], s[44:45] op_sel:[0,1] op_sel_hi:[1,1]\n"
    "v_pk_mul_f32 v[204:205], v[140:141], s[44:45] op_sel:[0,1] op_sel_hi:[1,1]\n"
    "v_pk_mul_f32 v[206:207], v[142:143], s[44:45] op_sel:[0,1] op_sel_hi:[1,1]\n"
    "v_exp_f32 v200, v200\n"
    "v_exp_f32 v201, v201\n"
    "v_exp_f32 v202, v202\n"
    "v_exp_f32 v203, v203\n"
    "v_exp_f32 v204, v204\n"
    "v_exp_f32 v205, v205\n"
    "v_exp_f32 v206, v206\n"
    "v_exp_f32 v207, v207\n"
    "v_pk_add_f32 v[200:201], v[200:201], 1.0 op_sel_hi:[1,0]\n"
    "v_pk_add_f32 v[202:203], v[202:203], 1.0 op_sel_hi:[1,0]\n"
    "v_pk_add_f32 v[204:205], v[204:205], 1.0 op_sel_hi:[1,0]\n"
    "v_pk_add_f32 v[206:207], v[206:207], 1.0 op_sel_hi:[1,0]\n"
    "s_waitcnt vmcnt(0)\n"
    "v_max3_u32 v188, v184, v185, v186\n"
    "v_max_u32 v188, v188, v187\n"
    "v_cmp_eq_u32 vcc, -1, v188\n"
    "s_cbranch_vccz LL1ok_%=\n"
    "s_mov_b32 s43, 0x100000\n"
    "LL1lp_%=:\n"
    "buffer_load_dwordx4 v[184:187], v22, s[16:19], s25 offen sc1\n"
    "s_waitcnt vmcnt(0)\n"
    "s_sub_u32 s43, s43, 1\n"
    "s_cbranch_scc1 LL1ok_%=\n"
    "v_max3_u32 v188, v184, v185, v186\n"
    "v_max_u32 v188, v188, v187\n"
    "v_cmp_eq_u32 vcc, -1, v188\n"
    "s_cbranch_vccnz LL1lp_%=\n"
    "LL1ok_%=:\n"
    "v_lshlrev_b32 v188, 16, v184\n"
    "v_and_b32 v189, s48, v184\n"
    "v_lshlrev_b32 v190, 16, v185\n"
    "v_and_b32 v191, s48, v185\n"
    "v_lshlrev_b32 v192, 16, v186\n"
    "v_and_b32 v193, s48, v186\n"
    "v_lshlrev_b32 v194, 16, v187\n"
    "v_and_b32 v195, s48, v187\n"
    "v_pk_add_f32 v[40:41], v[40:41], v[188:189]\n"
    "v_pk_add_f32 v[42:43], v[42:43], v[190:191]\n"
    "v_pk_add_f32 v[44:45], v[44:45], v[192:193]\n"
    "v_pk_add_f32 v[46:47], v[46:47], v[194:195]\n"
    "v_pk_mul_f32 v[240:241], v[40:41], s[46:47] op_sel_hi:[1,0]\n"
    "v_pk_mul_f32 v[242:243], v[42:43], s[46:47] op_sel_hi:[1,0]\n"
    "v_pk_mul_f32 v[244:245], v[44:45], s[46:47] op_sel_hi:[1,0]\n"
    "v_pk_mul_f32 v[246:247], v[46:47], s[46:47] op_sel_hi:[1,0]\n"
    "v_exp_f32 v240, -|v240|\n"
    "v_fma_f32 v248, -v40, v40, |v40|\n"
    "v_exp_f32 v241, -|v241|\n"
    "v_fma_f32 v249, -v41, v41, |v41|\n"
    "v_exp_f32 v242, -|v242|\n"
    "v_fma_f32 v250, -v42, v42, |v42|\n"
    "v_exp_f32 v243, -|v243|\n"
    "v_fma_f32 v251, -v43, v43, |v43|\n"
    "v_exp_f32 v244, -|v244|\n"
    "v_fma_f32 v252, -v44, v44, |v44|\n"
    "v_exp_f32 v245, -|v245|\n"
    "v_fma_f32 v253, -v45, v45, |v45|\n"
    "v_exp_f32 v246, -|v246|\n"
    "v_fma_f32 v254, -v46, v46, |v46|\n"
    "v_exp_f32 v247, -|v247|\n"
    "v_fma_f32 v255, -v47, v47, |v47|\n"
    "v_pk_fma_f32 v[168:169], v[240:241], 0.5, 0.5 op_sel_hi:[1,0,0] neg_lo:[1,0,0] neg_hi:[1,0,0]\n"
    "v_pk_fma_f32 v[170:171], v[242:243], 0.5, 0.5 op_sel_hi:[1,0,0] neg_lo:[1,0,0] neg_hi:[1,0,0]\n"
    "v_pk_fma_f32 v[172:173], v[244:245], 0.5, 0.5 op_sel_hi:[1,0,0] neg_lo:[1,0,0] neg_hi:[1,0,0]\n"
    "v_pk_fma_f32 v[174:175], v[246:247], 0.5, 0.5 op_sel_hi:[1,0,0] neg_lo:[1,0,0] neg_hi:[1,0,0]\n"
    "v_pk_fma_f32 v[240:241], v[240:241], 0.5, 0.5 op_sel_hi:[1,0,0]\n"
    "v_pk_fma_f32 v[242:243], v[242:243], 0.5, 0.5 op_sel_hi:[1,0,0]\n"
    "v_pk_fma_f32 v[244:245], v[244:245], 0.5, 0.5 op_sel_hi:[1,0,0]\n"
    "v_pk_fma_f32 v[246:247], v[246:247], 0.5, 0.5 op_sel_hi:[1,0,0]\n"
    "v_med3_f32 v248, v168, v248, |v40|\n"
    "v_med3_f32 v249, v169, v249, |v41|\n"
    "v_med3_f32 v250, v170, v250, |v42|\n"
    "v_med3_f32 v251, v171, v251, |v43|\n"
    "v_med3_f32 v252, v172, v252, |v44|\n"
    "v_med3_f32 v253, v173, v253, |v45|\n"
    "v_med3_f32 v254, v174, v254, |v46|\n"
    "v_med3_f32 v255, v175, v255, |v47|\n"
    "v_bfi_b32 v248, s47, v248, v40\n"
    "v_bfi_b32 v249, s47, v249, v41\n"
    "v_bfi_b32 v250, s47, v250, v42\n"
    "v_bfi_b32 v251, s47, v251, v43\n"
    "v_bfi_b32 v252, s47, v252, v44\n"
    "v_bfi_b32 v253, s47, v253, v45\n"
    "v_bfi_b32 v254, s47, v254, v46\n"
    "v_bfi_b32 v255, s47, v255, v47\n"
    "v_pk_mul_f32 v[240:241], v[240:241], v[200:201]\n"
    "v_pk_mul_f32 v[242:243], v[242:243], v[202:203]\n"
    "v_pk_mul_f32 v[244:245], v[244:245], v[204:205]\n"
    "v_pk_mul_f32 v[246:247], v[246:247], v[206:207]\n"
    "v_rcp_f32 v240, v240\n"
    "v_rcp_f32 v241, v241\n"
    "v_rcp_f32 v242, v242\n"
    "v_rcp_f32 v243, v243\n"
    "v_rcp_f32 v244, v244\n"
    "v_rcp_f32 v245, v245\n"
    "v_rcp_f32 v246, v246\n"
    "v_rcp_f32 v247, v247\n"
    "v_pk_mul_f32 v[248:249], v[248:249], v[240:241]\n"
    "v_pk_mul_f32 v[250:251], v[250:251], v[242:243]\n"
    "v_pk_mul_f32 v[252:253], v[252:253], v[244:245]\n"
    "v_pk_mul_f32 v[254:255], v[254:255], v[246:247]\n"
    "v_cvt_pk_bf16_f32 v240, v248, v249\n"
    "v_cvt_pk_bf16_f32 v241, v250, v251\n"
    "v_cvt_pk_bf16_f32 v242, v252, v253\n"
    "v_cvt_pk_bf16_f32 v243, v254, v255\n"
    "buffer_store_dwordx2 v[240:241], v25, s[20:23], 0 offen\n"
    "buffer_store_dwordx2 v[242:243], v25, s[20:23], 0 offen offset:2048\n"
    "s_endpgm\n"
    ".fill (LV0_%=+12288-.)/4, 4, 0xbf800000\n"
    "LV2_%=:\n"
    "s_nop 0\n"
    "v_add_f32 v64, v64, v160\n"
    "v_add_f32 v65, v65, v161\n"
    "v_mfma_f32_16x16x32_bf16 v[104:107], a[130:133], a[232:235], v[104:107]\n"
    "v_add_f32 v66, v66, v162\n"
    "v_add_f32 v67, v67, v163\n"
    "v_mfma_f32_16x16x32_bf16 v[108:111], a[130:133], a[212:215], v[108:111]\n"
    "v_add_f32 v68, v68, v160\n"
    "v_add_f32 v69, v69, v161\n"
    "v_mfma_f32_16x16x32_bf16 v[112:115], a[130:133], a[192:195], v[112:115]\n"
    "ds_read_b128 a[176:179], v15 offset:21504\n"
    "v_add_f32 v70, v70, v162\n"
    "v_mfma_f32_16x16x32_bf16 v[116:119], a[130:133], a[188:191], v[116:119]\n"
    "s_barrier\n"
    "ds_read_b128 a[184:187], v10 offset:62336\n"
    "v_add_f32 v71, v71, v163\n"
    "v_mfma_f32_16x16x32_bf16 v[120:123], a[130:133], a[196:199], v[120:123]\n"
    "s_waitcnt lgkmcnt(2)\n"
    "ds_read_b128 a[200:203], v10 offset:62464\n"
    "v_add_f32 v72, v72, v164\n"
    "v_mfma_f32_16x16x32_bf16 v[124:127], a[130:133], a[180:183], v[124:127]\n"
    "ds_read_b128 a[204:207], v10 offset:62592\n"
    "v_add_f32 v73, v73, v165\n"
    "v_mfma_f32_16x16x32_bf16 v[96:99], a[138:141], a[228:231], v[96:99]\n"
    "ds_read_b128 a[208:211], v10 offset:62720\n"
    "v_add_f32 v74, v74, v166\n"
    "v_mfma_f32_16x16x32_bf16 v[100:103], a[138:141], a[232:235], v[100:103]\n"
    "v_add_f32 v75, v75, v167\n"
    "v_add_f32 v76, v76, v164\n"
    "v_mfma_f32_16x16x32_bf16 v[104:107], a[138:141], a[212:215], v[104:107]\n"
    "v_add_f32 v77, v77, v165\n"
    "v_add_f32 v78, v78, v166\n"
    "v_mfma_f32_16x16x32_bf16 v[108:111], a[138:141], a[192:195], v[108:111]\n"
    "v_add_f32 v79, v79, v167\n"
    "v_add_f32 v80, v80, v168\n"
    "v_mfma_f32_16x16x32_bf16 v[112:115], a[138:141], a[188:191], v[112:115]\n"
    "ds_read_b128 a[188:191], v10 offset:62848\n"
    "v_add_f32 v81, v81, v169\n"
    "v_mfma_f32_16x16x32_bf16 v[116:119], a[138:141], a[196:199], v[116:119]\n"
    "v_add_f32 v82, v82, v170\n"
    "v_add_f32 v83, v83, v171\n"
    "v_mfma_f32_16x16x32_bf16 v[120:123], a[138:141], a[180:183], v[120:123]\n"
    "s_waitcnt lgkmcnt(5)\n"
    "v_add_f32 v84, v84, v168\n"
    "v_add_f32 v85, v85, v169\n"
    "v_mfma_f32_16x16x32_bf16 v[124:127], a[138:141], a[176:179], v[124:127]\n"
    "s_waitcnt lgkmcnt(4)\n"
    "v_add_f32 v86, v86, v170\n"
    "v_add_f32 v87, v87, v171\n"
    "v_mfma_f32_16x16x32_bf16 v[128:131], a[4:7], a[184:187], v[64:67]\n"
    "s_waitcnt lgkmcnt(3)\n"
    "ds_read_b128 a[176:179], v10 offset:62976\n"
    "v_add_f32 v88, v88, v172\n"
    "v_mfma_f32_16x16x32_bf16 v[132:135], a[4:7], a[200:203], v[68:71]\n"
    "s_waitcnt lgkmcnt(3)\n"
    "v_add_f32 v89, v89, v173\n"
    "v_add_f32 v90, v90, v174\n"
    "v_mfma_f32_16x16x32_bf16 v[136:139], a[4:7], a[204:207], v[72:75]\n"
    "s_waitcnt lgkmcnt(2)\n"
    "v_add_f32 v91, v91, v175\n"
    "v_add_f32 v92, v92, v172\n"
    "v_mfma_f32_16x16x32_bf16 v[140:143], a[4:7], a[208:211], v[76:79]\n"
    "v_add_f32 v93, v93, v173\n"
    "v_add_f32 v94, v94, v174\n"
    "v_mfma_f32_16x16x32_bf16 v[128:131], a[8:11], a[200:203], v[128:131]\n"
    "ds_read_b128 a[180:183], v10 offset:63104\n"
    "v_add_f32 v95, v95, v175\n"
    "v_mfma_f32_16x16x32_bf16 v[132:135], a[8:11], a[204:207], v[132:135]\n"
    "v_add_f32 v112, v112, v80\n"
    "v_add_f32 v113, v113, v81\n"
    "v_mfma_f32_16x16x32_bf16 v[136:139], a[8:11], a[208:211], v[136:139]\n"
    "s_waitcnt lgkmcnt(2)\n"
    "v_add_f32 v114, v114, v82\n"
    "v_add_f32 v115, v115, v83\n"
    "v_mfma_f32_16x16x32_bf16 v[140:143], a[8:11], a[188:191], v[140:143]\n"
    "v_add_f32 v116, v116, v84\n"
    "v_add_f32 v117, v117, v85\n"
    "v_mfma_f32_16x16x32_bf16 v[128:131], a[12:15], a[204:207], v[128:131]\n"
    "ds_read_b128 a[184:187], v11 offset:62208\n"
    "v_add_f32 v118, v118, v86\n"
    "v_mfma_f32_16x16x32_bf16 v[132:135], a[12:15], a[208:211], v[132:135]\n"
    "v_add_f32 v119, v119, v87\n"
    "v_cvt_pk_bf16_f32 v200, v112, v113\n"
    "v_mfma_f32_16x16x32_bf16 v[136:139], a[12:15], a[188:191], v[136:139]\n"
    "s_waitcnt lgkmcnt(2)\n"
    "v_cvt_pk_bf16_f32 v201, v114, v115\n"
    "v_cvt_pk_bf16_f32 v202, v116, v117\n"
    "v_mfma_f32_16x16x32_bf16 v[140:143], a[12:15], a[176:179], v[140:143]\n"
    "v_cvt_pk_bf16_f32 v203, v118, v119\n"
    "v_mfma_f32_16x16x32_bf16 v[128:131], a[16:19], a[208:211], v[128:131]\n"
    "s_xor_b32 s36, s39, 131072\n"
    "ds_read_b128 a[192:195], v11 offset:62336\n"
    "v_min_u32 v200, -2, v200\n"
    "v_mfma_f32_16x16x32_bf16 v[132:135], a[16:19], a[188:191], v[132:135]\n"
    "v_min_u32 v201, -2, v201\n"
    "v_min_u32 v202, -2, v202\n"
    "v_mfma_f32_16x16x32_bf16 v[136:139], a[16:19], a[176:179], v[136:139]\n"
    "s_waitcnt lgkmcnt(2)\n"
    "v_min_u32 v203, -2, v203\n"
    "v_mfma_f32_16x16x32_bf16 v[140:143], a[16:19], a[180:183], v[140:143]\n"
    "s_waitcnt vmcnt(0)\n"
    "v_cmp_ne_u64 vcc, v[28:29], v[26:27]\n"
    "s_cbranch_vccz LF2ok_%=\n"
    "s_mov_b32 s43, 0x100000\n"
    "LF2lp_%=:\n"
    "buffer_load_dwordx2 v[28:29], v23, s[16:19], s40 offen offset:0 sc1\n"
    "s_waitcnt vmcnt(0)\n"
    "s_sub_u32 s43, s43, 1\n"
    "s_cbranch_scc1 LF2ok_%=\n"
    "v_cmp_ne_u64 vcc, v[28:29], v[26:27]\n"
    "s_cbranch_vccnz LF2lp_%=\n"
    "LF2ok_%=:\n"
    "v_mfma_f32_16x16x32_bf16 v[128:131], a[20:23], a[188:191], v[128:131]\n"
    "ds_read_b128 a[188:191], v11 offset:62464\n"
    "v_mfma_f32_16x16x32_bf16 v[132:135], a[20:23], a[176:179], v[132:135]\n"
    "buffer_store_dwordx4 v[200:203], v22, s[16:19], s36 offen sc1\n"
    "v_mfma_f32_16x16x32_bf16 v[136:139], a[20:23], a[180:183], v[136:139]\n"
    "s_waitcnt lgkmcnt(2)\n"
    "v_add_f32 v120, v120, v88\n"
    "v_add_f32 v121, v121, v89\n"
    "v_mfma_f32_16x16x32_bf16 v[140:143], a[20:23], a[184:187], v[140:143]\n"
    "v_add_f32 v122, v122, v90\n"
    "v_add_f32 v123, v123, v91\n"
    "v_mfma_f32_16x16x32_bf16 v[128:131], a[24:27], a[176:179], v[128:131]\n"
    "ds_read_b128 a[176:179], v11 offset:62592\n"
    "v_add_f32 v124, v124, v92\n"
    "v_mfma_f32_16x16x32_bf16 v[132:135], a[24:27], a[180:183], v[132:135]\n"
    "v_add_f32 v125, v125, v93\n"
    "v_add_f32 v126, v126, v94\n"
    "v_mfma_f32_16x16x32_bf16 v[136:139], a[24:27], a[184:187], v[136:139]\n"
    "s_waitcnt lgkmcnt(2)\n"
    "v_add_f32 v127, v127, v95\n"
    "v_cvt_pk_bf16_f32 v204, v120, v121\n"
    "v_mfma_f32_16x16x32_bf16 v[140:143], a[24:27], a[192:195], v[140:143]\n"
    "v_cvt_pk_bf16_f32 v205, v122, v123\n"
    "v_cvt_pk_bf16_f32 v206, v124, v125\n"
    "v_mfma_f32_16x16x32_bf16 v[128:131], a[28:31], a[180:183], v[128:131]\n"
    "ds_read_b128 a[180:183], v11 offset:62720\n"
    "v_cvt_pk_bf16_f32 v207, v126, v127\n"
    "v_mfma_f32_16x16x32_bf16 v[132:135], a[28:31], a[184:187], v[132:135]\n"
    "s_xor_b32 s36, s39, 163840\n"
    "v_min_u32 v204, -2, v204\n"
    "v_min_u32 v205, -2, v205\n"
    "v_mfma_f32_16x16x32_bf16 v[136:139], a[28:31], a[192:195], v[136:139]\n"
    "s_waitcnt lgkmcnt(2)\n"
    "v_min_u32 v206, -2, v206\n"
    "v_min_u32 v207, -2, v207\n"
    "v_mfma_f32_16x16x32_bf16 v[140:143], a[28:31], a[188:191], v[140:143]\n"
    "buffer_store_dwordx4 v[204:207], v22, s[16:19], s36 offen sc1\n"
    "v_mfma_f32_16x16x32_bf16 v[128:131], a[32:35], a[184:187], v[128:131]\n"
    "ds_read_b128 a[184:187], v11 offset:62848\n"
    "v_add_f32 v96, v96, v64\n"
    "v_mfma_f32_16x16x32_bf16 v[132:135], a[32:35], a[192:195], v[132:135]\n"
    "v_add_f32 v97, v97, v65\n"
    "v_add_f32 v98, v98, v66\n"
    "v_mfma_f32_16x16x32_bf16 v[136:139], a[32:35], a[188:191], v[136:139]\n"
    "s_waitcnt lgkmcnt(2)\n"
    "v_add_f32 v99, v99, v67\n"
    "v_add_f32 v100, v100, v68\n"
    "v_mfma_f32_16x16x32_bf16 v[140:143], a[32:35], a[176:179], v[140:143]\n"
    "v_add_f32 v101, v101, v69\n"
    "v_add_f32 v102, v102, v70\n"
    "v_mfma_f32_16x16x32_bf16 v[128:131], a[36:39], a[192:195], v[128:131]\n"
    "ds_read_b128 a[192:195], v11 offset:62976\n"
    "v_add_f32 v103, v103, v71\n"
    "v_mfma_f32_16x16x32_bf16 v[132:135], a[36:39], a[188:191], v[132:135]\n"
    "v_cvt_pk_bf16_f32 v208, v96, v97\n"
    "v_cvt_pk_bf16_f32 v209, v98, v99\n"
    "v_mfma_f32_16x16x32_bf16 v[136:139], a[36:39], a[176:179], v[136:139]\n"
    "s_waitcnt lgkmcnt(2)\n"
    "v_cvt_pk_bf16_f32 v210, v100, v101\n"
    "v_cvt_pk_bf16_f32 v211, v102, v103\n"
    "v_mfma_f32_16x16x32_bf16 v[140:143], a[36:39], a[180:183], v[140:143]\n"
    "s_xor_b32 s36, s39, 196608\n"
    "v_min_u32 v208, -2, v208\n"
    "v_min_u32 v209, -2, v209\n"
    "v_mfma_f32_16x16x32_bf16 v[128:131], a[40:43], a[188:191], v[128:131]\n"
    "ds_read_b128 a[188:191], v11 offset:63104\n"
    "v_min_u32 v210, -2, v210\n"
    "v_mfma_f32_16x16x32_bf16 v[132:135], a[40:43], a[176:179], v[132:135]\n"
    "v_min_u32 v211, -2, v211\n"
    "v_mfma_f32_16x16x32_bf16 v[136:139], a[40:43], a[180:183], v[136:139]\n"
    "s_waitcnt lgkmcnt(2)\n"
    "buffer_store_dwordx4 v[208:211], v22, s[16:19], s36 offen sc1\n"
    "v_mfma_f32_16x16x32_bf16 v[140:143], a[40:43], a[184:187], v[140:143]\n"
    "v_add_f32 v104, v104, v72\n"
    "v_add_f32 v105, v105, v73\n"
    "v_mfma_f32_16x16x32_bf16 v[128:131], a[44:47], a[176:179], v[128:131]\n"
    "ds_read_b128 a[176:179], v12 offset:62208\n"
    "v_add_f32 v106, v106, v74\n"
    "v_mfma_f32_16x16x32_bf16 v[132:135], a[44:47], a[180:183], v[132:135]\n"
    "v_add_f32 v107, v107, v75\n"
    "v_add_f32 v108, v108, v76\n"
    "v_mfma_f32_16x16x32_bf16 v[136:139], a[44:47], a[184:187], v[136:139]\n"
    "s_waitcnt lgkmcnt(2)\n"
    "v_add_f32 v109, v109, v77\n"
    "v_add_f32 v110, v110, v78\n"
    "v_mfma_f32_16x16x32_bf16 v[140:143], a[44:47], a[192:195], v[140:143]\n"
    "v_add_f32 v111, v111, v79\n"
    "v_cvt_pk_bf16_f32 v212, v104, v105\n"
    "v_mfma_f32_16x16x32_bf16 v[128:131], a[48:51], a[180:183], v[128:131]\n"
    "ds_read_b128 a[180:183], v12 offset:62336\n"
    "v_cvt_pk_bf16_f32 v213, v106, v107\n"
    "v_mfma_f32_16x16x32_bf16 v[132:135], a[48:51], a[184:187], v[132:135]\n"
    "v_cvt_pk_bf16_f32 v214, v108, v109\n"
    "v_cvt_pk_bf16_f32 v215, v110, v111\n"
    "v_mfma_f32_16x16x32_bf16 v[136:139], a[48:51], a[192:195], v[136:139]\n"
    "s_waitcnt lgkmcnt(2)\n"
    "s_xor_b32 s36, s39, 229376\n"
    "v_min_u32 v212, -2, v212\n"
    "v_mfma_f32_16x16x32_bf16 v[140:143], a[48:51], a[188:191], v[140:143]\n"
    "v_min_u32 v213, -2, v213\n"
    "v_min_u32 v214, -2, v214\n"
    "v_mfma_f32_16x16x32_bf16 v[128:131], a[52:55], a[184:187], v[128:131]\n"
    "ds_read_b128 a[184:187], v12 offset:62464\n"
    "v_min_u32 v215, -2, v215\n"
    "v_mfma_f32_16x16x32_bf16 v[132:135], a[52:55], a[192:195], v[132:135]\n"
    "ds_read_b128 a[196:199], v13 offset:62336\n"
    "v_mfma_f32_16x16x32_bf16 v[136:139], a[52:55], a[188:191], v[136:139]\n"
    "s_waitcnt lgkmcnt(3)\n"
    "ds_read_b128 a[200:203], v13 offset:62464\n"
    "v_mfma_f32_16x16x32_bf16 v[140:143], a[52:55], a[176:179], v[140:143]\n"
    "ds_read_b128 a[204:207], v13 offset:62592\n"
    "v_mfma_f32_16x16x32_bf16 v[128:131], a[56:59], a[192:195], v[128:131]\n"
    "ds_read_b128 a[192:195], v13 offset:62720\n"
    "v_mfma_f32_16x16x32_bf16 v[132:135], a[56:59], a[188:191], v[132:135]\n"
    "buffer_store_dwordx4 v[212:215], v22, s[16:19], s36 offen sc1\n"
    "v_mfma_f32_16x16x32_bf16 v[136:139], a[56:59], a[176:179], v[136:139]\n"
    "s_waitcnt lgkmcnt(5)\n"
    "v_mfma_f32_16x16x32_bf16 v[140:143], a[56:59], a[180:183], v[140:143]\n"
    "v_mfma_f32_16x16x32_bf16 v[128:131], a[60:63], a[188:191], v[128:131]\n"
    "ds_read_b128 a[188:191], v13 offset:62848\n"
    "v_mfma_f32_16x16x32_bf16 v[132:135], a[60:63], a[176:179], v[132:135]\n"
    "v_mfma_f32_16x16x32_bf16 v[136:139], a[60:63], a[180:183], v[136:139]\n"
    "s_waitcnt lgkmcnt(5)\n"
    "v_mfma_f32_16x16x32_bf16 v[140:143], a[60:63], a[184:187], v[140:143]\n"
    "s_waitcnt lgkmcnt(4)\n"
    "v_mfma_f32_16x16x32_bf16 v[128:131], a[68:71], a[196:199], v[128:131]\n"
    "s_waitcnt lgkmcnt(3)\n"
    "ds_read_b128 a[176:179], v13 offset:62976\n"
    "v_mfma_f32_16x16x32_bf16 v[132:135], a[68:71], a[200:203], v[132:135]\n"
    "s_waitcnt lgkmcnt(3)\n"
    "v_mfma_f32_16x16x32_bf16 v[136:139], a[68:71], a[204:207], v[136:139]\n"
    "s_waitcnt lgkmcnt(2)\n"
    "v_mfma_f32_16x16x32_bf16 v[140:143], a[68:71], a[192:195], v[140:143]\n"
    "v_mfma_f32_16x16x32_bf16 v[128:131], a[72:75], a[200:203], v[128:131]\n"
    "ds_read_b128 a[180:183], v13 offset:63104\n"
    "v_mfma_f32_16x16x32_bf16 v[132:135], a[72:75], a[204:207], v[132:135]\n"
    "v_mfma_f32_16x16x32_bf16 v[136:139], a[72:75], a[192:195], v[136:139]\n"
    "s_waitcnt lgkmcnt(2)\n"
    "v_mfma_f32_16x16x32_bf16 v[140:143], a[72:75], a[188:191], v[140:143]\n"
    "v_mfma_f32_16x16x32_bf16 v[128:131], a[76:79], a[204:207], v[128:131]\n"
    "ds_read_b128 a[184:187], v14 offset:62208\n"
    "v_mfma_f32_16x16x32_bf16 v[132:135], a[76:79], a[192:195], v[132:135]\n"
    "v_mfma_f32_16x16x32_bf16 v[136:139], a[76:79], a[188:191], v[136:139]\n"
    "s_waitcnt lgkmcnt(2)\n"
    "v_mfma_f32_16x16x32_bf16 v[140:143], a[76:79], a[176:179], v[140:143]\n"
    "v_mfma_f32_16x16x32_bf16 v[128:131], a[80:83], a[192:195], v[128:131]\n"
    "ds_read_b128 a[192:195], v14 offset:62336\n"
    "v_mfma_f32_16x16x32_bf16 v[132:135], a[80:83], a[188:191], v[132:135]\n"
    "v_mfma_f32_16x16x32_bf16 v[136:139], a[80:83], a[176:179], v[136:139]\n"
    "s_waitcnt lgkmcnt(2)\n"
    "v_mfma_f32_16x16x32_bf16 v[140:143], a[80:83], a[180:183], v[140:143]\n"
    "v_mfma_f32_16x16x32_bf16 v[128:131], a[84:87], a[188:191], v[128:131]\n"
    "ds_read_b128 a[188:191], v14 offset:62464\n"
    "v_mfma_f32_16x16x32_bf16 v[132:135], a[84:87], a[176:179], v[132:135]\n"
    "v_mfma_f32_16x16x32_bf16 v[136:139], a[84:87], a[180:183], v[136:139]\n"
    "s_waitcnt lgkmcnt(2)\n"
    "v_mfma_f32_16x16x32_bf16 v[140:143], a[84:87], a[184:187], v[140:143]\n"
    "v_mfma_f32_16x16x32_bf16 v[128:131], a[88:91], a[176:179], v[128:131]\n"
    "ds_read_b128 a[176:179], v14 offset:62592\n"
    "v_mfma_f32_16x16x32_bf16 v[132:135], a[88:91], a[180:183], v[132:135]\n"
    "v_mfma_f32_16x16x32_bf16 v[136:139], a[88:91], a[184:187], v[136:139]\n"
    "s_waitcnt lgkmcnt(2)\n"
    "v_mfma_f32_16x16x32_bf16 v[140:143], a[88:91], a[192:195], v[140:143]\n"
    "v_mfma_f32_16x16x32_bf16 v[128:131], a[92:95], a[180:183], v[128:131]\n"
    "ds_read_b128 a[180:183], v14 offset:62720\n"
    "v_mfma_f32_16x16x32_bf16 v[132:135], a[92:95], a[184:187], v[132:135]\n"
    "v_mfma_f32_16x16x32_bf16 v[136:139], a[92:95], a[192:195], v[136:139]\n"
    "s_waitcnt lgkmcnt(2)\n"
    "v_mfma_f32_16x16x32_bf16 v[140:143], a[92:95], a[188:191], v[140:143]\n"
    "v_mfma_f32_16x16x32_bf16 v[128:131], a[96:99], a[184:187], v[128:131]\n"
    "ds_read_b128 a[184:187], v14 offset:62848\n"
    "v_mfma_f32_16x16x32_bf16 v[132:135], a[96:99], a[192:195], v[132:135]\n"
    "v_mfma_f32_16x16x32_bf16 v[136:139], a[96:99], a[188:191], v[136:139]\n"
    "s_waitcnt lgkmcnt(2)\n"
    "v_mfma_f32_16x16x32_bf16 v[140:143], a[96:99], a[176:179], v[140:143]\n"
    "v_mfma_f32_16x16x32_bf16 v[128:131], a[100:103], a[192:195], v[128:131]\n"
    "ds_read_b128 a[192:195], v14 offset:62976\n"
    "v_mfma_f32_16x16x32_bf16 v[132:135], a[100:103], a[188:191], v[132:135]\n"
    "v_mfma_f32_16x16x32_bf16 v[136:139], a[100:103], a[176:179], v[136:139]\n"
    "s_waitcnt lgkmcnt(2)\n"
    "v_mfma_f32_16x16x32_bf16 v[140:143], a[100:103], a[180:183], v[140:143]\n"
    "v_mfma_f32_16x16x32_bf16 v[128:131], a[104:107], a[188:191], v[128:131]\n"
    "ds_read_b128 a[188:191], v14 offset:63104\n"
    "v_mfma_f32_16x16x32_bf16 v[132:135], a[104:107], a[176:179], v[132:135]\n"
    "v_mfma_f32_16x16x32_bf16 v[136:139], a[104:107], a[180:183], v[136:139]\n"
    "s_waitcnt lgkmcnt(2)\n"
    "v_mfma_f32_16x16x32_bf16 v[140:143], a[104:107], a[184:187], v[140:143]\n"
    "v_mfma_f32_16x16x32_bf16 v[128:131], a[108:111], a[176:179], v[128:131]\n"
    "ds_read_b128 a[176:179], v15 offset:62208\n"
    "v_mfma_f32_16x16x32_bf16 v[132:135], a[108:111], a[180:183], v[132:135]\n"
    "v_mfma_f32_16x16x32_bf16 v[136:139], a[108:111], a[184:187], v[136:139]\n"
    "s_waitcnt lgkmcnt(2)\n"
    "v_mfma_f32_16x16x32_bf16 v[140:143], a[108:111], a[192:195], v[140:143]\n"
    "v_mfma_f32_16x16x32_bf16 v[128:131], a[112:115], a[180:183], v[128:131]\n"
    "ds_read_b128 a[180:183], v15 offset:62336\n"
    "v_mfma_f32_16x16x32_bf16 v[132:135], a[112:115], a[184:187], v[132:135]\n"
    "buffer_load_dwordx4 v[160:163], v22, s[16:19], s28 offen sc1\n"
    "v_mfma_f32_16x16x32_bf16 v[136:139], a[112:115], a[192:195], v[136:139]\n"
    "s_waitcnt lgkmcnt(2)\n"
    "v_mfma_f32_16x16x32_bf16 v[140:143], a[112:115], a[188:191], v[140:143]\n"
    "v_mfma_f32_16x16x32_bf16 v[128:131], a[116:119], a[184:187], v[128:131]\n"
    "ds_read_b128 a[184:187], v15 offset:62464\n"
    "v_mfma_f32_16x16x32_bf16 v[132:135], a[116:119], a[192:195], v[132:135]\n"
    "ds_read_b128 a[196:199], v10 offset:63104\n"
    "v_mfma_f32_16x16x32_bf16 v[136:139], a[116:119], a[188:191], v[136:139]\n"
    "s_waitcnt lgkmcnt(3)\n"
    "ds_read_b128 a[200:203], v11 offset:62208\n"
    "v_mfma_f32_16x16x32_bf16 v[140:143], a[116:119], a[176:179], v[140:143]\n"
    "buffer_load_dwordx4 v[164:167], v22, s[16:19], s30 offen sc1\n"
    "v_mfma_f32_16x16x32_bf16 v[128:131], a[120:123], a[192:195], v[128:131]\n"
    "ds_read_b128 a[192:195], v11 offset:62336\n"
    "v_mfma_f32_16x16x32_bf16 v[132:135], a[120:123], a[188:191], v[132:135]\n"
    "v_mfma_f32_16x16x32_bf16 v[136:139], a[120:123], a[176:179], v[136:139]\n"
    "s_waitcnt lgkmcnt(4)\n"
    "ds_read_b128 a[204:207], v11 offset:62464\n"
    "v_mfma_f32_16x16x32_bf16 v[140:143], a[120:123], a[180:183], v[140:143]\n"
    "buffer_load_dwordx4 v[168:171], v22, s[16:19], s29 offen sc1\n"
    "v_mfma_f32_16x16x32_bf16 v[128:131], a[124:127], a[188:191], v[128:131]\n"
    "ds_read_b128 a[188:191], v11 offset:62592\n"
    "v_mfma_f32_16x16x32_bf16 v[132:135], a[124:127], a[176:179], v[132:135]\n"
    "v_mfma_f32_16x16x32_bf16 v[136:139], a[124:127], a[180:183], v[136:139]\n"
    "s_waitcnt lgkmcnt(5)\n"
    "ds_read_b128 a[176:179], v11 offset:62720\n"
    "v_mfma_f32_16x16x32_bf16 v[140:143], a[124:127], a[184:187], v[140:143]\n"
    "s_waitcnt lgkmcnt(5)\n"
    "buffer_load_dwordx4 v[172:175], v22, s[16:19], s31 offen sc1\n"
    "v_mfma_f32_16x16x32_bf16 v[152:155], a[4:7], a[196:199], v[88:91]\n"
    "s_waitcnt lgkmcnt(4)\n"
    "ds_read_b128 a[180:183], v11 offset:62848\n"
    "v_mfma_f32_16x16x32_bf16 v[156:159], a[4:7], a[200:203], v[92:95]\n"
    "v_mfma_f32_16x16x32_bf16 v[152:155], a[8:11], a[200:203], v[152:155]\n"
    "s_waitcnt lgkmcnt(4)\n"
    "ds_read_b128 a[184:187], v11 offset:62976\n"
    "v_mfma_f32_16x16x32_bf16 v[156:159], a[8:11], a[192:195], v[156:159]\n"
    "v_cvt_pk_bf16_f32 v216, v128, v129\n"
    "v_cvt_pk_bf16_f32 v217, v130, v131\n"
    "v_mfma_f32_16x16x32_bf16 v[152:155], a[12:15], a[192:195], v[152:155]\n"
    "s_waitcnt lgkmcnt(4)\n"
    "ds_read_b128 a[192:195], v11 offset:63104\n"
    "v_cvt_pk_bf16_f32 v218, v132, v133\n"
    "v_mfma_f32_16x16x32_bf16 v[156:159], a[12:15], a[204:207], v[156:159]\n"
    "v_cvt_pk_bf16_f32 v219, v134, v135\n"
    "v_mfma_f32_16x16x32_bf16 v[152:155], a[16:19], a[204:207], v[152:155]\n"
    "s_waitcnt lgkmcnt(4)\n"
    "s_xor_b32 s24, s39, 65536\n"
    "ds_read_b128 a[196:199], v12 offset:62208\n"
    "v_mfma_f32_16x16x32_bf16 v[156:159], a[16:19], a[188:191], v[156:159]\n"
    "v_min_u32 v216, -2, v216\n"
    "v_min_u32 v217, -2, v217\n"
    "v_mfma_f32_16x16x32_bf16 v[152:155], a[20:23], a[188:191], v[152:155]\n"
    "s_waitcnt lgkmcnt(4)\n"
    "ds_read_b128 a[188:191], v12 offset:62336\n"
    "v_min_u32 v218, -2, v218\n"
    "v_mfma_f32_16x16x32_bf16 v[156:159], a[20:23], a[176:179], v[156:159]\n"
    "v_min_u32 v219, -2, v219\n"
    "v_mfma_f32_16x16x32_bf16 v[152:155], a[24:27], a[176:179], v[152:155]\n"
    "s_waitcnt lgkmcnt(4)\n"
    "ds_read_b128 a[176:179], v12 offset:62464\n"
    "v_mfma_f32_16x16x32_bf16 v[156:159], a[24:27], a[180:183], v[156:159]\n"
    "buffer_store_dwordx4 v[216:219], v22, s[16:19], s24 offen sc1\n"
    "v_mfma_f32_16x16x32_bf16 v[152:155], a[28:31], a[180:183], v[152:155]\n"
    "s_waitcnt lgkmcnt(4)\n"
    "ds_read_b128 a[180:183], v12 offset:62592\n"
    "v_cvt_pk_bf16_f32 v220, v136, v137\n"
    "v_mfma_f32_16x16x32_bf16 v[156:159], a[28:31], a[184:187], v[156:159]\n"
    "v_cvt_pk_bf16_f32 v221, v138, v139\n"
    "v_cvt_pk_bf16_f32 v222, v140, v141\n"
    "v_mfma_f32_16x16x32_bf16 v[152:155], a[32:35], a[184:187], v[152:155]\n"
    "s_waitcnt lgkmcnt(4)\n"
    "ds_read_b128 a[184:187], v12 offset:62720\n"
    "v_cvt_pk_bf16_f32 v223, v142, v143\n"
    "v_mfma_f32_16x16x32_bf16 v[156:159], a[32:35], a[192:195], v[156:159]\n"
    "s_xor_b32 s24, s39, 98304\n"
    "v_min_u32 v220, -2, v220\n"
    "v_min_u32 v221, -2, v221\n"
    "v_mfma_f32_16x16x32_bf16 v[152:155], a[36:39], a[192:195], v[152:155]\n"
    "s_waitcnt lgkmcnt(4)\n"
    "ds_read_b128 a[192:195], v12 offset:62848\n"
    "v_min_u32 v222, -2, v222\n"
    "v_mfma_f32_16x16x32_bf16 v[156:159], a[36:39], a[196:199], v[156:159]\n"
    "v_min_u32 v223, -2, v223\n"
    "v_mfma_f32_16x16x32_bf16 v[152:155], a[40:43], a[196:199], v[152:155]\n"
    "s_waitcnt lgkmcnt(4)\n"
    "ds_read_b128 a[196:199], v12 offset:62976\n"
    "v_mfma_f32_16x16x32_bf16 v[156:159], a[40:43], a[188:191], v[156:159]\n"
    "ds_read_b128 a[200:203], v13 offset:63104\n"
    "v_mfma_f32_16x16x32_bf16 v[152:155], a[44:47], a[188:191], v[152:155]\n"
    "s_waitcnt lgkmcnt(5)\n"
    "ds_read_b128 a[188:191], v14 offset:62208\n"
    "v_mfma_f32_16x16x32_bf16 v[156:159], a[44:47], a[176:179], v[156:159]\n"
    "buffer_store_dwordx4 v[220:223], v22, s[16:19], s24 offen sc1\n"
    "v_mfma_f32_16x16x32_bf16 v[152:155], a[48:51], a[176:179], v[152:155]\n"
    "s_waitcnt lgkmcnt(5)\n"
    "ds_read_b128 a[176:179], v14 offset:62336\n"
    "v_mfma_f32_16x16x32_bf16 v[156:159], a[48:51], a[180:183], v[156:159]\n"
    "v_mfma_f32_16x16x32_bf16 v[152:155], a[52:55], a[180:183], v[152:155]\n"
    "s_waitcnt lgkmcnt(5)\n"
    "ds_read_b128 a[180:183], v14 offset:62464\n"
    "v_mfma_f32_16x16x32_bf16 v[156:159], a[52:55], a[184:187], v[156:159]\n"
    "v_mfma_f32_16x16x32_bf16 v[152:155], a[56:59], a[184:187], v[152:155]\n"
    "s_waitcnt lgkmcnt(5)\n"
    "ds_read_b128 a[184:187], v14 offset:62592\n"
    "v_mfma_f32_16x16x32_bf16 v[156:159], a[56:59], a[192:195], v[156:159]\n"
    "v_mfma_f32_16x16x32_bf16 v[152:155], a[60:63], a[192:195], v[152:155]\n"
    "s_waitcnt lgkmcnt(5)\n"
    "ds_read_b128 a[192:195], v14 offset:62720\n"
    "v_mfma_f32_16x16x32_bf16 v[156:159], a[60:63], a[196:199], v[156:159]\n"
    "s_waitcnt lgkmcnt(5)\n"
    "v_mfma_f32_16x16x32_bf16 v[152:155], a[68:71], a[200:203], v[152:155]\n"
    "s_waitcnt lgkmcnt(4)\n"
    "ds_read_b128 a[196:199], v14 offset:62848\n"
    "v_mfma_f32_16x16x32_bf16 v[156:159], a[68:71], a[188:191], v[156:159]\n"
    "v_mfma_f32_16x16x32_bf16 v[152:155], a[72:75], a[188:191], v[152:155]\n"
    "s_waitcnt lgkmcnt(4)\n"
    "ds_read_b128 a[188:191], v14 offset:62976\n"
    "v_mfma_f32_16x16x32_bf16 v[156:159], a[72:75], a[176:179], v[156:159]\n"
    "v_mfma_f32_16x16x32_bf16 v[152:155], a[76:79], a[176:179], v[152:155]\n"
    "s_waitcnt lgkmcnt(4)\n"
    "ds_read_b128 a[176:179], v14 offset:63104\n"
    "v_mfma_f32_16x16x32_bf16 v[156:159], a[76:79], a[180:183], v[156:159]\n"
    "v_mfma_f32_16x16x32_bf16 v[152:155], a[80:83], a[180:183], v[152:155]\n"
    "s_waitcnt lgkmcnt(4)\n"
    "ds_read_b128 a[180:183], v15 offset:62208\n"
    "v_mfma_f32_16x16x32_bf16 v[156:159], a[80:83], a[184:187], v[156:159]\n"
    "v_mfma_f32_16x16x32_bf16 v[152:155], a[84:87], a[184:187], v[152:155]\n"
    "s_waitcnt lgkmcnt(4)\n"
    "ds_read_b128 a[184:187], v15 offset:62336\n"
    "v_mfma_f32_16x16x32_bf16 v[156:159], a[84:87], a[192:195], v[156:159]\n"
    "v_mfma_f32_16x16x32_bf16 v[152:155], a[88:91], a[192:195], v[152:155]\n"
    "s_waitcnt lgkmcnt(4)\n"
    "ds_read_b128 a[192:195], v15 offset:62464\n"
    "v_mfma_f32_16x16x32_bf16 v[156:159], a[88:91], a[196:199], v[156:159]\n"
    "v_mfma_f32_16x16x32_bf16 v[152:155], a[92:95], a[196:199], v[152:155]\n"
    "s_waitcnt lgkmcnt(4)\n"
    "ds_read_b128 a[196:199], v15 offset:62592\n"
    "v_mfma_f32_16x16x32_bf16 v[156:159], a[92:95], a[188:191], v[156:159]\n"
    "v_mfma_f32_16x16x32_bf16 v[152:155], a[96:99], a[188:191], v[152:155]\n"
    "s_waitcnt lgkmcnt(4)\n"
    "ds_read_b128 a[188:191], v15 offset:62720\n"
    "v_mfma_f32_16x16x32_bf16 v[156:159], a[96:99], a[176:179], v[156:159]\n"
    "v_mfma_f32_16x16x32_bf16 v[152:155], a[100:103], a[176:179], v[152:155]\n"
    "s_waitcnt lgkmcnt(4)\n"
    "ds_read_b128 a[176:179], v15 offset:62848\n"
    "v_mfma_f32_16x16x32_bf16 v[156:159], a[100:103], a[180:183], v[156:159]\n"
    "v_mfma_f32_16x16x32_bf16 v[152:155], a[104:107], a[180:183], v[152:155]\n"
    "s_waitcnt lgkmcnt(4)\n"
    "ds_read_b128 a[180:183], v15 offset:62976\n"
    "v_mfma_f32_16x16x32_bf16 v[156:159], a[104:107], a[184:187], v[156:159]\n"
    "ds_read_b128 a[200:203], v10 offset:62848\n"
    "v_mfma_f32_16x16x32_bf16 v[152:155], a[108:111], a[184:187], v[152:155]\n"
    "s_waitcnt lgkmcnt(5)\n"
    "ds_read_b128 a[184:187], v10 offset:62976\n"
    "v_mfma_f32_16x16x32_bf16 v[156:159], a[108:111], a[192:195], v[156:159]\n"
    "v_mfma_f32_16x16x32_bf16 v[152:155], a[112:115], a[192:195], v[152:155]\n"
    "s_waitcnt lgkmcnt(5)\n"
    "ds_read_b128 a[192:195], v10 offset:63104\n"
    "v_mfma_f32_16x16x32_bf16 v[156:159], a[112:115], a[196:199], v[156:159]\n"
    "v_mfma_f32_16x16x32_bf16 v[152:155], a[116:119], a[196:199], v[152:155]\n"
    "s_waitcnt lgkmcnt(5)\n"
    "ds_read_b128 a[196:199], v11 offset:62208\n"
    "v_mfma_f32_16x16x32_bf16 v[156:159], a[116:119], a[188:191], v[156:159]\n"
    "v_mfma_f32_16x16x32_bf16 v[152:155], a[120:123], a[188:191], v[152:155]\n"
    "s_waitcnt lgkmcnt(5)\n"
    "ds_read_b128 a[188:191], v11 offset:62336\n"
    "v_mfma_f32_16x16x32_bf16 v[156:159], a[120:123], a[176:179], v[156:159]\n"
    "v_mfma_f32_16x16x32_bf16 v[152:155], a[124:127], a[176:179], v[152:155]\n"
    "s_waitcnt lgkmcnt(5)\n"
    "ds_read_b128 a[176:179], v11 offset:62464\n"
    "v_mfma_f32_16x16x32_bf16 v[156:159], a[124:127], a[180:183], v[156:159]\n"
    "s_waitcnt lgkmcnt(5)\n"
    "v_mfma_f32_16x16x32_bf16 v[144:147], a[4:7], a[200:203], v[80:83]\n"
    "s_waitcnt lgkmcnt(4)\n"
    "ds_read_b128 a[180:183], v11 offset:62592\n"
    "v_mfma_f32_16x16x32_bf16 v[148:151], a[4:7], a[184:187], v[84:87]\n"
    "v_mfma_f32_16x16x32_bf16 v[144:147], a[8:11], a[184:187], v[144:147]\n"
    "s_waitcnt lgkmcnt(4)\n"
    "ds_read_b128 a[184:187], v11 offset:62720\n"
    "v_mfma_f32_16x16x32_bf16 v[148:151], a[8:11], a[192:195], v[148:151]\n"
    "v_cvt_pk_bf16_f32 v224, v152, v153\n"
    "v_cvt_pk_bf16_f32 v225, v154, v155\n"
    "v_mfma_f32_16x16x32_bf16 v[144:147], a[12:15], a[192:195], v[144:147]\n"
    "s_waitcnt lgkmcnt(4)\n"
    "ds_read_b128 a[192:195], v11 offset:62848\n"
    "v_cvt_pk_bf16_f32 v226, v156, v157\n"
    "v_mfma_f32_16x16x32_bf16 v[148:151], a[12:15], a[196:199], v[148:151]\n"
    "v_cvt_pk_bf16_f32 v227, v158, v159\n"
    "v_mfma_f32_16x16x32_bf16 v[144:147], a[16:19], a[196:199], v[144:147]\n"
    "s_waitcnt lgkmcnt(4)\n"
    "s_xor_b32 s24, s39, 32768\n"
    "ds_read_b128 a[196:199], v11 offset:62976\n"
    "v_mfma_f32_16x16x32_bf16 v[148:151], a[16:19], a[188:191], v[148:151]\n"
    "v_min_u32 v224, -2, v224\n"
    "v_min_u32 v225, -2, v225\n"
    "v_mfma_f32_16x16x32_bf16 v[144:147], a[20:23], a[188:191], v[144:147]\n"
    "s_waitcnt lgkmcnt(4)\n"
    "ds_read_b128 a[188:191], v11 offset:63104\n"
    "v_min_u32 v226, -2, v226\n"
    "v_mfma_f32_16x16x32_bf16 v[148:151], a[20:23], a[176:179], v[148:151]\n"
    "v_min_u32 v227, -2, v227\n"
    "v_mfma_f32_16x16x32_bf16 v[144:147], a[24:27], a[176:179], v[144:147]\n"
    "s_waitcnt lgkmcnt(4)\n"
    "ds_read_b128 a[176:179], v12 offset:62208\n"
    "v_mfma_f32_16x16x32_bf16 v[148:151], a[24:27], a[180:183], v[148:151]\n"
    "buffer_store_dwordx4 v[224:227], v22, s[16:19], s24 offen sc1\n"
    "v_mfma_f32_16x16x32_bf16 v[144:147], a[28:31], a[180:183], v[144:147]\n"
    "s_waitcnt lgkmcnt(4)\n"
    "ds_read_b128 a[180:183], v12 offset:62336\n"
    "v_mfma_f32_16x16x32_bf16 v[148:151], a[28:31], a[184:187], v[148:151]\n"
    "v_mfma_f32_16x16x32_bf16 v[144:147], a[32:35], a[184:187], v[144:147]\n"
    "s_waitcnt lgkmcnt(4)\n"
    "ds_read_b128 a[184:187], v12 offset:62464\n"
    "v_mfma_f32_16x16x32_bf16 v[148:151], a[32:35], a[192:195], v[148:151]\n"
    "v_mfma_f32_16x16x32_bf16 v[144:147], a[36:39], a[192:195], v[144:147]\n"
    "s_waitcnt lgkmcnt(4)\n"
    "ds_read_b128 a[192:195], v12 offset:62592\n"
    "v_mfma_f32_16x16x32_bf16 v[148:151], a[36:39], a[196:199], v[148:151]\n"
    "v_mfma_f32_16x16x32_bf16 v[144:147], a[40:43], a[196:199], v[144:147]\n"
    "s_waitcnt lgkmcnt(4)\n"
    "ds_read_b128 a[196:199], v12 offset:62720\n"
    "v_mfma_f32_16x16x32_bf16 v[148:151], a[40:43], a[188:191], v[148:151]\n"
    "ds_read_b128 a[200:203], v13 offset:62848\n"
    "v_mfma_f32_16x16x32_bf16 v[144:147], a[44:47], a[188:191], v[144:147]\n"
    "s_waitcnt lgkmcnt(5)\n"
    "s_waitcnt vmcnt(3)\n"
    "ds_read_b128 a[188:191], v13 offset:62976\n"
    "v_mfma_f32_16x16x32_bf16 v[148:151], a[44:47], a[176:179], v[148:151]\n"
    "v_max3_u32 v240, v160, v161, v162\n"
    "v_max3_u32 v240, v240, v163, v164\n"
    "v_mfma_f32_16x16x32_bf16 v[144:147], a[48:51], a[176:179], v[144:147]\n"
    "s_waitcnt lgkmcnt(5)\n"
    "ds_read_b128 a[176:179], v13 offset:63104\n"
    "v_max3_u32 v240, v240, v165, v166\n"
    "v_mfma_f32_16x16x32_bf16 v[148:151], a[48:51], a[180:183], v[148:151]\n"
    "buffer_load_dwordx4 v[176:179], v22, s[16:19], s26 offen sc1\n"
    "v_mfma_f32_16x16x32_bf16 v[144:147], a[52:55], a[180:183], v[144:147]\n"
    "s_waitcnt lgkmcnt(5)\n"
    "ds_read_b128 a[180:183], v14 offset:62208\n"
    "v_max3_u32 v240, v240, v167, v168\n"
    "v_mfma_f32_16x16x32_bf16 v[148:151], a[52:55], a[184:187], v[148:151]\n"
    "v_max3_u32 v240, v240, v169, v170\n"
    "v_max3_u32 v240, v240, v171, v172\n"
    "v_mfma_f32_16x16x32_bf16 v[144:147], a[56:59], a[184:187], v[144:147]\n"
    "s_waitcnt lgkmcnt(5)\n"
    "ds_read_b128 a[184:187], v14 offset:62336\n"
    "v_max3_u32 v240, v240, v173, v174\n"
    "v_mfma_f32_16x16x32_bf16 v[148:151], a[56:59], a[192:195], v[148:151]\n"
    "buffer_load_dwordx4 v[180:183], v22, s[16:19], s27 offen sc1\n"
    "v_mfma_f32_16x16x32_bf16 v[144:147], a[60:63], a[192:195], v[144:147]\n"
    "s_waitcnt lgkmcnt(5)\n"
    "ds_read_b128 a[192:195], v14 offset:62464\n"
    "v_max_u32 v240, v240, v175\n"
    "v_mfma_f32_16x16x32_bf16 v[148:151], a[60:63], a[196:199], v[148:151]\n"
    "s_waitcnt lgkmcnt(5)\n"
    "v_cmp_eq_u32 vcc, -1, v240\n"
    "s_cbranch_vccz LE2ok_%=\n"
    "s_mov_b32 s43, 0x100000\n"
    "LE2lp_%=:\n"
    "buffer_load_dwordx4 v[160:163], v22, s[16:19], s28 offen sc1\n"
    "buffer_load_dwordx4 v[164:167], v22, s[16:19], s30 offen sc1\n"
    "buffer_load_dwordx4 v[168:171], v22, s[16:19], s29 offen sc1\n"
    "buffer_load_dwordx4 v[172:175], v22, s[16:19], s31 offen sc1\n"
    "s_waitcnt vmcnt(0)\n"
    "s_sub_u32 s43, s43, 1\n"
    "s_cbranch_scc1 LE2ok_%=\n"
    "v_max3_u32 v240, v160, v161, v162\n"
    "v_max3_u32 v240, v240, v163, v164\n"
    "v_max3_u32 v240, v240, v165, v166\n"
    "v_max3_u32 v240, v240, v167, v168\n"
    "v_max3_u32 v240, v240, v169, v170\n"
    "v_max3_u32 v240, v240, v171, v172\n"
    "v_max3_u32 v240, v240, v173, v174\n"
    "v_max_u32 v240, v240, v175\n"
    "v_cmp_eq_u32 vcc, -1, v240\n"
    "s_cbranch_vccnz LE2lp_%=\n"
    "LE2ok_%=:\n"
    "v_mfma_f32_16x16x32_bf16 v[144:147], a[68:71], a[200:203], v[144:147]\n"
    "s_waitcnt lgkmcnt(4)\n"
    "ds_read_b128 a[196:199], v14 offset:62592\n"
    "v_lshlrev_b32 v188, 16, v160\n"
    "v_mfma_f32_16x16x32_bf16 v[148:151], a[68:71], a[188:191], v[148:151]\n"
    "v_and_b32 v189, s48, v160\n"
    "v_lshlrev_b32 v190, 16, v161\n"
    "v_mfma_f32_16x16x32_bf16 v[144:147], a[72:75], a[188:191], v[144:147]\n"
    "s_waitcnt lgkmcnt(4)\n"
    "ds_read_b128 a[188:191], v14 offset:62720\n"
    "v_and_b32 v191, s48, v161\n"
    "v_mfma_f32_16x16x32_bf16 v[148:151], a[72:75], a[176:179], v[148:151]\n"
    "v_lshlrev_b32 v192, 16, v162\n"
    "v_and_b32 v193, s48, v162\n"
    "v_mfma_f32_16x16x32_bf16 v[144:147], a[76:79], a[176:179], v[144:147]\n"
    "s_waitcnt lgkmcnt(4)\n"
    "ds_read_b128 a[176:179], v14 offset:62848\n"
    "v_lshlrev_b32 v194, 16, v163\n"
    "v_mfma_f32_16x16x32_bf16 v[148:151], a[76:79], a[180:183], v[148:151]\n"
    "v_and_b32 v195, s48, v163\n"
    "v_lshlrev_b32 v240, 16, v164\n"
    "v_mfma_f32_16x16x32_bf16 v[144:147], a[80:83], a[180:183], v[144:147]\n"
    "s_waitcnt lgkmcnt(4)\n"
    "ds_read_b128 a[180:183], v14 offset:62976\n"
    "v_and_b32 v241, s48, v164\n"
    "v_mfma_f32_16x16x32_bf16 v[148:151], a[80:83], a[184:187], v[148:151]\n"
    "v_lshlrev_b32 v242, 16, v165\n"
    "v_and_b32 v243, s48, v165\n"
    "v_mfma_f32_16x16x32_bf16 v[144:147], a[84:87], a[184:187], v[144:147]\n"
    "s_waitcnt lgkmcnt(4)\n"
    "ds_read_b128 a[184:187], v14 offset:63104\n"
    "v_lshlrev_b32 v244, 16, v166\n"
    "v_mfma_f32_16x16x32_bf16 v[148:151], a[84:87], a[192:195], v[148:151]\n"
    "v_and_b32 v245, s48, v166\n"
    "v_lshlrev_b32 v246, 16, v167\n"
    "v_mfma_f32_16x16x32_bf16 v[144:147], a[88:91], a[192:195], v[144:147]\n"
    "s_waitcnt lgkmcnt(4)\n"
    "ds_read_b128 a[192:195], v15 offset:62208\n"
    "v_and_b32 v247, s48, v167\n"
    "v_mfma_f32_16x16x32_bf16 v[148:151], a[88:91], a[196:199], v[148:151]\n"
    "v_add_f32 v188, v188, v240\n"
    "v_add_f32 v189, v189, v241\n"
    "v_mfma_f32_16x16x32_bf16 v[144:147], a[92:95], a[196:199], v[144:147]\n"
    "s_waitcnt lgkmcnt(4)\n"
    "ds_read_b128 a[196:199], v15 offset:62336\n"
    "v_add_f32 v190, v190, v242\n"
    "v_mfma_f32_16x16x32_bf16 v[148:151], a[92:95], a[188:191], v[148:151]\n"
    "v_add_f32 v191, v191, v243\n"
    "v_add_f32 v192, v192, v244\n"
    "v_mfma_f32_16x16x32_bf16 v[144:147], a[96:99], a[188:191], v[144:147]\n"
    "s_waitcnt lgkmcnt(4)\n"
    "ds_read_b128 a[188:191], v15 offset:62464\n"
    "v_add_f32 v193, v193, v245\n"
    "v_mfma_f32_16x16x32_bf16 v[148:151], a[96:99], a[176:179], v[148:151]\n"
    "v_add_f32 v194, v194, v246\n"
    "v_add_f32 v195, v195, v247\n"
    "v_mfma_f32_16x16x32_bf16 v[144:147], a[100:103], a[176:179], v[144:147]\n"
    "s_waitcnt lgkmcnt(4)\n"
    "ds_read_b128 a[176:179], v15 offset:62592\n"
    "v_lshlrev_b32 v40, 16, v168\n"
    "v_mfma_f32_16x16x32_bf16 v[148:151], a[100:103], a[180:183], v[148:151]\n"
    "v_and_b32 v41, s48, v168\n"
    "v_lshlrev_b32 v42, 16, v169\n"
    "v_mfma_f32_16x16x32_bf16 v[144:147], a[104:107], a[180:183], v[144:147]\n"
    "s_waitcnt lgkmcnt(4)\n"
    "ds_read_b128 a[180:183], v15 offset:62720\n"
    "v_and_b32 v43, s48, v169\n"
    "v_mfma_f32_16x16x32_bf16 v[148:151], a[104:107], a[184:187], v[148:151]\n"
    "v_lshlrev_b32 v44, 16, v170\n"
    "v_and_b32 v45, s48, v170\n"
    "v_mfma_f32_16x16x32_bf16 v[144:147], a[108:111], a[184:187], v[144:147]\n"
    "s_waitcnt lgkmcnt(4)\n"
    "buffer_load_dwordx4 v[184:187], v22, s[16:19], s25 offen sc1\n"
    "v_mfma_f32_16x16x32_bf16 v[148:151], a[108:111], a[192:195], v[148:151]\n"
    "v_lshlrev_b32 v46, 16, v171\n"
    "v_and_b32 v47, s48, v171\n"
    "v_mfma_f32_16x16x32_bf16 v[144:147], a[112:115], a[192:195], v[144:147]\n"
    "s_waitcnt lgkmcnt(3)\n"
    "v_lshlrev_b32 v240, 16, v172\n"
    "v_and_b32 v241, s48, v172\n"
    "v_mfma_f32_16x16x32_bf16 v[148:151], a[112:115], a[196:199], v[148:151]\n"
    "v_lshlrev_b32 v242, 16, v173\n"
    "v_and_b32 v243, s48, v173\n"
    "v_mfma_f32_16x16x32_bf16 v[144:147], a[116:119], a[196:199], v[144:147]\n"
    "s_waitcnt lgkmcnt(2)\n"
    "v_lshlrev_b32 v244, 16, v174\n"
    "v_and_b32 v245, s48, v174\n"
    "v_mfma_f32_16x16x32_bf16 v[148:151], a[116:119], a[188:191], v[148:151]\n"
    "v_lshlrev_b32 v246, 16, v175\n"
    "v_and_b32 v247, s48, v175\n"
    "v_mfma_f32_16x16x32_bf16 v[144:147], a[120:123], a[188:191], v[144:147]\n"
    "s_waitcnt lgkmcnt(1)\n"
    "v_add_f32 v40, v40, v240\n"
    "v_add_f32 v41, v41, v241\n"
    "v_mfma_f32_16x16x32_bf16 v[148:151], a[120:123], a[176:179], v[148:151]\n"
    "v_add_f32 v42, v42, v242\n"
    "v_add_f32 v43, v43, v243\n"
    "v_mfma_f32_16x16x32_bf16 v[144:147], a[124:127], a[176:179], v[144:147]\n"
    "s_waitcnt lgkmcnt(0)\n"
    "v_add_f32 v44, v44, v244\n"
    "v_add_f32 v45, v45, v245\n"
    "v_mfma_f32_16x16x32_bf16 v[148:151], a[124:127], a[180:183], v[148:151]\n"
    "v_add_f32 v46, v46, v246\n"
    "v_add_f32 v47, v47, v247\n"
    "s_waitcnt vmcnt(1)\n"
    "v_max3_u32 v240, v176, v177, v178\n"
    "v_max3_u32 v240, v240, v179, v180\n"
    "v_max3_u32 v240, v240, v181, v182\n"
    "v_max_u32 v240, v240, v183\n"
    "v_cmp_eq_u32 vcc, -1, v240\n"
    "s_cbranch_vccz LM2ok_%=\n"
    "s_mov_b32 s43, 0x100000\n"
    "LM2lp_%=:\n"
    "buffer_load_dwordx4 v[176:179], v22, s[16:19], s26 offen sc1\n"
    "buffer_load_dwordx4 v[180:183], v22, s[16:19], s27 offen sc1\n"
    "s_waitcnt vmcnt(0)\n"
    "s_sub_u32 s43, s43, 1\n"
    "s_cbranch_scc1 LM2ok_%=\n"
    "v_max3_u32 v240, v176, v177, v178\n"
    "v_max3_u32 v240, v240, v179, v180\n"
    "v_max3_u32 v240, v240, v181, v182\n"
    "v_max_u32 v240, v240, v183\n"
    "v_cmp_eq_u32 vcc, -1, v240\n"
    "s_cbranch_vccnz LM2lp_%=\n"
    "LM2ok_%=:\n"
    "v_mfma_f32_16x16x32_bf16 v[144:147], v[236:239], v[176:179], v[144:147]\n"
    "v_mfma_f32_16x16x32_bf16 v[148:151], v[234:237], v[176:179], v[148:151]\n"
    "v_mfma_f32_16x16x32_bf16 v[40:43], v[236:239], v[180:183], v[40:43]\n"
    "v_mfma_f32_16x16x32_bf16 v[44:47], v[234:237], v[180:183], v[44:47]\n"
    "s_nop 4\n"
    "v_pk_add_f32 v[144:145], v[144:145], v[188:189]\n"
    "v_pk_add_f32 v[146:147], v[146:147], v[190:191]\n"
    "v_pk_add_f32 v[148:149], v[148:149], v[192:193]\n"
    "v_pk_add_f32 v[150:151], v[150:151], v[194:195]\n"
    "v_pk_mul_f32 v[240:241], v[144:145], s[46:47] op_sel_hi:[1,0]\n"
    "v_pk_mul_f32 v[242:243], v[146:147], s[46:47] op_sel_hi:[1,0]\n"
    "v_pk_mul_f32 v[244:245], v[148:149], s[46:47] op_sel_hi:[1,0]\n"
    "v_pk_mul_f32 v[246:247], v[150:151], s[46:47] op_sel_hi:[1,0]\n"
    "v_exp_f32 v240, -|v240|\n"
    "v_fma_f32 v248, -v144, v144, |v144|\n"
    "v_exp_f32 v241, -|v241|\n"
    "v_fma_f32 v249, -v145, v145, |v145|\n"
    "v_exp_f32 v242, -|v242|\n"
    "v_fma_f32 v250, -v146, v146, |v146|\n"
    "v_exp_f32 v243, -|v243|\n"
    "v_fma_f32 v251, -v147, v147, |v147|\n"
    "v_exp_f32 v244, -|v244|\n"
    "v_fma_f32 v252, -v148, v148, |v148|\n"
    "v_exp_f32 v245, -|v245|\n"
    "v_fma_f32 v253, -v149, v149, |v149|\n"
    "v_exp_f32 v246, -|v246|\n"
    "v_fma_f32 v254, -v150, v150, |v150|\n"
    "v_exp_f32 v247, -|v247|\n"
    "v_fma_f32 v255, -v151, v151, |v151|\n"
    "v_pk_fma_f32 v[168:169], v[240:241], 0.5, 0.5 op_sel_hi:[1,0,0] neg_lo:[1,0,0] neg_hi:[1,0,0]\n"
    "v_pk_fma_f32 v[170:171], v[242:243], 0.5, 0.5 op_sel_hi:[1,0,0] neg_lo:[1,0,0] neg_hi:[1,0,0]\n"
    "v_pk_fma_f32 v[172:173], v[244:245], 0.5, 0.5 op_sel_hi:[1,0,0] neg_lo:[1,0,0] neg_hi:[1,0,0]\n"
    "v_pk_fma_f32 v[174:175], v[246:247], 0.5, 0.5 op_sel_hi:[1,0,0] neg_lo:[1,0,0] neg_hi:[1,0,0]\n"
    "v_pk_fma_f32 v[240:241], v[240:241], 0.5, 0.5 op_sel_hi:[1,0,0]\n"
    "v_pk_fma_f32 v[242:243], v[242:243], 0.5, 0.5 op_sel_hi:[1,0,0]\n"
    "v_pk_fma_f32 v[244:245], v[244:245], 0.5, 0.5 op_sel_hi:[1,0,0]\n"
    "v_pk_fma_f32 v[246:247], v[246:247], 0.5, 0.5 op_sel_hi:[1,0,0]\n"
    "v_med3_f32 v248, v168, v248, |v144|\n"
    "v_med3_f32 v249, v169, v249, |v145|\n"
    "v_med3_f32 v250, v170, v250, |v146|\n"
    "v_med3_f32 v251, v171, v251, |v147|\n"
    "v_med3_f32 v252, v172, v252, |v148|\n"
    "v_med3_f32 v253, v173, v253, |v149|\n"
    "v_med3_f32 v254, v174, v254, |v150|\n"
    "v_med3_f32 v255, v175, v255, |v151|\n"
    "v_bfi_b32 v248, s47, v248, v144\n"
    "v_bfi_b32 v249, s47, v249, v145\n"
    "v_bfi_b32 v250, s47, v250, v146\n"
    "v_bfi_b32 v251, s47, v251, v147\n"
    "v_bfi_b32 v252, s47, v252, v148\n"
    "v_bfi_b32 v253, s47, v253, v149\n"
    "v_bfi_b32 v254, s47, v254, v150\n"
    "v_bfi_b32 v255, s47, v255, v151\n"
    "s_waitcnt vmcnt(0)\n"
    "v_max3_u32 v188, v184, v185, v186\n"
    "v_max_u32 v188, v188, v187\n"
    "v_cmp_eq_u32 vcc, -1, v188\n"
    "s_cbranch_vccz LL2ok_%=\n"
    "s_mov_b32 s43, 0x100000\n"
    "LL2lp_%=:\n"
    "buffer_load_dwordx4 v[184:187], v22, s[16:19], s25 offen sc1\n"
    "s_waitcnt vmcnt(0)\n"
    "s_sub_u32 s43, s43, 1\n"
    "s_cbranch_scc1 LL2ok_%=\n"
    "v_max3_u32 v188, v184, v185, v186\n"
    "v_max_u32 v188, v188, v187\n"
    "v_cmp_eq_u32 vcc, -1, v188\n"
    "s_cbranch_vccnz LL2lp_%=\n"
    "LL2ok_%=:\n"
    "v_lshlrev_b32 v188, 16, v184\n"
    "v_and_b32 v189, s48, v184\n"
    "v_lshlrev_b32 v190, 16, v185\n"
    "v_and_b32 v191, s48, v185\n"
    "v_lshlrev_b32 v192, 16, v186\n"
    "v_and_b32 v193, s48, v186\n"
    "v_lshlrev_b32 v194, 16, v187\n"
    "v_and_b32 v195, s48, v187\n"
    "v_pk_add_f32 v[40:41], v[40:41], v[188:189]\n"
    "v_pk_add_f32 v[42:43], v[42:43], v[190:191]\n"
    "v_pk_add_f32 v[44:45], v[44:45], v[192:193]\n"
    "v_pk_add_f32 v[46:47], v[46:47], v[194:195]\n"
    "v_pk_mul_f32 v[200:201], v[40:41], s[44:45] op_sel:[0,1] op_sel_hi:[1,1]\n"
    "v_pk_mul_f32 v[202:203], v[42:43], s[44:45] op_sel:[0,1] op_sel_hi:[1,1]\n"
    "v_pk_mul_f32 v[204:205], v[44:45], s[44:45] op_sel:[0,1] op_sel_hi:[1,1]\n"
    "v_pk_mul_f32 v[206:207], v[46:47], s[44:45] op_sel:[0,1] op_sel_hi:[1,1]\n"
    "v_exp_f32 v200, v200\n"
    "v_exp_f32 v201, v201\n"
    "v_exp_f32 v202, v202\n"
    "v_exp_f32 v203, v203\n"
    "v_exp_f32 v204, v204\n"
    "v_exp_f32 v205, v205\n"
    "v_exp_f32 v206, v206\n"
    "v_exp_f32 v207, v207\n"
    "v_pk_add_f32 v[200:201], v[200:201], 1.0 op_sel_hi:[1,0]\n"
    "v_pk_add_f32 v[202:203], v[202:203], 1.0 op_sel_hi:[1,0]\n"
    "v_pk_add_f32 v[204:205], v[204:205], 1.0 op_sel_hi:[1,0]\n"
    "v_pk_add_f32 v[206:207], v[206:207], 1.0 op_sel_hi:[1,0]\n"
    "v_pk_mul_f32 v[240:241], v[240:241], v[200:201]\n"
    "v_pk_mul_f32 v[242:243], v[242:243], v[202:203]\n"
    "v_pk_mul_f32 v[244:245], v[244:245], v[204:205]\n"
    "v_pk_mul_f32 v[246:247], v[246:247], v[206:207]\n"
    "v_rcp_f32 v240, v240\n"
    "v_rcp_f32 v241, v241\n"
    "v_rcp_f32 v242, v242\n"
    "v_rcp_f32 v243, v243\n"
    "v_rcp_f32 v244, v244\n"
    "v_rcp_f32 v245, v245\n"
    "v_rcp_f32 v246, v246\n"
    "v_rcp_f32 v247, v247\n"
    "v_pk_mul_f32 v[248:249], v[248:249], v[240:241]\n"
    "v_pk_mul_f32 v[250:251], v[250:251], v[242:243]\n"
    "v_pk_mul_f32 v[252:253], v[252:253], v[244:245]\n"
    "v_pk_mul_f32 v[254:255], v[254:255], v[246:247]\n"
    "v_cvt_pk_bf16_f32 v240, v248, v249\n"
    "v_cvt_pk_bf16_f32 v241, v250, v251\n"
    "v_cvt_pk_bf16_f32 v242, v252, v253\n"
    "v_cvt_pk_bf16_f32 v243, v254, v255\n"
    "buffer_store_dwordx2 v[240:241], v25, s[20:23], 0 offen\n"
    "buffer_store_dwordx2 v[242:243], v25, s[20:23], 0 offen offset:2048\n"
    "s_endpgm\n"
    ".fill (LV0_%=+18432-.)/4, 4, 0xbf800000\n"
    "LV3_%=:\n"
    "s_nop 0\n"
    "v_add_f32 v64, v64, v160\n"
    "v_add_f32 v65, v65, v161\n"
    "v_mfma_f32_16x16x32_bf16 v[104:107], a[130:133], a[232:235], v[104:107]\n"
    "v_add_f32 v66, v66, v162\n"
    "v_add_f32 v67, v67, v163\n"
    "v_mfma_f32_16x16x32_bf16 v[108:111], a[130:133], a[212:215], v[108:111]\n"
    "v_add_f32 v68, v68, v160\n"
    "v_add_f32 v69, v69, v161\n"
    "v_mfma_f32_16x16x32_bf16 v[112:115], a[130:133], a[192:195], v[112:115]\n"
    "ds_read_b128 a[176:179], v15 offset:21504\n"
    "v_add_f32 v70, v70, v162\n"
    "v_mfma_f32_16x16x32_bf16 v[116:119], a[130:133], a[188:191], v[116:119]\n"
    "s_barrier\n"
    "ds_read_b128 a[184:187], v10 offset:62336\n"
    "v_add_f32 v71, v71, v163\n"
    "v_mfma_f32_16x16x32_bf16 v[120:123], a[130:133], a[196:199], v[120:123]\n"
    "s_waitcnt lgkmcnt(2)\n"
    "ds_read_b128 a[200:203], v10 offset:62464\n"
    "v_add_f32 v72, v72, v164\n"
    "v_mfma_f32_16x16x32_bf16 v[124:127], a[130:133], a[180:183], v[124:127]\n"
    "ds_read_b128 a[204:207], v10 offset:62592\n"
    "v_add_f32 v73, v73, v165\n"
    "v_mfma_f32_16x16x32_bf16 v[96:99], a[138:141], a[228:231], v[96:99]\n"
    "ds_read_b128 a[208:211], v10 offset:62720\n"
    "v_add_f32 v74, v74, v166\n"
    "v_mfma_f32_16x16x32_bf16 v[100:103], a[138:141], a[232:235], v[100:103]\n"
    "v_add_f32 v75, v75, v167\n"
    "v_add_f32 v76, v76, v164\n"
    "v_mfma_f32_16x16x32_bf16 v[104:107], a[138:141], a[212:215], v[104:107]\n"
    "v_add_f32 v77, v77, v165\n"
    "v_add_f32 v78, v78, v166\n"
    "v_mfma_f32_16x16x32_bf16 v[108:111], a[138:141], a[192:195], v[108:111]\n"
    "v_add_f32 v79, v79, v167\n"
    "v_add_f32 v80, v80, v168\n"
    "v_mfma_f32_16x16x32_bf16 v[112:115], a[138:141], a[188:191], v[112:115]\n"
    "ds_read_b128 a[188:191], v10 offset:62848\n"
    "v_add_f32 v81, v81, v169\n"
    "v_mfma_f32_16x16x32_bf16 v[116:119], a[138:141], a[196:199], v[116:119]\n"
    "v_add_f32 v82, v82, v170\n"
    "v_add_f32 v83, v83, v171\n"
    "v_mfma_f32_16x16x32_bf16 v[120:123], a[138:141], a[180:183], v[120:123]\n"
    "s_waitcnt lgkmcnt(5)\n"
    "v_add_f32 v84, v84, v168\n"
    "v_add_f32 v85, v85, v169\n"
    "v_mfma_f32_16x16x32_bf16 v[124:127], a[138:141], a[176:179], v[124:127]\n"
    "s_waitcnt lgkmcnt(4)\n"
    "v_add_f32 v86, v86, v170\n"
    "v_add_f32 v87, v87, v171\n"
    "v_mfma_f32_16x16x32_bf16 v[128:131], a[4:7], a[184:187], v[64:67]\n"
    "s_waitcnt lgkmcnt(3)\n"
    "ds_read_b128 a[176:179], v10 offset:62976\n"
    "v_add_f32 v88, v88, v172\n"
    "v_mfma_f32_16x16x32_bf16 v[132:135], a[4:7], a[200:203], v[68:71]\n"
    "s_waitcnt lgkmcnt(3)\n"
    "v_add_f32 v89, v89, v173\n"
    "v_add_f32 v90, v90, v174\n"
    "v_mfma_f32_16x16x32_bf16 v[136:139], a[4:7], a[204:207], v[72:75]\n"
    "s_waitcnt lgkmcnt(2)\n"
    "v_add_f32 v91, v91, v175\n"
    "v_add_f32 v92, v92, v172\n"
    "v_mfma_f32_16x16x32_bf16 v[140:143], a[4:7], a[208:211], v[76:79]\n"
    "v_add_f32 v93, v93, v173\n"
    "v_add_f32 v94, v94, v174\n"
    "v_mfma_f32_16x16x32_bf16 v[128:131], a[8:11], a[200:203], v[128:131]\n"
    "ds_read_b128 a[180:183], v10 offset:63104\n"
    "v_add_f32 v95, v95, v175\n"
    "v_mfma_f32_16x16x32_bf16 v[132:135], a[8:11], a[204:207], v[132:135]\n"
    "v_add_f32 v120, v120, v88\n"
    "v_add_f32 v121, v121, v89\n"
    "v_mfma_f32_16x16x32_bf16 v[136:139], a[8:11], a[208:211], v[136:139]\n"
    "s_waitcnt lgkmcnt(2)\n"
    "v_add_f32 v122, v122, v90\n"
    "v_add_f32 v123, v123, v91\n"
    "v_mfma_f32_16x16x32_bf16 v[140:143], a[8:11], a[188:191], v[140:143]\n"
    "v_add_f32 v124, v124, v92\n"
    "v_add_f32 v125, v125, v93\n"
    "v_mfma_f32_16x16x32_bf16 v[128:131], a[12:15], a[204:207], v[128:131]\n"
    "ds_read_b128 a[184:187], v11 offset:62208\n"
    "v_add_f32 v126, v126, v94\n"
    "v_mfma_f32_16x16x32_bf16 v[132:135], a[12:15], a[208:211], v[132:135]\n"
    "v_add_f32 v127, v127, v95\n"
    "v_cvt_pk_bf16_f32 v200, v120, v121\n"
    "v_mfma_f32_16x16x32_bf16 v[136:139], a[12:15], a[188:191], v[136:139]\n"
    "s_waitcnt lgkmcnt(2)\n"
    "v_cvt_pk_bf16_f32 v201, v122, v123\n"
    "v_cvt_pk_bf16_f32 v202, v124, v125\n"
    "v_mfma_f32_16x16x32_bf16 v[140:143], a[12:15], a[176:179], v[140:143]\n"
    "v_cvt_pk_bf16_f32 v203, v126, v127\n"
    "v_mfma_f32_16x16x32_bf16 v[128:131], a[16:19], a[208:211], v[128:131]\n"
    "s_xor_b32 s36, s39, 131072\n"
    "ds_read_b128 a[192:195], v11 offset:62336\n"
    "v_min_u32 v200, -2, v200\n"
    "v_mfma_f32_16x16x32_bf16 v[132:135], a[16:19], a[188:191], v[132:135]\n"
    "v_min_u32 v201, -2, v201\n"
    "v_min_u32 v202, -2, v202\n"
    "v_mfma_f32_16x16x32_bf16 v[136:139], a[16:19], a[176:179], v[136:139]\n"
    "s_waitcnt lgkmcnt(2)\n"
    "v_min_u32 v203, -2, v203\n"
    "v_mfma_f32_16x16x32_bf16 v[140:143], a[16:19], a[180:183], v[140:143]\n"
    "s_waitcnt vmcnt(0)\n"
    "v_cmp_ne_u64 vcc, v[28:29], v[26:27]\n"
    "s_cbranch_vccz LF3ok_%=\n"
    "s_mov_b32 s43, 0x100000\n"
    "LF3lp_%=:\n"
    "buffer_load_dwordx2 v[28:29], v23, s[16:19], s40 offen offset:0 sc1\n"
    "s_waitcnt vmcnt(0)\n"
    "s_sub_u32 s43, s43, 1\n"
    "s_cbranch_scc1 LF3ok_%=\n"
    "v_cmp_ne_u64 vcc, v[28:29], v[26:27]\n"
    "s_cbranch_vccnz LF3lp_%=\n"
    "LF3ok_%=:\n"
    "v_mfma_f32_16x16x32_bf16 v[128:131], a[20:23], a[188:191], v[128:131]\n"
    "ds_read_b128 a[188:191], v11 offset:62464\n"
    "v_mfma_f32_16x16x32_bf16 v[132:135], a[20:23], a[176:179], v[132:135]\n"
    "buffer_store_dwordx4 v[200:203], v22, s[16:19], s36 offen sc1\n"
    "v_mfma_f32_16x16x32_bf16 v[136:139], a[20:23], a[180:183], v[136:139]\n"
    "s_waitcnt lgkmcnt(2)\n"
    "v_add_f32 v112, v112, v80\n"
    "v_add_f32 v113, v113, v81\n"
    "v_mfma_f32_16x16x32_bf16 v[140:143], a[20:23], a[184:187], v[140:143]\n"
    "v_add_f32 v114, v114, v82\n"
    "v_add_f32 v115, v115, v83\n"
    "v_mfma_f32_16x16x32_bf16 v[128:131], a[24:27], a[176:179], v[128:131]\n"
    "ds_read_b128 a[176:179], v11 offset:62592\n"
    "v_add_f32 v116, v116, v84\n"
    "v_mfma_f32_16x16x32_bf16 v[132:135], a[24:27], a[180:183], v[132:135]\n"
    "v_add_f32 v117, v117, v85\n"
    "v_add_f32 v118, v118, v86\n"
    "v_mfma_f32_16x16x32_bf16 v[136:139], a[24:27], a[184:187], v[136:139]\n"
    "s_waitcnt lgkmcnt(2)\n"
    "v_add_f32 v119, v119, v87\n"
    "v_cvt_pk_bf16_f32 v204, v112, v113\n"
    "v_mfma_f32_16x16x32_bf16 v[140:143], a[24:27], a[192:195], v[140:143]\n"
    "v_cvt_pk_bf16_f32 v205, v114, v115\n"
    "v_cvt_pk_bf16_f32 v206, v116, v117\n"
    "v_mfma_f32_16x16x32_bf16 v[128:131], a[28:31], a[180:183], v[128:131]\n"
    "ds_read_b128 a[180:183], v11 offset:62720\n"
    "v_cvt_pk_bf16_f32 v207, v118, v119\n"
    "v_mfma_f32_16x16x32_bf16 v[132:135], a[28:31], a[184:187], v[132:135]\n"
    "s_xor_b32 s36, s39, 163840\n"
    "v_min_u32 v204, -2, v204\n"
    "v_min_u32 v205, -2, v205\n"
    "v_mfma_f32_16x16x32_bf16 v[136:139], a[28:31], a[192:195], v[136:139]\n"
    "s_waitcnt lgkmcnt(2)\n"
    "v_min_u32 v206, -2, v206\n"
    "v_min_u32 v207, -2, v207\n"
    "v_mfma_f32_16x16x32_bf16 v[140:143], a[28:31], a[188:191], v[140:143]\n"
    "buffer_store_dwordx4 v[204:207], v22, s[16:19], s36 offen sc1\n"
    "v_mfma_f32_16x16x32_bf16 v[128:131], a[32:35], a[184:187], v[128:131]\n"
    "ds_read_b128 a[184:187], v11 offset:62848\n"
    "v_add_f32 v104, v104, v72\n"
    "v_mfma_f32_16x16x32_bf16 v[132:135], a[32:35], a[192:195], v[132:135]\n"
    "v_add_f32 v105, v105, v73\n"
    "v_add_f32 v106, v106, v74\n"
    "v_mfma_f32_16x16x32_bf16 v[136:139], a[32:35], a[188:191], v[136:139]\n"
    "s_waitcnt lgkmcnt(2)\n"
    "v_add_f32 v107, v107, v75\n"
    "v_add_f32 v108, v108, v76\n"
    "v_mfma_f32_16x16x32_bf16 v[140:143], a[32:35], a[176:179], v[140:143]\n"
    "v_add_f32 v109, v109, v77\n"
    "v_add_f32 v110, v110, v78\n"
    "v_mfma_f32_16x16x32_bf16 v[128:131], a[36:39], a[192:195], v[128:131]\n"
    "ds_read_b128 a[192:195], v11 offset:62976\n"
    "v_add_f32 v111, v111, v79\n"
    "v_mfma_f32_16x16x32_bf16 v[132:135], a[36:39], a[188:191], v[132:135]\n"
    "v_cvt_pk_bf16_f32 v208, v104, v105\n"
    "v_cvt_pk_bf16_f32 v209, v106, v107\n"
    "v_mfma_f32_16x16x32_bf16 v[136:139], a[36:39], a[176:179], v[136:139]\n"
    "s_waitcnt lgkmcnt(2)\n"
    "v_cvt_pk_bf16_f32 v210, v108, v109\n"
    "v_cvt_pk_bf16_f32 v211, v110, v111\n"
    "v_mfma_f32_16x16x32_bf16 v[140:143], a[36:39], a[180:183], v[140:143]\n"
    "s_xor_b32 s36, s39, 196608\n"
    "v_min_u32 v208, -2, v208\n"
    "v_min_u32 v209, -2, v209\n"
    "v_mfma_f32_16x16x32_bf16 v[128:131], a[40:43], a[188:191], v[128:131]\n"
    "ds_read_b128 a[188:191], v11 offset:63104\n"
    "v_min_u32 v210, -2, v210\n"
    "v_mfma_f32_16x16x32_bf16 v[132:135], a[40:43], a[176:179], v[132:135]\n"
    "v_min_u32 v211, -2, v211\n"
    "v_mfma_f32_16x16x32_bf16 v[136:139], a[40:43], a[180:183], v[136:139]\n"
    "s_waitcnt lgkmcnt(2)\n"
    "buffer_store_dwordx4 v[208:211], v22, s[16:19], s36 offen sc1\n"
    "v_mfma_f32_16x16x32_bf16 v[140:143], a[40:43], a[184:187], v[140:143]\n"
    "v_add_f32 v96, v96, v64\n"
    "v_add_f32 v97, v97, v65\n"
    "v_mfma_f32_16x16x32_bf16 v[128:131], a[44:47], a[176:179], v[128:131]\n"
    "ds_read_b128 a[176:179], v12 offset:62208\n"
    "v_add_f32 v98, v98, v66\n"
    "v_mfma_f32_16x16x32_bf16 v[132:135], a[44:47], a[180:183], v[132:135]\n"
    "v_add_f32 v99, v99, v67\n"
    "v_add_f32 v100, v100, v68\n"
    "v_mfma_f32_16x16x32_bf16 v[136:139], a[44:47], a[184:187], v[136:139]\n"
    "s_waitcnt lgkmcnt(2)\n"
    "v_add_f32 v101, v101, v69\n"
    "v_add_f32 v102, v102, v70\n"
    "v_mfma_f32_16x16x32_bf16 v[140:143], a[44:47], a[192:195], v[140:143]\n"
    "v_add_f32 v103, v103, v71\n"
    "v_cvt_pk_bf16_f32 v212, v96, v97\n"
    "v_mfma_f32_16x16x32_bf16 v[128:131], a[48:51], a[180:183], v[128:131]\n"
    "ds_read_b128 a[180:183], v12 offset:62336\n"
    "v_cvt_pk_bf16_f32 v213, v98, v99\n"
    "v_mfma_f32_16x16x32_bf16 v[132:135], a[48:51], a[184:187], v[132:135]\n"
    "v_cvt_pk_bf16_f32 v214, v100, v101\n"
    "v_cvt_pk_bf16_f32 v215, v102, v103\n"
    "v_mfma_f32_16x16x32_bf16 v[136:139], a[48:51], a[192:195], v[136:139]\n"
    "s_waitcnt lgkmcnt(2)\n"
    "s_xor_b32 s36, s39, 229376\n"
    "v_min_u32 v212, -2, v212\n"
    "v_mfma_f32_16x16x32_bf16 v[140:143], a[48:51], a[188:191], v[140:143]\n"
    "v_min_u32 v213, -2, v213\n"
    "v_min_u32 v214, -2, v214\n"
    "v_mfma_f32_16x16x32_bf16 v[128:131], a[52:55], a[184:187], v[128:131]\n"
    "ds_read_b128 a[184:187], v12 offset:62464\n"
    "v_min_u32 v215, -2, v215\n"
    "v_mfma_f32_16x16x32_bf16 v[132:135], a[52:55], a[192:195], v[132:135]\n"
    "ds_read_b128 a[196:199], v13 offset:62336\n"
    "v_mfma_f32_16x16x32_bf16 v[136:139], a[52:55], a[188:191], v[136:139]\n"
    "s_waitcnt lgkmcnt(3)\n"
    "ds_read_b128 a[200:203], v13 offset:62464\n"
    "v_mfma_f32_16x16x32_bf16 v[140:143], a[52:55], a[176:179], v[140:143]\n"
    "ds_read_b128 a[204:207], v13 offset:62592\n"
    "v_mfma_f32_16x16x32_bf16 v[128:131], a[56:59], a[192:195], v[128:131]\n"
    "ds_read_b128 a[192:195], v13 offset:62720\n"
    "v_mfma_f32_16x16x32_bf16 v[132:135], a[56:59], a[188:191], v[132:135]\n"
    "buffer_store_dwordx4 v[212:215], v22, s[16:19], s36 offen sc1\n"
    "v_mfma_f32_16x16x32_bf16 v[136:139], a[56:59], a[176:179], v[136:139]\n"
    "s_waitcnt lgkmcnt(5)\n"
    "v_mfma_f32_16x16x32_bf16 v[140:143], a[56:59], a[180:183], v[140:143]\n"
    "v_mfma_f32_16x16x32_bf16 v[128:131], a[60:63], a[188:191], v[128:131]\n"
    "ds_read_b128 a[188:191], v13 offset:62848\n"
    "v_mfma_f32_16x16x32_bf16 v[132:135], a[60:63], a[176:179], v[132:135]\n"
    "v_mfma_f32_16x16x32_bf16 v[136:139], a[60:63], a[180:183], v[136:139]\n"
    "s_waitcnt lgkmcnt(5)\n"
    "v_mfma_f32_16x16x32_bf16 v[140:143], a[60:63], a[184:187], v[140:143]\n"
    "s_waitcnt lgkmcnt(4)\n"
    "v_mfma_f32_16x16x32_bf16 v[128:131], a[68:71], a[196:199], v[128:131]\n"
    "s_waitcnt lgkmcnt(3)\n"
    "ds_read_b128 a[176:179], v13 offset:62976\n"
    "v_mfma_f32_16x16x32_bf16 v[132:135], a[68:71], a[200:203], v[132:135]\n"
    "s_waitcnt lgkmcnt(3)\n"
    "v_mfma_f32_16x16x32_bf16 v[136:139], a[68:71], a[204:207], v[136:139]\n"
    "s_waitcnt lgkmcnt(2)\n"
    "v_mfma_f32_16x16x32_bf16 v[140:143], a[68:71], a[192:195], v[140:143]\n"
    "v_mfma_f32_16x16x32_bf16 v[128:131], a[72:75], a[200:203], v[128:131]\n"
    "ds_read_b128 a[180:183], v13 offset:63104\n"
    "v_mfma_f32_16x16x32_bf16 v[132:135], a[72:75], a[204:207], v[132:135]\n"
    "v_mfma_f32_16x16x32_bf16 v[136:139], a[72:75], a[192:195], v[136:139]\n"
    "s_waitcnt lgkmcnt(2)\n"
    "v_mfma_f32_16x16x32_bf16 v[140:143], a[72:75], a[188:191], v[140:143]\n"
    "v_mfma_f32_16x16x32_bf16 v[128:131], a[76:79], a[204:207], v[128:131]\n"
    "ds_read_b128 a[184:187], v14 offset:62208\n"
    "v_mfma_f32_16x16x32_bf16 v[132:135], a[76:79], a[192:195], v[132:135]\n"
    "v_mfma_f32_16x16x32_bf16 v[136:139], a[76:79], a[188:191], v[136:139]\n"
    "s_waitcnt lgkmcnt(2)\n"
    "v_mfma_f32_16x16x32_bf16 v[140:143], a[76:79], a[176:179], v[140:143]\n"
    "v_mfma_f32_16x16x32_bf16 v[128:131], a[80:83], a[192:195], v[128:131]\n"
    "ds_read_b128 a[192:195], v14 offset:62336\n"
    "v_mfma_f32_16x16x32_bf16 v[132:135], a[80:83], a[188:191], v[132:135]\n"
    "v_mfma_f32_16x16x32_bf16 v[136:139], a[80:83], a[176:179], v[136:139]\n"
    "s_waitcnt lgkmcnt(2)\n"
    "v_mfma_f32_16x16x32_bf16 v[140:143], a[80:83], a[180:183], v[140:143]\n"
    "v_mfma_f32_16x16x32_bf16 v[128:131], a[84:87], a[188:191], v[128:131]\n"
    "ds_read_b128 a[188:191], v14 offset:62464\n"
    "v_mfma_f32_16x16x32_bf16 v[132:135], a[84:87], a[176:179], v[132:135]\n"
    "v_mfma_f32_16x16x32_bf16 v[136:139], a[84:87], a[180:183], v[136:139]\n"
    "s_waitcnt lgkmcnt(2)\n"
    "v_mfma_f32_16x16x32_bf16 v[140:143], a[84:87], a[184:187], v[140:143]\n"
    "v_mfma_f32_16x16x32_bf16 v[128:131], a[88:91], a[176:179], v[128:131]\n"
    "ds_read_b128 a[176:179], v14 offset:62592\n"
    "v_mfma_f32_16x16x32_bf16 v[132:135], a[88:91], a[180:183], v[132:135]\n"
    "v_mfma_f32_16x16x32_bf16 v[136:139], a[88:91], a[184:187], v[136:139]\n"
    "s_waitcnt lgkmcnt(2)\n"
    "v_mfma_f32_16x16x32_bf16 v[140:143], a[88:91], a[192:195], v[140:143]\n"
    "v_mfma_f32_16x16x32_bf16 v[128:131], a[92:95], a[180:183], v[128:131]\n"
    "ds_read_b128 a[180:183], v14 offset:62720\n"
    "v_mfma_f32_16x16x32_bf16 v[132:135], a[92:95], a[184:187], v[132:135]\n"
    "v_mfma_f32_16x16x32_bf16 v[136:139], a[92:95], a[192:195], v[136:139]\n"
    "s_waitcnt lgkmcnt(2)\n"
    "v_mfma_f32_16x16x32_bf16 v[140:143], a[92:95], a[188:191], v[140:143]\n"
    "v_mfma_f32_16x16x32_bf16 v[128:131], a[96:99], a[184:187], v[128:131]\n"
    "ds_read_b128 a[184:187], v14 offset:62848\n"
    "v_mfma_f32_16x16x32_bf16 v[132:135], a[96:99], a[192:195], v[132:135]\n"
    "v_mfma_f32_16x16x32_bf16 v[136:139], a[96:99], a[188:191], v[136:139]\n"
    "s_waitcnt lgkmcnt(2)\n"
    "v_mfma_f32_16x16x32_bf16 v[140:143], a[96:99], a[176:179], v[140:143]\n"
    "v_mfma_f32_16x16x32_bf16 v[128:131], a[100:103], a[192:195], v[128:131]\n"
    "ds_read_b128 a[192:195], v14 offset:62976\n"
    "v_mfma_f32_16x16x32_bf16 v[132:135], a[100:103], a[188:191], v[132:135]\n"
    "v_mfma_f32_16x16x32_bf16 v[136:139], a[100:103], a[176:179], v[136:139]\n"
    "s_waitcnt lgkmcnt(2)\n"
    "v_mfma_f32_16x16x32_bf16 v[140:143], a[100:103], a[180:183], v[140:143]\n"
    "v_mfma_f32_16x16x32_bf16 v[128:131], a[104:107], a[188:191], v[128:131]\n"
    "ds_read_b128 a[188:191], v14 offset:63104\n"
    "v_mfma_f32_16x16x32_bf16 v[132:135], a[104:107], a[176:179], v[132:135]\n"
    "v_mfma_f32_16x16x32_bf16 v[136:139], a[104:107], a[180:183], v[136:139]\n"
    "s_waitcnt lgkmcnt(2)\n"
    "v_mfma_f32_16x16x32_bf16 v[140:143], a[104:107], a[184:187], v[140:143]\n"
    "v_mfma_f32_16x16x32_bf16 v[128:131], a[108:111], a[176:179], v[128:131]\n"
    "ds_read_b128 a[176:179], v15 offset:62208\n"
    "v_mfma_f32_16x16x32_bf16 v[132:135], a[108:111], a[180:183], v[132:135]\n"
    "v_mfma_f32_16x16x32_bf16 v[136:139], a[108:111], a[184:187], v[136:139]\n"
    "s_waitcnt lgkmcnt(2)\n"
    "v_mfma_f32_16x16x32_bf16 v[140:143], a[108:111], a[192:195], v[140:143]\n"
    "v_mfma_f32_16x16x32_bf16 v[128:131], a[112:115], a[180:183], v[128:131]\n"
    "ds_read_b128 a[180:183], v15 offset:62336\n"
    "v_mfma_f32_16x16x32_bf16 v[132:135], a[112:115], a[184:187], v[132:135]\n"
    "buffer_load_dwordx4 v[160:163], v22, s[16:19], s28 offen sc1\n"
    "v_mfma_f32_16x16x32_bf16 v[136:139], a[112:115], a[192:195], v[136:139]\n"
    "s_waitcnt lgkmcnt(2)\n"
    "v_mfma_f32_16x16x32_bf16 v[140:143], a[112:115], a[188:191], v[140:143]\n"
    "v_mfma_f32_16x16x32_bf16 v[128:131], a[116:119], a[184:187], v[128:131]\n"
    "ds_read_b128 a[184:187], v15 offset:62464\n"
    "v_mfma_f32_16x16x32_bf16 v[132:135], a[116:119], a[192:195], v[132:135]\n"
    "ds_read_b128 a[196:199], v10 offset:62848\n"
    "v_mfma_f32_16x16x32_bf16 v[136:139], a[116:119], a[188:191], v[136:139]\n"
    "s_waitcnt lgkmcnt(3)\n"
    "ds_read_b128 a[200:203], v10 offset:62976\n"
    "v_mfma_f32_16x16x32_bf16 v[140:143], a[116:119], a[176:179], v[140:143]\n"
    "buffer_load_dwordx4 v[164:167], v22, s[16:19], s30 offen sc1\n"
    "v_mfma_f32_16x16x32_bf16 v[128:131], a[120:123], a[192:195], v[128:131]\n"
    "ds_read_b128 a[192:195], v10 offset:63104\n"
    "v_mfma_f32_16x16x32_bf16 v[132:135], a[120:123], a[188:191], v[132:135]\n"
    "v_mfma_f32_16x16x32_bf16 v[136:139], a[120:123], a[176:179], v[136:139]\n"
    "s_waitcnt lgkmcnt(4)\n"
    "ds_read_b128 a[204:207], v11 offset:62208\n"
    "v_mfma_f32_16x16x32_bf16 v[140:143], a[120:123], a[180:183], v[140:143]\n"
    "buffer_load_dwordx4 v[168:171], v22, s[16:19], s29 offen sc1\n"
    "v_mfma_f32_16x16x32_bf16 v[128:131], a[124:127], a[188:191], v[128:131]\n"
    "ds_read_b128 a[188:191], v11 offset:62336\n"
    "v_mfma_f32_16x16x32_bf16 v[132:135], a[124:127], a[176:179], v[132:135]\n"
    "v_mfma_f32_16x16x32_bf16 v[136:139], a[124:127], a[180:183], v[136:139]\n"
    "s_waitcnt lgkmcnt(5)\n"
    "ds_read_b128 a[176:179], v11 offset:62464\n"
    "v_mfma_f32_16x16x32_bf16 v[140:143], a[124:127], a[184:187], v[140:143]\n"
    "s_waitcnt lgkmcnt(5)\n"
    "buffer_load_dwordx4 v[172:175], v22, s[16:19], s31 offen sc1\n"
    "v_mfma_f32_16x16x32_bf16 v[144:147], a[4:7], a[196:199], v[80:83]\n"
    "s_waitcnt lgkmcnt(4)\n"
    "ds_read_b128 a[180:183], v11 offset:62592\n"
    "v_mfma_f32_16x16x32_bf16 v[148:151], a[4:7], a[200:203], v[84:87]\n"
    "v_mfma_f32_16x16x32_bf16 v[144:147], a[8:11], a[200:203], v[144:147]\n"
    "s_waitcnt lgkmcnt(4)\n"
    "ds_read_b128 a[184:187], v11 offset:62720\n"
    "v_mfma_f32_16x16x32_bf16 v[148:151], a[8:11], a[192:195], v[148:151]\n"
    "v_cvt_pk_bf16_f32 v216, v136, v137\n"
    "v_cvt_pk_bf16_f32 v217, v138, v139\n"
    "v_mfma_f32_16x16x32_bf16 v[144:147], a[12:15], a[192:195], v[144:147]\n"
    "s_waitcnt lgkmcnt(4)\n"
    "ds_read_b128 a[192:195], v11 offset:62848\n"
    "v_cvt_pk_bf16_f32 v218, v140, v141\n"
    "v_mfma_f32_16x16x32_bf16 v[148:151], a[12:15], a[204:207], v[148:151]\n"
    "v_cvt_pk_bf16_f32 v219, v142, v143\n"
    "v_mfma_f32_16x16x32_bf16 v[144:147], a[16:19], a[204:207], v[144:147]\n"
    "s_waitcnt lgkmcnt(4)\n"
    "s_xor_b32 s24, s39, 65536\n"
    "ds_read_b128 a[196:199], v11 offset:62976\n"
    "v_mfma_f32_16x16x32_bf16 v[148:151], a[16:19], a[188:191], v[148:151]\n"
    "v_min_u32 v216, -2, v216\n"
    "v_min_u32 v217, -2, v217\n"
    "v_mfma_f32_16x16x32_bf16 v[144:147], a[20:23], a[188:191], v[144:147]\n"
    "s_waitcnt lgkmcnt(4)\n"
    "ds_read_b128 a[188:191], v11 offset:63104\n"
    "v_min_u32 v218, -2, v218\n"
    "v_mfma_f32_16x16x32_bf16 v[148:151], a[20:23], a[176:179], v[148:151]\n"
    "v_min_u32 v219, -2, v219\n"
    "v_mfma_f32_16x16x32_bf16 v[144:147], a[24:27], a[176:179], v[144:147]\n"
    "s_waitcnt lgkmcnt(4)\n"
    "ds_read_b128 a[176:179], v12 offset:62208\n"
    "v_mfma_f32_16x16x32_bf16 v[148:151], a[24:27], a[180:183], v[148:151]\n"
    "buffer_store_dwordx4 v[216:219], v22, s[16:19], s24 offen sc1\n"
    "v_mfma_f32_16x16x32_bf16 v[144:147], a[28:31], a[180:183], v[144:147]\n"
    "s_waitcnt lgkmcnt(4)\n"
    "ds_read_b128 a[180:183], v12 offset:62336\n"
    "v_cvt_pk_bf16_f32 v220, v128, v129\n"
    "v_mfma_f32_16x16x32_bf16 v[148:151], a[28:31], a[184:187], v[148:151]\n"
    "v_cvt_pk_bf16_f32 v221, v130, v131\n"
    "v_cvt_pk_bf16_f32 v222, v132, v133\n"
    "v_mfma_f32_16x16x32_bf16 v[144:147], a[32:35], a[184:187], v[144:147]\n"
    "s_waitcnt lgkmcnt(4)\n"
    "ds_read_b128 a[184:187], v12 offset:62464\n"
    "v_cvt_pk_bf16_f32 v223, v134, v135\n"
    "v_mfma_f32_16x16x32_bf16 v[148:151], a[32:35], a[192:195], v[148:151]\n"
    "s_xor_b32 s24, s39, 98304\n"
    "v_min_u32 v220, -2, v220\n"
    "v_min_u32 v221, -2, v221\n"
    "v_mfma_f32_16x16x32_bf16 v[144:147], a[36:39], a[192:195], v[144:147]\n"
    "s_waitcnt lgkmcnt(4)\n"
    "ds_read_b128 a[192:195], v12 offset:62592\n"
    "v_min_u32 v222, -2, v222\n"
    "v_mfma_f32_16x16x32_bf16 v[148:151], a[36:39], a[196:199], v[148:151]\n"
    "v_min_u32 v223, -2, v223\n"
    "v_mfma_f32_16x16x32_bf16 v[144:147], a[40:43], a[196:199], v[144:147]\n"
    "s_waitcnt lgkmcnt(4)\n"
    "ds_read_b128 a[196:199], v12 offset:62720\n"
    "v_mfma_f32_16x16x32_bf16 v[148:151], a[40:43], a[188:191], v[148:151]\n"
    "ds_read_b128 a[200:203], v13 offset:62848\n"
    "v_mfma_f32_16x16x32_bf16 v[144:147], a[44:47], a[188:191], v[144:147]\n"
    "s_waitcnt lgkmcnt(5)\n"
    "ds_read_b128 a[188:191], v13 offset:62976\n"
    "v_mfma_f32_16x16x32_bf16 v[148:151], a[44:47], a[176:179], v[148:151]\n"
    "buffer_store_dwordx4 v[220:223], v22, s[16:19], s24 offen sc1\n"
    "v_mfma_f32_16x16x32_bf16 v[144:147], a[48:51], a[176:179], v[144:147]\n"
    "s_waitcnt lgkmcnt(5)\n"
    "ds_read_b128 a[176:179], v13 offset:63104\n"
    "v_mfma_f32_16x16x32_bf16 v[148:151], a[48:51], a[180:183], v[148:151]\n"
    "v_mfma_f32_16x16x32_bf16 v[144:147], a[52:55], a[180:183], v[144:147]\n"
    "s_waitcnt lgkmcnt(5)\n"
    "ds_read_b128 a[180:183], v14 offset:62208\n"
    "v_mfma_f32_16x16x32_bf16 v[148:151], a[52:55], a[184:187], v[148:151]\n"
    "v_mfma_f32_16x16x32_bf16 v[144:147], a[56:59], a[184:187], v[144:147]\n"
    "s_waitcnt lgkmcnt(5)\n"
    "ds_read_b128 a[184:187], v14 offset:62336\n"
    "v_mfma_f32_16x16x32_bf16 v[148:151], a[56:59], a[192:195], v[148:151]\n"
    "v_mfma_f32_16x16x32_bf16 v[144:147], a[60:63], a[192:195], v[144:147]\n"
    "s_waitcnt lgkmcnt(5)\n"
    "ds_read_b128 a[192:195], v14 offset:62464\n"
    "v_mfma_f32_16x16x32_bf16 v[148:151], a[60:63], a[196:199], v[148:151]\n"
    "s_waitcnt lgkmcnt(5)\n"
    "v_mfma_f32_16x16x32_bf16 v[144:147], a[68:71], a[200:203], v[144:147]\n"
    "s_waitcnt lgkmcnt(4)\n"
    "ds_read_b128 a[196:199], v14 offset:62592\n"
    "v_mfma_f32_16x16x32_bf16 v[148:151], a[68:71], a[188:191], v[148:151]\n"
    "v_mfma_f32_16x16x32_bf16 v[144:147], a[72:75], a[188:191], v[144:147]\n"
    "s_waitcnt lgkmcnt(4)\n"
    "ds_read_b128 a[188:191], v14 offset:62720\n"
    "v_mfma_f32_16x16x32_bf16 v[148:151], a[72:75], a[176:179], v[148:151]\n"
    "v_mfma_f32_16x16x32_bf16 v[144:147], a[76:79], a[176:179], v[144:147]\n"
    "s_waitcnt lgkmcnt(4)\n"
    "ds_read_b128 a[176:179], v14 offset:62848\n"
    "v_mfma_f32_16x16x32_bf16 v[148:151], a[76:79], a[180:183], v[148:151]\n"
    "v_mfma_f32_16x16x32_bf16 v[144:147], a[80:83], a[180:183], v[144:147]\n"
    "s_waitcnt lgkmcnt(4)\n"
    "ds_read_b128 a[180:183], v14 offset:62976\n"
    "v_mfma_f32_16x16x32_bf16 v[148:151], a[80:83], a[184:187], v[148:151]\n"
    "v_mfma_f32_16x16x32_bf16 v[144:147], a[84:87], a[184:187], v[144:147]\n"
    "s_waitcnt lgkmcnt(4)\n"
    "ds_read_b128 a[184:187], v14 offset:63104\n"
    "v_mfma_f32_16x16x32_bf16 v[148:151], a[84:87], a[192:195], v[148:151]\n"
    "v_mfma_f32_16x16x32_bf16 v[144:147], a[88:91], a[192:195], v[144:147]\n"
    "s_waitcnt lgkmcnt(4)\n"
    "ds_read_b128 a[192:195], v15 offset:62208\n"
    "v_mfma_f32_16x16x32_bf16 v[148:151], a[88:91], a[196:199], v[148:151]\n"
    "v_mfma_f32_16x16x32_bf16 v[144:147], a[92:95], a[196:199], v[144:147]\n"
    "s_waitcnt lgkmcnt(4)\n"
    "ds_read_b128 a[196:199], v15 offset:62336\n"
    "v_mfma_f32_16x16x32_bf16 v[148:151], a[92:95], a[188:191], v[148:151]\n"
    "v_mfma_f32_16x16x32_bf16 v[144:147], a[96:99], a[188:191], v[144:147]\n"
    "s_waitcnt lgkmcnt(4)\n"
    "ds_read_b128 a[188:191], v15 offset:62464\n"
    "v_mfma_f32_16x16x32_bf16 v[148:151], a[96:99], a[176:179], v[148:151]\n"
    "v_mfma_f32_16x16x32_bf16 v[144:147], a[100:103], a[176:179], v[144:147]\n"
    "s_waitcnt lgkmcnt(4)\n"
    "ds_read_b128 a[176:179], v15 offset:62592\n"
    "v_mfma_f32_16x16x32_bf16 v[148:151], a[100:103], a[180:183], v[148:151]\n"
    "v_mfma_f32_16x16x32_bf16 v[144:147], a[104:107], a[180:183], v[144:147]\n"
    "s_waitcnt lgkmcnt(4)\n"
    "ds_read_b128 a[180:183], v15 offset:62720\n"
    "v_mfma_f32_16x16x32_bf16 v[148:151], a[104:107], a[184:187], v[148:151]\n"
    "ds_read_b128 a[200:203], v10 offset:63104\n"
    "v_mfma_f32_16x16x32_bf16 v[144:147], a[108:111], a[184:187], v[144:147]\n"
    "s_waitcnt lgkmcnt(5)\n"
    "ds_read_b128 a[184:187], v11 offset:62208\n"
    "v_mfma_f32_16x16x32_bf16 v[148:151], a[108:111], a[192:195], v[148:151]\n"
    "v_mfma_f32_16x16x32_bf16 v[144:147], a[112:115], a[192:195], v[144:147]\n"
    "s_waitcnt lgkmcnt(5)\n"
    "ds_read_b128 a[192:195], v11 offset:62336\n"
    "v_mfma_f32_16x16x32_bf16 v[148:151], a[112:115], a[196:199], v[148:151]\n"
    "v_mfma_f32_16x16x32_bf16 v[144:147], a[116:119], a[196:199], v[144:147]\n"
    "s_waitcnt lgkmcnt(5)\n"
    "ds_read_b128 a[196:199], v11 offset:62464\n"
    "v_mfma_f32_16x16x32_bf16 v[148:151], a[116:119], a[188:191], v[148:151]\n"
    "v_mfma_f32_16x16x32_bf16 v[144:147], a[120:123], a[188:191], v[144:147]\n"
    "s_waitcnt lgkmcnt(5)\n"
    "ds_read_b128 a[188:191], v11 offset:62592\n"
    "v_mfma_f32_16x16x32_bf16 v[148:151], a[120:123], a[176:179], v[148:151]\n"
    "v_mfma_f32_16x16x32_bf16 v[144:147], a[124:127], a[176:179], v[144:147]\n"
    "s_waitcnt lgkmcnt(5)\n"
    "ds_read_b128 a[176:179], v11 offset:62720\n"
    "v_mfma_f32_16x16x32_bf16 v[148:151], a[124:127], a[180:183], v[148:151]\n"
    "s_waitcnt lgkmcnt(5)\n"
    "v_mfma_f32_16x16x32_bf16 v[152:155], a[4:7], a[200:203], v[88:91]\n"
    "s_waitcnt lgkmcnt(4)\n"
    "ds_read_b128 a[180:183], v11 offset:62848\n"
    "v_mfma_f32_16x16x32_bf16 v[156:159], a[4:7], a[184:187], v[92:95]\n"
    "v_mfma_f32_16x16x32_bf16 v[152:155], a[8:11], a[184:187], v[152:155]\n"
    "s_waitcnt lgkmcnt(4)\n"
    "ds_read_b128 a[184:187], v11 offset:62976\n"
    "v_mfma_f32_16x16x32_bf16 v[156:159], a[8:11], a[192:195], v[156:159]\n"
    "v_cvt_pk_bf16_f32 v224, v144, v145\n"
    "v_cvt_pk_bf16_f32 v225, v146, v147\n"
    "v_mfma_f32_16x16x32_bf16 v[152:155], a[12:15], a[192:195], v[152:155]\n"
    "s_waitcnt lgkmcnt(4)\n"
    "ds_read_b128 a[192:195], v11 offset:63104\n"
    "v_cvt_pk_bf16_f32 v226, v148, v149\n"
    "v_mfma_f32_16x16x32_bf16 v[156:159], a[12:15], a[196:199], v[156:159]\n"
    "v_cvt_pk_bf16_f32 v227, v150, v151\n"
    "v_mfma_f32_16x16x32_bf16 v[152:155], a[16:19], a[196:199], v[152:155]\n"
    "s_waitcnt lgkmcnt(4)\n"
    "s_xor_b32 s24, s39, 32768\n"
    "ds_read_b128 a[196:199], v12 offset:62208\n"
    "v_mfma_f32_16x16x32_bf16 v[156:159], a[16:19], a[188:191], v[156:159]\n"
    "v_min_u32 v224, -2, v224\n"
    "v_min_u32 v225, -2, v225\n"
    "v_mfma_f32_16x16x32_bf16 v[152:155], a[20:23], a[188:191], v[152:155]\n"
    "s_waitcnt lgkmcnt(4)\n"
    "ds_read_b128 a[188:191], v12 offset:62336\n"
    "v_min_u32 v226, -2, v226\n"
    "v_mfma_f32_16x16x32_bf16 v[156:159], a[20:23], a[176:179], v[156:159]\n"
    "v_min_u32 v227, -2, v227\n"
    "v_mfma_f32_16x16x32_bf16 v[152:155], a[24:27], a[176:179], v[152:155]\n"
    "s_waitcnt lgkmcnt(4)\n"
    "ds_read_b128 a[176:179], v12 offset:62464\n"
    "v_mfma_f32_16x16x32_bf16 v[156:159], a[24:27], a[180:183], v[156:159]\n"
    "buffer_store_dwordx4 v[224:227], v22, s[16:19], s24 offen sc1\n"
    "v_mfma_f32_16x16x32_bf16 v[152:155], a[28:31], a[180:183], v[152:155]\n"
    "s_waitcnt lgkmcnt(4)\n"
    "ds_read_b128 a[180:183], v12 offset:62592\n"
    "v_mfma_f32_16x16x32_bf16 v[156:159], a[28:31], a[184:187], v[156:159]\n"
    "v_mfma_f32_16x16x32_bf16 v[152:155], a[32:35], a[184:187], v[152:155]\n"
    "s_waitcnt lgkmcnt(4)\n"
    "ds_read_b128 a[184:187], v12 offset:62720\n"
    "v_mfma_f32_16x16x32_bf16 v[156:159], a[32:35], a[192:195], v[156:159]\n"
    "v_mfma_f32_16x16x32_bf16 v[152:155], a[36:39], a[192:195], v[152:155]\n"
    "s_waitcnt lgkmcnt(4)\n"
    "ds_read_b128 a[192:195], v12 offset:62848\n"
    "v_mfma_f32_16x16x32_bf16 v[156:159], a[36:39], a[196:199], v[156:159]\n"
    "v_mfma_f32_16x16x32_bf16 v[152:155], a[40:43], a[196:199], v[152:155]\n"
    "s_waitcnt lgkmcnt(4)\n"
    "ds_read_b128 a[196:199], v12 offset:62976\n"
    "v_mfma_f32_16x16x32_bf16 v[156:159], a[40:43], a[188:191], v[156:159]\n"
    "ds_read_b128 a[200:203], v13 offset:63104\n"
    "v_mfma_f32_16x16x32_bf16 v[152:155], a[44:47], a[188:191], v[152:155]\n"
    "s_waitcnt lgkmcnt(5)\n"
    "s_waitcnt vmcnt(3)\n"
    "ds_read_b128 a[188:191], v14 offset:62208\n"
    "v_mfma_f32_16x16x32_bf16 v[156:159], a[44:47], a[176:179], v[156:159]\n"
    "v_max3_u32 v240, v160, v161, v162\n"
    "v_max3_u32 v240, v240, v163, v164\n"
    "v_mfma_f32_16x16x32_bf16 v[152:155], a[48:51], a[176:179], v[152:155]\n"
    "s_waitcnt lgkmcnt(5)\n"
    "ds_read_b128 a[176:179], v14 offset:62336\n"
    "v_max3_u32 v240, v240, v165, v166\n"
    "v_mfma_f32_16x16x32_bf16 v[156:159], a[48:51], a[180:183], v[156:159]\n"
    "buffer_load_dwordx4 v[176:179], v22, s[16:19], s26 offen sc1\n"
    "v_mfma_f32_16x16x32_bf16 v[152:155], a[52:55], a[180:183], v[152:155]\n"
    "s_waitcnt lgkmcnt(5)\n"
    "ds_read_b128 a[180:183], v14 offset:62464\n"
    "v_max3_u32 v240, v240, v167, v168\n"
    "v_mfma_f32_16x16x32_bf16 v[156:159], a[52:55], a[184:187], v[156:159]\n"
    "v_max3_u32 v240, v240, v169, v170\n"
    "v_max3_u32 v240, v240, v171, v172\n"
    "v_mfma_f32_16x16x32_bf16 v[152:155], a[56:59], a[184:187], v[152:155]\n"
    "s_waitcnt lgkmcnt(5)\n"
    "ds_read_b128 a[184:187], v14 offset:62592\n"
    "v_max3_u32 v240, v240, v173, v174\n"
    "v_mfma_f32_16x16x32_bf16 v[156:159], a[56:59], a[192:195], v[156:159]\n"
    "buffer_load_dwordx4 v[180:183], v22, s[16:19], s27 offen sc1\n"
    "v_mfma_f32_16x16x32_bf16 v[152:155], a[60:63], a[192:195], v[152:155]\n"
    "s_waitcnt lgkmcnt(5)\n"
    "ds_read_b128 a[192:195], v14 offset:62720\n"
    "v_max_u32 v240, v240, v175\n"
    "v_mfma_f32_16x16x32_bf16 v[156:159], a[60:63], a[196:199], v[156:159]\n"
    "s_waitcnt lgkmcnt(5)\n"
    "v_cmp_eq_u32 vcc, -1, v240\n"
    "s_cbranch_vccz LE3ok_%=\n"
    "s_mov_b32 s43, 0x100000\n"
    "LE3lp_%=:\n"
    "buffer_load_dwordx4 v[160:163], v22, s[16:19], s28 offen sc1\n"
    "buffer_load_dwordx4 v[164:167], v22, s[16:19], s30 offen sc1\n"
    "buffer_load_dwordx4 v[168:171], v22, s[16:19], s29 offen sc1\n"
    "buffer_load_dwordx4 v[172:175], v22, s[16:19], s31 offen sc1\n"
    "s_waitcnt vmcnt(0)\n"
    "s_sub_u32 s43, s43, 1\n"
    "s_cbranch_scc1 LE3ok_%=\n"
    "v_max3_u32 v240, v160, v161, v162\n"
    "v_max3_u32 v240, v240, v163, v164\n"
    "v_max3_u32 v240, v240, v165, v166\n"
    "v_max3_u32 v240, v240, v167, v168\n"
    "v_max3_u32 v240, v240, v169, v170\n"
    "v_max3_u32 v240, v240, v171, v172\n"
    "v_max3_u32 v240, v240, v173, v174\n"
    "v_max_u32 v240, v240, v175\n"
    "v_cmp_eq_u32 vcc, -1, v240\n"
    "s_cbranch_vccnz LE3lp_%=\n"
    "LE3ok_%=:\n"
    "v_mfma_f32_16x16x32_bf16 v[152:155], a[68:71], a[200:203], v[152:155]\n"
    "s_waitcnt lgkmcnt(4)\n"
    "ds_read_b128 a[196:199], v14 offset:62848\n"
    "v_lshlrev_b32 v188, 16, v160\n"
    "v_mfma_f32_16x16x32_bf16 v[156:159], a[68:71], a[188:191], v[156:159]\n"
    "v_and_b32 v189, s48, v160\n"
    "v_lshlrev_b32 v190, 16, v161\n"
    "v_mfma_f32_16x16x32_bf16 v[152:155], a[72:75], a[188:191], v[152:155]\n"
    "s_waitcnt lgkmcnt(4)\n"
    "ds_read_b128 a[188:191], v14 offset:62976\n"
    "v_and_b32 v191, s48, v161\n"
    "v_mfma_f32_16x16x32_bf16 v[156:159], a[72:75], a[176:179], v[156:159]\n"
    "v_lshlrev_b32 v192, 16, v162\n"
    "v_and_b32 v193, s48, v162\n"
    "v_mfma_f32_16x16x32_bf16 v[152:155], a[76:79], a[176:179], v[152:155]\n"
    "s_waitcnt lgkmcnt(4)\n"
    "ds_read_b128 a[176:179], v14 offset:63104\n"
    "v_lshlrev_b32 v194, 16, v163\n"
    "v_mfma_f32_16x16x32_bf16 v[156:159], a[76:79], a[180:183], v[156:159]\n"
    "v_and_b32 v195, s48, v163\n"
    "v_lshlrev_b32 v240, 16, v164\n"
    "v_mfma_f32_16x16x32_bf16 v[152:155], a[80:83], a[180:183], v[152:155]\n"
    "s_waitcnt lgkmcnt(4)\n"
    "ds_read_b128 a[180:183], v15 offset:62208\n"
    "v_and_b32 v241, s48, v164\n"
    "v_mfma_f32_16x16x32_bf16 v[156:159], a[80:83], a[184:187], v[156:159]\n"
    "v_lshlrev_b32 v242, 16, v165\n"
    "v_and_b32 v243, s48, v165\n"
    "v_mfma_f32_16x16x32_bf16 v[152:155], a[84:87], a[184:187], v[152:155]\n"
    "s_waitcnt lgkmcnt(4)\n"
    "ds_read_b128 a[184:187], v15 offset:62336\n"
    "v_lshlrev_b32 v244, 16, v166\n"
    "v_mfma_f32_16x16x32_bf16 v[156:159], a[84:87], a[192:195], v[156:159]\n"
    "v_and_b32 v245, s48, v166\n"
    "v_lshlrev_b32 v246, 16, v167\n"
    "v_mfma_f32_16x16x32_bf16 v[152:155], a[88:91], a[192:195], v[152:155]\n"
    "s_waitcnt lgkmcnt(4)\n"
    "ds_read_b128 a[192:195], v15 offset:62464\n"
    "v_and_b32 v247, s48, v167\n"
    "v_mfma_f32_16x16x32_bf16 v[156:159], a[88:91], a[196:199], v[156:159]\n"
    "v_add_f32 v188, v188, v240\n"
    "v_add_f32 v189, v189, v241\n"
    "v_mfma_f32_16x16x32_bf16 v[152:155], a[92:95], a[196:199], v[152:155]\n"
    "s_waitcnt lgkmcnt(4)\n"
    "ds_read_b128 a[196:199], v15 offset:62592\n"
    "v_add_f32 v190, v190, v242\n"
    "v_mfma_f32_16x16x32_bf16 v[156:159], a[92:95], a[188:191], v[156:159]\n"
    "v_add_f32 v191, v191, v243\n"
    "v_add_f32 v192, v192, v244\n"
    "v_mfma_f32_16x16x32_bf16 v[152:155], a[96:99], a[188:191], v[152:155]\n"
    "s_waitcnt lgkmcnt(4)\n"
    "ds_read_b128 a[188:191], v15 offset:62720\n"
    "v_add_f32 v193, v193, v245\n"
    "v_mfma_f32_16x16x32_bf16 v[156:159], a[96:99], a[176:179], v[156:159]\n"
    "v_add_f32 v194, v194, v246\n"
    "v_add_f32 v195, v195, v247\n"
    "v_mfma_f32_16x16x32_bf16 v[152:155], a[100:103], a[176:179], v[152:155]\n"
    "s_waitcnt lgkmcnt(4)\n"
    "ds_read_b128 a[176:179], v15 offset:62848\n"
    "v_lshlrev_b32 v40, 16, v168\n"
    "v_mfma_f32_16x16x32_bf16 v[156:159], a[100:103], a[180:183], v[156:159]\n"
    "v_and_b32 v41, s48, v168\n"
    "v_lshlrev_b32 v42, 16, v169\n"
    "v_mfma_f32_16x16x32_bf16 v[152:155], a[104:107], a[180:183], v[152:155]\n"
    "s_waitcnt lgkmcnt(4)\n"
    "ds_read_b128 a[180:183], v15 offset:62976\n"
    "v_and_b32 v43, s48, v169\n"
    "v_mfma_f32_16x16x32_bf16 v[156:159], a[104:107], a[184:187], v[156:159]\n"
    "v_lshlrev_b32 v44, 16, v170\n"
    "v_and_b32 v45, s48, v170\n"
    "v_mfma_f32_16x16x32_bf16 v[152:155], a[108:111], a[184:187], v[152:155]\n"
    "s_waitcnt lgkmcnt(4)\n"
    "buffer_load_dwordx4 v[184:187], v22, s[16:19], s25 offen sc1\n"
    "v_mfma_f32_16x16x32_bf16 v[156:159], a[108:111], a[192:195], v[156:159]\n"
    "v_lshlrev_b32 v46, 16, v171\n"
    "v_and_b32 v47, s48, v171\n"
    "v_mfma_f32_16x16x32_bf16 v[152:155], a[112:115], a[192:195], v[152:155]\n"
    "s_waitcnt lgkmcnt(3)\n"
    "v_lshlrev_b32 v240, 16, v172\n"
    "v_and_b32 v241, s48, v172\n"
    "v_mfma_f32_16x16x32_bf16 v[156:159], a[112:115], a[196:199], v[156:159]\n"
    "v_lshlrev_b32 v242, 16, v173\n"
    "v_and_b32 v243, s48, v173\n"
    "v_mfma_f32_16x16x32_bf16 v[152:155], a[116:119], a[196:199], v[152:155]\n"
    "s_waitcnt lgkmcnt(2)\n"
    "v_lshlrev_b32 v244, 16, v174\n"
    "v_and_b32 v245, s48, v174\n"
    "v_mfma_f32_16x16x32_bf16 v[156:159], a[116:119], a[188:191], v[156:159]\n"
    "v_lshlrev_b32 v246, 16, v175\n"
    "v_and_b32 v247, s48, v175\n"
    "v_mfma_f32_16x16x32_bf16 v[152:155], a[120:123], a[188:191], v[152:155]\n"
    "s_waitcnt lgkmcnt(1)\n"
    "v_add_f32 v40, v40, v240\n"
    "v_add_f32 v41, v41, v241\n"
    "v_mfma_f32_16x16x32_bf16 v[156:159], a[120:123], a[176:179], v[156:159]\n"
    "v_add_f32 v42, v42, v242\n"
    "v_add_f32 v43, v43, v243\n"
    "v_mfma_f32_16x16x32_bf16 v[152:155], a[124:127], a[176:179], v[152:155]\n"
    "s_waitcnt lgkmcnt(0)\n"
    "v_add_f32 v44, v44, v244\n"
    "v_add_f32 v45, v45, v245\n"
    "v_mfma_f32_16x16x32_bf16 v[156:159], a[124:127], a[180:183], v[156:159]\n"
    "v_add_f32 v46, v46, v246\n"
    "v_add_f32 v47, v47, v247\n"
    "s_waitcnt vmcnt(1)\n"
    "v_max3_u32 v240, v176, v177, v178\n"
    "v_max3_u32 v240, v240, v179, v180\n"
    "v_max3_u32 v240, v240, v181, v182\n"
    "v_max_u32 v240, v240, v183\n"
    "v_cmp_eq_u32 vcc, -1, v240\n"
    "s_cbranch_vccz LM3ok_%=\n"
    "s_mov_b32 s43, 0x100000\n"
    "LM3lp_%=:\n"
    "buffer_load_dwordx4 v[176:179], v22, s[16:19], s26 offen sc1\n"
    "buffer_load_dwordx4 v[180:183], v22, s[16:19], s27 offen sc1\n"
    "s_waitcnt vmcnt(0)\n"
    "s_sub_u32 s43, s43, 1\n"
    "s_cbranch_scc1 LM3ok_%=\n"
    "v_max3_u32 v240, v176, v177, v178\n"
    "v_max3_u32 v240, v240, v179, v180\n"
    "v_max3_u32 v240, v240, v181, v182\n"
    "v_max_u32 v240, v240, v183\n"
    "v_cmp_eq_u32 vcc, -1, v240\n"
    "s_cbranch_vccnz LM3lp_%=\n"
    "LM3ok_%=:\n"
    "v_mfma_f32_16x16x32_bf16 v[152:155], v[236:239], v[176:179], v[152:155]\n"
    "v_mfma_f32_16x16x32_bf16 v[156:159], v[234:237], v[176:179], v[156:159]\n"
    "v_mfma_f32_16x16x32_bf16 v[40:43], v[236:239], v[180:183], v[40:43]\n"
    "v_mfma_f32_16x16x32_bf16 v[44:47], v[234:237], v[180:183], v[44:47]\n"
    "s_nop 4\n"
    "v_pk_add_f32 v[152:153], v[152:153], v[188:189]\n"
    "v_pk_add_f32 v[154:155], v[154:155], v[190:191]\n"
    "v_pk_add_f32 v[156:157], v[156:157], v[192:193]\n"
    "v_pk_add_f32 v[158:159], v[158:159], v[194:195]\n"
    "v_pk_mul_f32 v[200:201], v[152:153], s[44:45] op_sel:[0,1] op_sel_hi:[1,1]\n"
    "v_pk_mul_f32 v[202:203], v[154:155], s[44:45] op_sel:[0,1] op_sel_hi:[1,1]\n"
    "v_pk_mul_f32 v[204:205], v[156:157], s[44:45] op_sel:[0,1] op_sel_hi:[1,1]\n"
    "v_pk_mul_f32 v[206:207], v[158:159], s[44:45] op_sel:[0,1] op_sel_hi:[1,1]\n"
    "v_exp_f32 v200, v200\n"
    "v_exp_f32 v201, v201\n"
    "v_exp_f32 v202, v202\n"
    "v_exp_f32 v203, v203\n"
    "v_exp_f32 v204, v204\n"
    "v_exp_f32 v205, v205\n"
    "v_exp_f32 v206, v206\n"
    "v_exp_f32 v207, v207\n"
    "v_pk_add_f32 v[200:201], v[200:201], 1.0 op_sel_hi:[1,0]\n"
    "v_pk_add_f32 v[202:203], v[202:203], 1.0 op_sel_hi:[1,0]\n"
    "v_pk_add_f32 v[204:205], v[204:205], 1.0 op_sel_hi:[1,0]\n"
    "v_pk_add_f32 v[206:207], v[206:207], 1.0 op_sel_hi:[1,0]\n"
    "s_waitcnt vmcnt(0)\n"
    "v_max3_u32 v188, v184, v185, v186\n"
    "v_max_u32 v188, v188, v187\n"
    "v_cmp_eq_u32 vcc, -1, v188\n"
    "s_cbranch_vccz LL3ok_%=\n"
    "s_mov_b32 s43, 0x100000\n"
    "LL3lp_%=:\n"
    "buffer_load_dwordx4 v[184:187], v22, s[16:19], s25 offen sc1\n"
    "s_waitcnt vmcnt(0)\n"
    "s_sub_u32 s43, s43, 1\n"
    "s_cbranch_scc1 LL3ok_%=\n"
    "v_max3_u32 v188, v184, v185, v186\n"
    "v_max_u32 v188, v188, v187\n"
    "v_cmp_eq_u32 vcc, -1, v188\n"
    "s_cbranch_vccnz LL3lp_%=\n"
    "LL3ok_%=:\n"
    "v_lshlrev_b32 v188, 16, v184\n"
    "v_and_b32 v189, s48, v184\n"
    "v_lshlrev_b32 v190, 16, v185\n"
    "v_and_b32 v191, s48, v185\n"
    "v_lshlrev_b32 v192, 16, v186\n"
    "v_and_b32 v193, s48, v186\n"
    "v_lshlrev_b32 v194, 16, v187\n"
    "v_and_b32 v195, s48, v187\n"
    "v_pk_add_f32 v[40:41], v[40:41], v[188:189]\n"
    "v_pk_add_f32 v[42:43], v[42:43], v[190:191]\n"
    "v_pk_add_f32 v[44:45], v[44:45], v[192:193]\n"
    "v_pk_add_f32 v[46:47], v[46:47], v[194:195]\n"
    "v_pk_mul_f32 v[240:241], v[40:41], s[46:47] op_sel_hi:[1,0]\n"
    "v_pk_mul_f32 v[242:243], v[42:43], s[46:47] op_sel_hi:[1,0]\n"
    "v_pk_mul_f32 v[244:245], v[44:45], s[46:47] op_sel_hi:[1,0]\n"
    "v_pk_mul_f32 v[246:247], v[46:47], s[46:47] op_sel_hi:[1,0]\n"
    "v_exp_f32 v240, -|v240|\n"
    "v_fma_f32 v248, -v40, v40, |v40|\n"
    "v_exp_f32 v241, -|v241|\n"
    "v_fma_f32 v249, -v41, v41, |v41|\n"
    "v_exp_f32 v242, -|v242|\n"
    "v_fma_f32 v250, -v42, v42, |v42|\n"
    "v_exp_f32 v243, -|v243|\n"
    "v_fma_f32 v251, -v43, v43, |v43|\n"
    "v_exp_f32 v244, -|v244|\n"
    "v_fma_f32 v252, -v44, v44, |v44|\n"
    "v_exp_f32 v245, -|v245|\n"
    "v_fma_f32 v253, -v45, v45, |v45|\n"
    "v_exp_f32 v246, -|v246|\n"
    "v_fma_f32 v254, -v46, v46, |v46|\n"
    "v_exp_f32 v247, -|v247|\n"
    "v_fma_f32 v255, -v47, v47, |v47|\n"
    "v_pk_fma_f32 v[168:169], v[240:241], 0.5, 0.5 op_sel_hi:[1,0,0] neg_lo:[1,0,0] neg_hi:[1,0,0]\n"
    "v_pk_fma_f32 v[170:171], v[242:243], 0.5, 0.5 op_sel_hi:[1,0,0] neg_lo:[1,0,0] neg_hi:[1,0,0]\n"
    "v_pk_fma_f32 v[172:173], v[244:245], 0.5, 0.5 op_sel_hi:[1,0,0] neg_lo:[1,0,0] neg_hi:[1,0,0]\n"
    "v_pk_fma_f32 v[174:175], v[246:247], 0.5, 0.5 op_sel_hi:[1,0,0] neg_lo:[1,0,0] neg_hi:[1,0,0]\n"
    "v_pk_fma_f32 v[240:241], v[240:241], 0.5, 0.5 op_sel_hi:[1,0,0]\n"
    "v_pk_fma_f32 v[242:243], v[242:243], 0.5, 0.5 op_sel_hi:[1,0,0]\n"
    "v_pk_fma_f32 v[244:245], v[244:245], 0.5, 0.5 op_sel_hi:[1,0,0]\n"
    "v_pk_fma_f32 v[246:247], v[246:247], 0.5, 0.5 op_sel_hi:[1,0,0]\n"
    "v_med3_f32 v248, v168, v248, |v40|\n"
    "v_med3_f32 v249, v169, v249, |v41|\n"
    "v_med3_f32 v250, v170, v250, |v42|\n"
    "v_med3_f32 v251, v171, v251, |v43|\n"
    "v_med3_f32 v252, v172, v252, |v44|\n"
    "v_med3_f32 v253, v173, v253, |v45|\n"
    "v_med3_f32 v254, v174, v254, |v46|\n"
    "v_med3_f32 v255, v175, v255, |v47|\n"
    "v_bfi_b32 v248, s47, v248, v40\n"
    "v_bfi_b32 v249, s47, v249, v41\n"
    "v_bfi_b32 v250, s47, v250, v42\n"
    "v_bfi_b32 v251, s47, v251, v43\n"
    "v_bfi_b32 v252, s47, v252, v44\n"
    "v_bfi_b32 v253, s47, v253, v45\n"
    "v_bfi_b32 v254, s47, v254, v46\n"
    "v_bfi_b32 v255, s47, v255, v47\n"
    "v_pk_mul_f32 v[240:241], v[240:241], v[200:201]\n"
    "v_pk_mul_f32 v[242:243], v[242:243], v[202:203]\n"
    "v_pk_mul_f32 v[244:245], v[244:245], v[204:205]\n"
    "v_pk_mul_f32 v[246:247], v[246:247], v[206:207]\n"
    "v_rcp_f32 v240, v240\n"
    "v_rcp_f32 v241, v241\n"
    "v_rcp_f32 v242, v242\n"
    "v_rcp_f32 v243, v243\n"
    "v_rcp_f32 v244, v244\n"
    "v_rcp_f32 v245, v245\n"
    "v_rcp_f32 v246, v246\n"
    "v_rcp_f32 v247, v247\n"
    "v_pk_mul_f32 v[248:249], v[248:249], v[240:241]\n"
    "v_pk_mul_f32 v[250:251], v[250:251], v[242:243]\n"
    "v_pk_mul_f32 v[252:253], v[252:253], v[244:245]\n"
    "v_pk_mul_f32 v[254:255], v[254:255], v[246:247]\n"
    "v_cvt_pk_bf16_f32 v240, v248, v249\n"
    "v_cvt_pk_bf16_f32 v241, v250, v251\n"
    "v_cvt_pk_bf16_f32 v242, v252, v253\n"
    "v_cvt_pk_bf16_f32 v243, v254, v255\n"
    "buffer_store_dwordx2 v[240:241], v25, s[20:23], 0 offen\n"
    "buffer_store_dwordx2 v[242:243], v25, s[20:23], 0 offen offset:2048\n"
    "s_endpgm\n"
    ".fill (LV0_%=+24576-.)/4, 4, 0xbf800000\n"
    "LV4_%=:\n"
    "ds_read_b128 a[192:195], v10 offset:62720\n"
    "v_mfma_f32_16x16x32_bf16 v[168:171], v[192:195], a[212:215], v[168:171]\n"
    "s_waitcnt lgkmcnt(2)\n"
    "ds_read_b128 a[196:199], v10 offset:62848\n"
    "v_mfma_f32_16x16x32_bf16 v[172:175], v[192:195], a[184:187], v[172:175]\n"
    "ds_read_b128 a[184:187], v10 offset:62976\n"
    "v_mfma_f32_16x16x32_bf16 v[96:99], a[138:141], a[236:239], v[96:99]\n"
    "ds_read_b128 a[200:203], v10 offset:63104\n"
    "v_mfma_f32_16x16x32_bf16 v[100:103], a[138:141], a[228:231], v[100:103]\n"
    "s_nop 0\n"
    "v_add_f32 v80, v80, v168\n"
    "v_add_f32 v81, v81, v169\n"
    "v_mfma_f32_16x16x32_bf16 v[104:107], a[138:141], a[204:207], v[104:107]\n"
    "v_add_f32 v82, v82, v170\n"
    "v_add_f32 v83, v83, v171\n"
    "v_mfma_f32_16x16x32_bf16 v[108:111], a[138:141], a[220:223], v[108:111]\n"
    "v_add_f32 v84, v84, v168\n"
    "v_add_f32 v85, v85, v169\n"
    "v_mfma_f32_16x16x32_bf16 v[112:115], a[138:141], a[176:179], v[112:115]\n"
    "ds_read_b128 a[176:179], v11 offset:62208\n"
    "v_add_f32 v86, v86, v170\n"
    "v_mfma_f32_16x16x32_bf16 v[116:119], a[138:141], a[232:235], v[116:119]\n"
    "v_add_f32 v87, v87, v171\n"
    "v_add_f32 v88, v88, v172\n"
    "v_mfma_f32_16x16x32_bf16 v[120:123], a[138:141], a[180:183], v[120:123]\n"
    "s_waitcnt lgkmcnt(5)\n"
    "v_add_f32 v89, v89, v173\n"
    "v_add_f32 v90, v90, v174\n"
    "v_mfma_f32_16x16x32_bf16 v[124:127], a[138:141], a[188:191], v[124:127]\n"
    "s_waitcnt lgkmcnt(4)\n"
    "v_add_f32 v91, v91, v175\n"
    "v_add_f32 v92, v92, v172\n"
    "v_mfma_f32_16x16x32_bf16 v[144:147], a[0:3], a[192:195], v[80:83]\n"
    "s_waitcnt lgkmcnt(3)\n"
    "ds_read_b128 a[180:183], v11 offset:62336\n"
    "v_add_f32 v93, v93, v173\n"
    "v_mfma_f32_16x16x32_bf16 v[148:151], a[0:3], a[196:199], v[84:87]\n"
    "s_waitcnt lgkmcnt(3)\n"
    "v_add_f32 v94, v94, v174\n"
    "v_add_f32 v95, v95, v175\n"
    "v_mfma_f32_16x16x32_bf16 v[152:155], a[0:3], a[184:187], v[88:91]\n"
    "s_waitcnt lgkmcnt(2)\n"
    "v_add_f32 v64, v64, v160\n"
    "v_add_f32 v65, v65, v161\n"
    "v_mfma_f32_16x16x32_bf16 v[156:159], a[0:3], a[200:203], v[92:95]\n"
    "v_add_f32 v66, v66, v162\n"
    "v_add_f32 v67, v67, v163\n"
    "v_mfma_f32_16x16x32_bf16 v[144:147], a[4:7], a[196:199], v[144:147]\n"
    "ds_read_b128 a[188:191], v11 offset:62464\n"
    "v_add_f32 v68, v68, v160\n"
    "v_mfma_f32_16x16x32_bf16 v[148:151], a[4:7], a[184:187], v[148:151]\n"
    "v_add_f32 v69, v69, v161\n"
    "v_add_f32 v70, v70, v162\n"
    "v_mfma_f32_16x16x32_bf16 v[152:155], a[4:7], a[200:203], v[152:155]\n"
    "s_waitcnt lgkmcnt(2)\n"
    "v_add_f32 v71, v71, v163\n"
    "v_add_f32 v72, v72, v164\n"
    "v_mfma_f32_16x16x32_bf16 v[156:159], a[4:7], a[176:179], v[156:159]\n"
    "v_add_f32 v73, v73, v165\n"
    "v_add_f32 v74, v74, v166\n"
    "v_mfma_f32_16x16x32_bf16 v[144:147], a[8:11], a[184:187], v[144:147]\n"
    "ds_read_b128 a[184:187], v11 offset:62592\n"
    "v_add_f32 v75, v75, v167\n"
    "v_mfma_f32_16x16x32_bf16 v[148:151], a[8:11], a[200:203], v[148:151]\n"
    "v_add_f32 v76, v76, v164\n"
    "v_add_f32 v77, v77, v165\n"
    "v_mfma_f32_16x16x32_bf16 v[152:155], a[8:11], a[176:179], v[152:155]\n"
    "s_waitcnt lgkmcnt(2)\n"
    "v_add_f32 v78, v78, v166\n"
    "v_add_f32 v79, v79, v167\n"
    "v_mfma_f32_16x16x32_bf16 v[156:159], a[8:11], a[180:183], v[156:159]\n"
    "v_add_f32 v96, v96, v64\n"
    "v_add_f32 v97, v97, v65\n"
    "v_mfma_f32_16x16x32_bf16 v[144:147], a[12:15], a[200:203], v[144:147]\n"
    "ds_read_b128 a[192:195], v11 offset:62720\n"
    "v_add_f32 v98, v98, v66\n"
    "v_mfma_f32_16x16x32_bf16 v[148:151], a[12:15], a[176:179], v[148:151]\n"
    "v_add_f32 v99, v99, v67\n"
    "v_add_f32 v100, v100, v68\n"
    "v_mfma_f32_16x16x32_bf16 v[152:155], a[12:15], a[180:183], v[152:155]\n"
    "s_waitcnt lgkmcnt(2)\n"
    "v_add_f32 v101, v101, v69\n"
    "v_add_f32 v102, v102, v70\n"
    "v_mfma_f32_16x16x32_bf16 v[156:159], a[12:15], a[188:191], v[156:159]\n"
    "v_add_f32 v103, v103, v71\n"
    "v_cvt_pk_bf16_f32 v200, v96, v97\n"
    "v_mfma_f32_16x16x32_bf16 v[144:147], a[16:19], a[176:179], v[144:147]\n"
    "ds_read_b128 a[176:179], v11 offset:62848\n"
    "v_cvt_pk_bf16_f32 v201, v98, v99\n"
    "v_mfma_f32_16x16x32_bf16 v[148:151], a[16:19], a[180:183], v[148:151]\n"
    "v_cvt_pk_bf16_f32 v202, v100, v101\n"
    "v_cvt_pk_bf16_f32 v203, v102, v103\n"
    "v_mfma_f32_16x16x32_bf16 v[152:155], a[16:19], a[188:191], v[152:155]\n"
    "s_waitcnt lgkmcnt(2)\n"
    "s_xor_b32 s36, s39, 131072\n"
    "v_min_u32 v200, -2, v200\n"
    "v_mfma_f32_16x16x32_bf16 v[156:159], a[16:19], a[184:187], v[156:159]\n"
    "v_min_u32 v201, -2, v201\n"
    "v_min_u32 v202, -2, v202\n"
    "v_mfma_f32_16x16x32_bf16 v[144:147], a[20:23], a[180:183], v[144:147]\n"
    "ds_read_b128 a[180:183], v11 offset:62976\n"
    "v_min_u32 v203, -2, v203\n"
    "v_mfma_f32_16x16x32_bf16 v[148:151], a[20:23], a[188:191], v[148:151]\n"
    "v_add_f32 v104, v104, v72\n"
    "v_add_f32 v105, v105, v73\n"
    "v_mfma_f32_16x16x32_bf16 v[152:155], a[20:23], a[184:187], v[152:155]\n"
    "s_waitcnt lgkmcnt(2)\n"
    "v_add_f32 v106, v106, v74\n"
    "v_add_f32 v107, v107, v75\n"
    "v_mfma_f32_16x16x32_bf16 v[156:159], a[20:23], a[192:195], v[156:159]\n"
    "v_add_f32 v108, v108, v76\n"
    "v_add_f32 v109, v109, v77\n"
    "v_mfma_f32_16x16x32_bf16 v[144:147], a[24:27], a[188:191], v[144:147]\n"
    "ds_read_b128 a[188:191], v11 offset:63104\n"
    "v_add_f32 v110, v110, v78\n"
    "v_mfma_f32_16x16x32_bf16 v[148:151], a[24:27], a[184:187], v[148:151]\n"
    "v_add_f32 v111, v111, v79\n"
    "v_cvt_pk_bf16_f32 v204, v104, v105\n"
    "v_mfma_f32_16x16x32_bf16 v[152:155], a[24:27], a[192:195], v[152:155]\n"
    "s_waitcnt lgkmcnt(2)\n"
    "v_cvt_pk_bf16_f32 v205, v106, v107\n"
    "v_cvt_pk_bf16_f32 v206, v108, v109\n"
    "v_mfma_f32_16x16x32_bf16 v[156:159], a[24:27], a[176:179], v[156:159]\n"
    "v_cvt_pk_bf16_f32 v207, v110, v111\n"
    "v_mfma_f32_16x16x32_bf16 v[144:147], a[28:31], a[184:187], v[144:147]\n"
    "s_xor_b32 s36, s39, 163840\n"
    "ds_read_b128 a[184:187], v12 offset:62208\n"
    "v_min_u32 v204, -2, v204\n"
    "v_mfma_f32_16x16x32_bf16 v[148:151], a[28:31], a[192:195], v[148:151]\n"
    "v_min_u32 v205, -2, v205\n"
    "v_min_u32 v206, -2, v206\n"
    "v_mfma_f32_16x16x32_bf16 v[152:155], a[28:31], a[176:179], v[152:155]\n"
    "s_waitcnt lgkmcnt(2)\n"
    "v_min_u32 v207, -2, v207\n"
    "v_mfma_f32_16x16x32_bf16 v[156:159], a[28:31], a[180:183], v[156:159]\n"
    "s_waitcnt vmcnt(0)\n"
    "v_cmp_ne_u64 vcc, v[28:29], v[26:27]\n"
    "s_cbranch_vccz LF4ok_%=\n"
    "s_mov_b32 s43, 0x100000\n"
    "LF4lp_%=:\n"
    "buffer_load_dwordx2 v[28:29], v23, s[16:19], s40 offen offset:0 sc1\n"
    "s_waitcnt vmcnt(0)\n"
    "s_sub_u32 s43, s43, 1\n"
    "s_cbranch_scc1 LF4ok_%=\n"
    "v_cmp_ne_u64 vcc, v[28:29], v[26:27]\n"
    "s_cbranch_vccnz LF4lp_%=\n"
    "LF4ok_%=:\n"
    "v_mfma_f32_16x16x32_bf16 v[144:147], a[32:35], a[192:195], v[144:147]\n"
    "s_xor_b32 s36, s39, 131072\n"
    "ds_read_b128 a[192:195], v12 offset:62336\n"
    "v_mfma_f32_16x16x32_bf16 v[148:151], a[32:35], a[176:179], v[148:151]\n"
    "buffer_store_dwordx4 v[200:203], v22, s[16:19], s36 offen sc1\n"
    "v_mfma_f32_16x16x32_bf16 v[152:155], a[32:35], a[180:183], v[152:155]\n"
    "s_waitcnt lgkmcnt(2)\n"
    "s_xor_b32 s36, s39, 163840\n"
    "v_mfma_f32_16x16x32_bf16 v[156:159], a[32:35], a[188:191], v[156:159]\n"
    "v_mfma_f32_16x16x32_bf16 v[144:147], a[36:39], a[176:179], v[144:147]\n"
    "ds_read_b128 a[176:179], v12 offset:62464\n"
    "v_mfma_f32_16x16x32_bf16 v[148:151], a[36:39], a[180:183], v[148:151]\n"
    "buffer_store_dwordx4 v[204:207], v22, s[16:19], s36 offen sc1\n"
    "v_mfma_f32_16x16x32_bf16 v[152:155], a[36:39], a[188:191], v[152:155]\n"
    "s_waitcnt lgkmcnt(2)\n"
    "v_add_f32 v112, v112, v80\n"
    "v_add_f32 v113, v113, v81\n"
    "v_mfma_f32_16x16x32_bf16 v[156:159], a[36:39], a[184:187], v[156:159]\n"
    "v_add_f32 v114, v114, v82\n"
    "v_add_f32 v115, v115, v83\n"
    "v_mfma_f32_16x16x32_bf16 v[144:147], a[40:43], a[180:183], v[144:147]\n"
    "ds_read_b128 a[180:183], v12 offset:62592\n"
    "v_add_f32 v116, v116, v84\n"
    "v_mfma_f32_16x16x32_bf16 v[148:151], a[40:43], a[188:191], v[148:151]\n"
    "v_add_f32 v117, v117, v85\n"
    "v_add_f32 v118, v118, v86\n"
    "v_mfma_f32_16x16x32_bf16 v[152:155], a[40:43], a[184:187], v[152:155]\n"
    "s_waitcnt lgkmcnt(2)\n"
    "v_add_f32 v119, v119, v87\n"
    "v_cvt_pk_bf16_f32 v208, v112, v113\n"
    "v_mfma_f32_16x16x32_bf16 v[156:159], a[40:43], a[192:195], v[156:159]\n"
    "v_cvt_pk_bf16_f32 v209, v114, v115\n"
    "v_cvt_pk_bf16_f32 v210, v116, v117\n"
    "v_mfma_f32_16x16x32_bf16 v[144:147], a[44:47], a[188:191], v[144:147]\n"
    "ds_read_b128 a[188:191], v12 offset:62720\n"
    "v_cvt_pk_bf16_f32 v211, v118, v119\n"
    "v_mfma_f32_16x16x32_bf16 v[148:151], a[44:47], a[184:187], v[148:151]\n"
    "s_xor_b32 s36, s39, 196608\n"
    "v_min_u32 v208, -2, v208\n"
    "v_min_u32 v209, -2, v209\n"
    "v_mfma_f32_16x16x32_bf16 v[152:155], a[44:47], a[192:195], v[152:155]\n"
    "s_waitcnt lgkmcnt(2)\n"
    "v_min_u32 v210, -2, v210\n"
    "v_min_u32 v211, -2, v211\n"
    "v_mfma_f32_16x16x32_bf16 v[156:159], a[44:47], a[176:179], v[156:159]\n"
    "buffer_store_dwordx4 v[208:211], v22, s[16:19], s36 offen sc1\n"
    "v_mfma_f32_16x16x32_bf16 v[144:147], a[48:51], a[184:187], v[144:147]\n"
    "ds_read_b128 a[184:187], v12 offset:62848\n"
    "v_add_f32 v120, v120, v88\n"
    "v_mfma_f32_16x16x32_bf16 v[148:151], a[48:51], a[192:195], v[148:151]\n"
    "v_add_f32 v121, v121, v89\n"
    "v_add_f32 v122, v122, v90\n"
    "v_mfma_f32_16x16x32_bf16 v[152:155], a[48:51], a[176:179], v[152:155]\n"
    "s_waitcnt lgkmcnt(2)\n"
    "v_add_f32 v123, v123, v91\n"
    "v_add_f32 v124, v124, v92\n"
    "v_mfma_f32_16x16x32_bf16 v[156:159], a[48:51], a[180:183], v[156:159]\n"
    "v_add_f32 v125, v125, v93\n"
    "v_add_f32 v126, v126, v94\n"
    "v_mfma_f32_16x16x32_bf16 v[144:147], a[52:55], a[192:195], v[144:147]\n"
    "ds_read_b128 a[192:195], v12 offset:62976\n"
    "v_add_f32 v127, v127, v95\n"
    "v_mfma_f32_16x16x32_bf16 v[148:151], a[52:55], a[176:179], v[148:151]\n"
    "ds_read_b128 a[196:199], v13 offset:62720\n"
    "v_cvt_pk_bf16_f32 v212, v120, v121\n"
    "v_mfma_f32_16x16x32_bf16 v[152:155], a[52:55], a[180:183], v[152:155]\n"
    "s_waitcnt lgkmcnt(3)\n"
    "ds_read_b128 a[200:203], v13 offset:62848\n"
    "v_cvt_pk_bf16_f32 v213, v122, v123\n"
    "v_mfma_f32_16x16x32_bf16 v[156:159], a[52:55], a[188:191], v[156:159]\n"
    "ds_read_b128 a[204:207], v13 offset:62976\n"
    "v_cvt_pk_bf16_f32 v214, v124, v125\n"
    "v_mfma_f32_16x16x32_bf16 v[144:147], a[56:59], a[176:179], v[144:147]\n"
    "ds_read_b128 a[176:179], v13 offset:63104\n"
    "v_cvt_pk_bf16_f32 v215, v126, v127\n"
    "v_mfma_f32_16x16x32_bf16 v[148:151], a[56:59], a[180:183], v[148:151]\n"
    "s_xor_b32 s36, s39, 229376\n"
    "v_min_u32 v212, -2, v212\n"
    "v_min_u32 v213, -2, v213\n"
    "v_mfma_f32_16x16x32_bf16 v[152:155], a[56:59], a[188:191], v[152:155]\n"
    "s_waitcnt lgkmcnt(5)\n"
    "v_min_u32 v214, -2, v214\n"
    "v_min_u32 v215, -2, v215\n"
    "v_mfma_f32_16x16x32_bf16 v[156:159], a[56:59], a[184:187], v[156:159]\n"
    "buffer_store_dwordx4 v[212:215], v22, s[16:19], s36 offen sc1\n"
    "v_mfma_f32_16x16x32_bf16 v[144:147], a[60:63], a[180:183], v[144:147]\n"
    "ds_read_b128 a[180:183], v14 offset:62208\n"
    "v_mfma_f32_16x16x32_bf16 v[148:151], a[60:63], a[188:191], v[148:151]\n"
    "v_mfma_f32_16x16x32_bf16 v[152:155], a[60:63], a[184:187], v[152:155]\n"
    "s_waitcnt lgkmcnt(5)\n"
    "v_mfma_f32_16x16x32_bf16 v[156:159], a[60:63], a[192:195], v[156:159]\n"
    "s_waitcnt lgkmcnt(4)\n"
    "v_mfma_f32_16x16x32_bf16 v[144:147], a[64:67], a[196:199], v[144:147]\n"
    "s_waitcnt lgkmcnt(3)\n"
    "ds_read_b128 a[184:187], v14 offset:62336\n"
    "v_mfma_f32_16x16x32_bf16 v[148:151], a[64:67], a[200:203], v[148:151]\n"
    "s_waitcnt lgkmcnt(3)\n"
    "v_mfma_f32_16x16x32_bf16 v[152:155], a[64:67], a[204:207], v[152:155]\n"
    "s_waitcnt lgkmcnt(2)\n"
    "v_mfma_f32_16x16x32_bf16 v[156:159], a[64:67], a[176:179], v[156:159]\n"
    "v_mfma_f32_16x16x32_bf16 v[144:147], a[68:71], a[200:203], v[144:147]\n"
    "ds_read_b128 a[188:191], v14 offset:62464\n"
    "v_mfma_f32_16x16x32_bf16 v[148:151], a[68:71], a[204:207], v[148:151]\n"
    "v_mfma_f32_16x16x32_bf16 v[152:155], a[68:71], a[176:179], v[152:155]\n"
    "s_waitcnt lgkmcnt(2)\n"
    "v_mfma_f32_16x16x32_bf16 v[156:159], a[68:71], a[180:183], v[156:159]\n"
    "v_mfma_f32_16x16x32_bf16 v[144:147], a[72:75], a[204:207], v[144:147]\n"
    "ds_read_b128 a[192:195], v14 offset:62592\n"
    "v_mfma_f32_16x16x32_bf16 v[148:151], a[72:75], a[176:179], v[148:151]\n"
    "v_mfma_f32_16x16x32_bf16 v[152:155], a[72:75], a[180:183], v[152:155]\n"
    "s_waitcnt lgkmcnt(2)\n"
    "v_mfma_f32_16x16x32_bf16 v[156:159], a[72:75], a[184:187], v[156:159]\n"
    "v_mfma_f32_16x16x32_bf16 v[144:147], a[76:79], a[176:179], v[144:147]\n"
    "ds_read_b128 a[176:179], v14 offset:62720\n"
    "v_mfma_f32_16x16x32_bf16 v[148:151], a[76:79], a[180:183], v[148:151]\n"
    "v_mfma_f32_16x16x32_bf16 v[152:155], a[76:79], a[184:187], v[152:155]\n"
    "s_waitcnt lgkmcnt(2)\n"
    "v_mfma_f32_16x16x32_bf16 v[156:159], a[76:79], a[188:191], v[156:159]\n"
    "v_mfma_f32_16x16x32_bf16 v[144:147], a[80:83], a[180:183], v[144:147]\n"
    "ds_read_b128 a[180:183], v14 offset:62848\n"
    "v_mfma_f32_16x16x32_bf16 v[148:151], a[80:83], a[184:187], v[148:151]\n"
    "v_mfma_f32_16x16x32_bf16 v[152:155], a[80:83], a[188:191], v[152:155]\n"
    "s_waitcnt lgkmcnt(2)\n"
    "v_mfma_f32_16x16x32_bf16 v[156:159], a[80:83], a[192:195], v[156:159]\n"
    "v_mfma_f32_16x16x32_bf16 v[144:147], a[84:87], a[184:187], v[144:147]\n"
    "ds_read_b128 a[184:187], v14 offset:62976\n"
    "v_mfma_f32_16x16x32_bf16 v[148:151], a[84:87], a[188:191], v[148:151]\n"
    "v_mfma_f32_16x16x32_bf16 v[152:155], a[84:87], a[192:195], v[152:155]\n"
    "s_waitcnt lgkmcnt(2)\n"
    "v_mfma_f32_16x16x32_bf16 v[156:159], a[84:87], a[176:179], v[156:159]\n"
    "v_mfma_f32_16x16x32_bf16 v[144:147], a[88:91], a[188:191], v[144:147]\n"
    "ds_read_b128 a[188:191], v14 offset:63104\n"
    "v_mfma_f32_16x16x32_bf16 v[148:151], a[88:91], a[192:195], v[148:151]\n"
    "v_mfma_f32_16x16x32_bf16 v[152:155], a[88:91], a[176:179], v[152:155]\n"
    "s_waitcnt lgkmcnt(2)\n"
    "v_mfma_f32_16x16x32_bf16 v[156:159], a[88:91], a[180:183], v[156:159]\n"
    "v_mfma_f32_16x16x32_bf16 v[144:147], a[92:95], a[192:195], v[144:147]\n"
    "ds_read_b128 a[192:195], v15 offset:62208\n"
    "v_mfma_f32_16x16x32_bf16 v[148:151], a[92:95], a[176:179], v[148:151]\n"
    "v_mfma_f32_16x16x32_bf16 v[152:155], a[92:95], a[180:183], v[152:155]\n"
    "s_waitcnt lgkmcnt(2)\n"
    "v_mfma_f32_16x16x32_bf16 v[156:159], a[92:95], a[184:187], v[156:159]\n"
    "v_mfma_f32_16x16x32_bf16 v[144:147], a[96:99], a[176:179], v[144:147]\n"
    "ds_read_b128 a[176:179], v15 offset:62336\n"
    "v_mfma_f32_16x16x32_bf16 v[148:151], a[96:99], a[180:183], v[148:151]\n"
    "v_mfma_f32_16x16x32_bf16 v[152:155], a[96:99], a[184:187], v[152:155]\n"
    "s_waitcnt lgkmcnt(2)\n"
    "v_mfma_f32_16x16x32_bf16 v[156:159], a[96:99], a[188:191], v[156:159]\n"
    "v_mfma_f32_16x16x32_bf16 v[144:147], a[100:103], a[180:183], v[144:147]\n"
    "ds_read_b128 a[180:183], v15 offset:62464\n"
    "v_mfma_f32_16x16x32_bf16 v[148:151], a[100:103], a[184:187], v[148:151]\n"
    "v_mfma_f32_16x16x32_bf16 v[152:155], a[100:103], a[188:191], v[152:155]\n"
    "s_waitcnt lgkmcnt(2)\n"
    "v_mfma_f32_16x16x32_bf16 v[156:159], a[100:103], a[192:195], v[156:159]\n"
    "v_mfma_f32_16x16x32_bf16 v[144:147], a[104:107], a[184:187], v[144:147]\n"
    "ds_read_b128 a[184:187], v15 offset:62592\n"
    "v_mfma_f32_16x16x32_bf16 v[148:151], a[104:107], a[188:191], v[148:151]\n"
    "v_mfma_f32_16x16x32_bf16 v[152:155], a[104:107], a[192:195], v[152:155]\n"
    "s_waitcnt lgkmcnt(2)\n"
    "v_mfma_f32_16x16x32_bf16 v[156:159], a[104:107], a[176:179], v[156:159]\n"
    "v_mfma_f32_16x16x32_bf16 v[144:147], a[108:111], a[188:191], v[144:147]\n"
    "ds_read_b128 a[188:191], v15 offset:62720\n"
    "v_mfma_f32_16x16x32_bf16 v[148:151], a[108:111], a[192:195], v[148:151]\n"
    "v_mfma_f32_16x16x32_bf16 v[152:155], a[108:111], a[176:179], v[152:155]\n"
    "s_waitcnt lgkmcnt(2)\n"
    "v_mfma_f32_16x16x32_bf16 v[156:159], a[108:111], a[180:183], v[156:159]\n"
    "v_mfma_f32_16x16x32_bf16 v[144:147], a[112:115], a[192:195], v[144:147]\n"
    "ds_read_b128 a[192:195], v15 offset:62848\n"
    "v_mfma_f32_16x16x32_bf16 v[148:151], a[112:115], a[176:179], v[148:151]\n"
    "v_mfma_f32_16x16x32_bf16 v[152:155], a[112:115], a[180:183], v[152:155]\n"
    "s_waitcnt lgkmcnt(2)\n"
    "buffer_load_dwordx4 v[160:163], v22, s[16:19], s28 offen sc1\n"
    "v_mfma_f32_16x16x32_bf16 v[156:159], a[112:115], a[184:187], v[156:159]\n"
    "v_mfma_f32_16x16x32_bf16 v[144:147], a[116:119], a[176:179], v[144:147]\n"
    "ds_read_b128 a[176:179], v15 offset:62976\n"
    "v_mfma_f32_16x16x32_bf16 v[148:151], a[116:119], a[180:183], v[148:151]\n"
    "ds_read_b128 a[196:199], v10 offset:62464\n"
    "v_mfma_f32_16x16x32_bf16 v[152:155], a[116:119], a[184:187], v[152:155]\n"
    "s_waitcnt lgkmcnt(3)\n"
    "ds_read_b128 a[200:203], v10 offset:62592\n"
    "v_mfma_f32_16x16x32_bf16 v[156:159], a[116:119], a[188:191], v[156:159]\n"
    "buffer_load_dwordx4 v[164:167], v22, s[16:19], s30 offen sc1\n"
    "v_mfma_f32_16x16x32_bf16 v[144:147], a[120:123], a[180:183], v[144:147]\n"
    "ds_read_b128 a[180:183], v10 offset:62720\n"
    "v_mfma_f32_16x16x32_bf16 v[148:151], a[120:123], a[184:187], v[148:151]\n"
    "v_mfma_f32_16x16x32_bf16 v[152:155], a[120:123], a[188:191], v[152:155]\n"
    "s_waitcnt lgkmcnt(4)\n"
    "ds_read_b128 a[204:207], v10 offset:62848\n"
    "v_mfma_f32_16x16x32_bf16 v[156:159], a[120:123], a[192:195], v[156:159]\n"
    "buffer_load_dwordx4 v[168:171], v22, s[16:19], s29 offen sc1\n"
    "v_mfma_f32_16x16x32_bf16 v[144:147], a[124:127], a[184:187], v[144:147]\n"
    "ds_read_b128 a[184:187], v10 offset:62976\n"
    "v_mfma_f32_16x16x32_bf16 v[148:151], a[124:127], a[188:191], v[148:151]\n"
    "v_mfma_f32_16x16x32_bf16 v[152:155], a[124:127], a[192:195], v[152:155]\n"
    "s_waitcnt lgkmcnt(5)\n"
    "ds_read_b128 a[188:191], v10 offset:63104\n"
    "v_mfma_f32_16x16x32_bf16 v[156:159], a[124:127], a[176:179], v[156:159]\n"
    "s_waitcnt lgkmcnt(5)\n"
    "buffer_load_dwordx4 v[172:175], v22, s[16:19], s31 offen sc1\n"
    "v_mfma_f32_16x16x32_bf16 v[136:139], a[0:3], a[196:199], v[72:75]\n"
    "s_waitcnt lgkmcnt(4)\n"
    "ds_read_b128 a[176:179], v11 offset:62208\n"
    "v_mfma_f32_16x16x32_bf16 v[140:143], a[0:3], a[200:203], v[76:79]\n"
    "v_mfma_f32_16x16x32_bf16 v[136:139], a[4:7], a[200:203], v[136:139]\n"
    "s_waitcnt lgkmcnt(4)\n"
    "ds_read_b128 a[192:195], v11 offset:62336\n"
    "v_mfma_f32_16x16x32_bf16 v[140:143], a[4:7], a[180:183], v[140:143]\n"
    "v_cvt_pk_bf16_f32 v216, v144, v145\n"
    "v_cvt_pk_bf16_f32 v217, v146, v147\n"
    "v_mfma_f32_16x16x32_bf16 v[136:139], a[8:11], a[180:183], v[136:139]\n"
    "s_waitcnt lgkmcnt(4)\n"
    "ds_read_b128 a[180:183], v11 offset:62464\n"
    "v_cvt_pk_bf16_f32 v218, v148, v149\n"
    "v_mfma_f32_16x16x32_bf16 v[140:143], a[8:11], a[204:207], v[140:143]\n"
    "v_cvt_pk_bf16_f32 v219, v150, v151\n"
    "v_mfma_f32_16x16x32_bf16 v[136:139], a[12:15], a[204:207], v[136:139]\n"
    "s_waitcnt lgkmcnt(4)\n"
    "s_xor_b32 s24, s39, 65536\n"
    "ds_read_b128 a[196:199], v11 offset:62592\n"
    "v_mfma_f32_16x16x32_bf16 v[140:143], a[12:15], a[184:187], v[140:143]\n"
    "v_min_u32 v216, -2, v216\n"
    "v_min_u32 v217, -2, v217\n"
    "v_mfma_f32_16x16x32_bf16 v[136:139], a[16:19], a[184:187], v[136:139]\n"
    "s_waitcnt lgkmcnt(4)\n"
    "ds_read_b128 a[184:187], v11 offset:62720\n"
    "v_min_u32 v218, -2, v218\n"
    "v_mfma_f32_16x16x32_bf16 v[140:143], a[16:19], a[188:191], v[140:143]\n"
    "v_min_u32 v219, -2, v219\n"
    "v_mfma_f32_16x16x32_bf16 v[136:139], a[20:23], a[188:191], v[136:139]\n"
    "s_waitcnt lgkmcnt(4)\n"
    "ds_read_b128 a[188:191], v11 offset:62848\n"
    "v_mfma_f32_16x16x32_bf16 v[140:143], a[20:23], a[176:179], v[140:143]\n"
    "buffer_store_dwordx4 v[216:219], v22, s[16:19], s24 offen sc1\n"
    "v_mfma_f32_16x16x32_bf16 v[136:139], a[24:27], a[176:179], v[136:139]\n"
    "s_waitcnt lgkmcnt(4)\n"
    "ds_read_b128 a[176:179], v11 offset:62976\n"
    "v_cvt_pk_bf16_f32 v220, v152, v153\n"
    "v_mfma_f32_16x16x32_bf16 v[140:143], a[24:27], a[192:195], v[140:143]\n"
    "v_cvt_pk_bf16_f32 v221, v154, v155\n"
    "v_cvt_pk_bf16_f32 v222, v156, v157\n"
    "v_mfma_f32_16x16x32_bf16 v[136:139], a[28:31], a[192:195], v[136:139]\n"
    "s_waitcnt lgkmcnt(4)\n"
    "ds_read_b128 a[192:195], v11 offset:63104\n"
    "v_cvt_pk_bf16_f32 v223, v158, v159\n"
    "v_mfma_f32_16x16x32_bf16 v[140:143], a[28:31], a[180:183], v[140:143]\n"
    "s_xor_b32 s24, s39, 98304\n"
    "v_min_u32 v220, -2, v220\n"
    "v_min_u32 v221, -2, v221\n"
    "v_mfma_f32_16x16x32_bf16 v[136:139], a[32:35], a[180:183], v[136:139]\n"
    "s_waitcnt lgkmcnt(4)\n"
    "ds_read_b128 a[180:183], v12 offset:62208\n"
    "v_min_u32 v222, -2, v222\n"
    "v_mfma_f32_16x16x32_bf16 v[140:143], a[32:35], a[196:199], v[140:143]\n"
    "v_min_u32 v223, -2, v223\n"
    "v_mfma_f32_16x16x32_bf16 v[136:139], a[36:39], a[196:199], v[136:139]\n"
    "s_waitcnt lgkmcnt(4)\n"
    "ds_read_b128 a[196:199], v12 offset:62336\n"
    "v_mfma_f32_16x16x32_bf16 v[140:143], a[36:39], a[184:187], v[140:143]\n"
    "buffer_store_dwordx4 v[220:223], v22, s[16:19], s24 offen sc1\n"
    "v_mfma_f32_16x16x32_bf16 v[136:139], a[40:43], a[184:187], v[136:139]\n"
    "s_waitcnt lgkmcnt(4)\n"
    "ds_read_b128 a[184:187], v12 offset:62464\n"
    "v_mfma_f32_16x16x32_bf16 v[140:143], a[40:43], a[188:191], v[140:143]\n"
    "ds_read_b128 a[200:203], v13 offset:62464\n"
    "v_mfma_f32_16x16x32_bf16 v[136:139], a[44:47], a[188:191], v[136:139]\n"
    "s_waitcnt lgkmcnt(5)\n"
    "ds_read_b128 a[188:191], v13 offset:62592\n"
    "v_mfma_f32_16x16x32_bf16 v[140:143], a[44:47], a[176:179], v[140:143]\n"
    "v_mfma_f32_16x16x32_bf16 v[136:139], a[48:51], a[176:179], v[136:139]\n"
    "s_waitcnt lgkmcnt(5)\n"
    "ds_read_b128 a[176:179], v13 offset:62720\n"
    "v_mfma_f32_16x16x32_bf16 v[140:143], a[48:51], a[192:195], v[140:143]\n"
    "v_mfma_f32_16x16x32_bf16 v[136:139], a[52:55], a[192:195], v[136:139]\n"
    "s_waitcnt lgkmcnt(5)\n"
    "ds_read_b128 a[192:195], v13 offset:62848\n"
    "v_mfma_f32_16x16x32_bf16 v[140:143], a[52:55], a[180:183], v[140:143]\n"
    "v_mfma_f32_16x16x32_bf16 v[136:139], a[56:59], a[180:183], v[136:139]\n"
    "s_waitcnt lgkmcnt(5)\n"
    "ds_read_b128 a[180:183], v13 offset:62976\n"
    "v_mfma_f32_16x16x32_bf16 v[140:143], a[56:59], a[196:199], v[140:143]\n"
    "v_mfma_f32_16x16x32_bf16 v[136:139], a[60:63], a[196:199], v[136:139]\n"
    "s_waitcnt lgkmcnt(5)\n"
    "ds_read_b128 a[196:199], v13 offset:63104\n"
    "v_mfma_f32_16x16x32_bf16 v[140:143], a[60:63], a[184:187], v[140:143]\n"
    "s_waitcnt lgkmcnt(5)\n"
    "v_mfma_f32_16x16x32_bf16 v[136:139], a[64:67], a[200:203], v[136:139]\n"
    "s_waitcnt lgkmcnt(4)\n"
    "ds_read_b128 a[184:187], v14 offset:62208\n"
    "v_mfma_f32_16x16x32_bf16 v[140:143], a[64:67], a[188:191], v[140:143]\n"
    "v_mfma_f32_16x16x32_bf16 v[136:139], a[68:71], a[188:191], v[136:139]\n"
    "s_waitcnt lgkmcnt(4)\n"
    "ds_read_b128 a[188:191], v14 offset:62336\n"
    "v_mfma_f32_16x16x32_bf16 v[140:143], a[68:71], a[176:179], v[140:143]\n"
    "v_mfma_f32_16x16x32_bf16 v[136:139], a[72:75], a[176:179], v[136:139]\n"
    "s_waitcnt lgkmcnt(4)\n"
    "ds_read_b128 a[176:179], v14 offset:62464\n"
    "v_mfma_f32_16x16x32_bf16 v[140:143], a[72:75], a[192:195], v[140:143]\n"
    "v_mfma_f32_16x16x32_bf16 v[136:139], a[76:79], a[192:195], v[136:139]\n"
    "s_waitcnt lgkmcnt(4)\n"
    "ds_read_b128 a[192:195], v14 offset:62592\n"
    "v_mfma_f32_16x16x32_bf16 v[140:143], a[76:79], a[180:183], v[140:143]\n"
    "v_mfma_f32_16x16x32_bf16 v[136:139], a[80:83], a[180:183], v[136:139]\n"
    "s_waitcnt lgkmcnt(4)\n"
    "ds_read_b128 a[180:183], v14 offset:62720\n"
    "v_mfma_f32_16x16x32_bf16 v[140:143], a[80:83], a[196:199], v[140:143]\n"
    "v_mfma_f32_16x16x32_bf16 v[136:139], a[84:87], a[196:199], v[136:139]\n"
    "s_waitcnt lgkmcnt(4)\n"
    "ds_read_b128 a[196:199], v14 offset:62848\n"
    "v_mfma_f32_16x16x32_bf16 v[140:143], a[84:87], a[184:187], v[140:143]\n"
    "v_mfma_f32_16x16x32_bf16 v[136:139], a[88:91], a[184:187], v[136:139]\n"
    "s_waitcnt lgkmcnt(4)\n"
    "ds_read_b128 a[184:187], v14 offset:62976\n"
    "v_mfma_f32_16x16x32_bf16 v[140:143], a[88:91], a[188:191], v[140:143]\n"
    "v_mfma_f32_16x16x32_bf16 v[136:139], a[92:95], a[188:191], v[136:139]\n"
    "s_waitcnt lgkmcnt(4)\n"
    "ds_read_b128 a[188:191], v14 offset:63104\n"
    "v_mfma_f32_16x16x32_bf16 v[140:143], a[92:95], a[176:179], v[140:143]\n"
    "v_mfma_f32_16x16x32_bf16 v[136:139], a[96:99], a[176:179], v[136:139]\n"
    "s_waitcnt lgkmcnt(4)\n"
    "ds_read_b128 a[176:179], v15 offset:62208\n"
    "v_mfma_f32_16x16x32_bf16 v[140:143], a[96:99], a[192:195], v[140:143]\n"
    "v_mfma_f32_16x16x32_bf16 v[136:139], a[100:103], a[192:195], v[136:139]\n"
    "s_waitcnt lgkmcnt(4)\n"
    "ds_read_b128 a[192:195], v15 offset:62336\n"
    "v_mfma_f32_16x16x32_bf16 v[140:143], a[100:103], a[180:183], v[140:143]\n"
    "v_mfma_f32_16x16x32_bf16 v[136:139], a[104:107], a[180:183], v[136:139]\n"
    "s_waitcnt lgkmcnt(4)\n"
    "ds_read_b128 a[180:183], v15 offset:62464\n"
    "v_mfma_f32_16x16x32_bf16 v[140:143], a[104:107], a[196:199], v[140:143]\n"
    "ds_read_b128 a[200:203], v10 offset:62208\n"
    "v_mfma_f32_16x16x32_bf16 v[136:139], a[108:111], a[196:199], v[136:139]\n"
    "s_waitcnt lgkmcnt(5)\n"
    "ds_read_b128 a[196:199], v10 offset:62336\n"
    "v_mfma_f32_16x16x32_bf16 v[140:143], a[108:111], a[184:187], v[140:143]\n"
    "v_mfma_f32_16x16x32_bf16 v[136:139], a[112:115], a[184:187], v[136:139]\n"
    "s_waitcnt lgkmcnt(5)\n"
    "ds_read_b128 a[184:187], v10 offset:62464\n"
    "v_mfma_f32_16x16x32_bf16 v[140:143], a[112:115], a[188:191], v[140:143]\n"
    "v_mfma_f32_16x16x32_bf16 v[136:139], a[116:119], a[188:191], v[136:139]\n"
    "s_waitcnt lgkmcnt(5)\n"
    "ds_read_b128 a[188:191], v10 offset:62592\n"
    "v_mfma_f32_16x16x32_bf16 v[140:143], a[116:119], a[176:179], v[140:143]\n"
    "v_mfma_f32_16x16x32_bf16 v[136:139], a[120:123], a[176:179], v[136:139]\n"
    "s_waitcnt lgkmcnt(5)\n"
    "ds_read_b128 a[176:179], v10 offset:62720\n"
    "v_mfma_f32_16x16x32_bf16 v[140:143], a[120:123], a[192:195], v[140:143]\n"
    "v_mfma_f32_16x16x32_bf16 v[136:139], a[124:127], a[192:195], v[136:139]\n"
    "s_waitcnt lgkmcnt(5)\n"
    "ds_read_b128 a[192:195], v10 offset:62848\n"
    "v_mfma_f32_16x16x32_bf16 v[140:143], a[124:127], a[180:183], v[140:143]\n"
    "s_waitcnt lgkmcnt(5)\n"
    "v_mfma_f32_16x16x32_bf16 v[128:131], a[0:3], a[200:203], v[64:67]\n"
    "s_waitcnt lgkmcnt(4)\n"
    "ds_read_b128 a[180:183], v10 offset:62976\n"
    "v_mfma_f32_16x16x32_bf16 v[132:135], a[0:3], a[196:199], v[68:71]\n"
    "v_mfma_f32_16x16x32_bf16 v[128:131], a[4:7], a[196:199], v[128:131]\n"
    "s_waitcnt lgkmcnt(4)\n"
    "ds_read_b128 a[196:199], v10 offset:63104\n"
    "v_mfma_f32_16x16x32_bf16 v[132:135], a[4:7], a[184:187], v[132:135]\n"
    "v_cvt_pk_bf16_f32 v224, v136, v137\n"
    "v_cvt_pk_bf16_f32 v225, v138, v139\n"
    "v_mfma_f32_16x16x32_bf16 v[128:131], a[8:11], a[184:187], v[128:131]\n"
    "s_waitcnt lgkmcnt(4)\n"
    "ds_read_b128 a[184:187], v11 offset:62208\n"
    "v_cvt_pk_bf16_f32 v226, v140, v141\n"
    "v_mfma_f32_16x16x32_bf16 v[132:135], a[8:11], a[188:191], v[132:135]\n"
    "v_cvt_pk_bf16_f32 v227, v142, v143\n"
    "v_mfma_f32_16x16x32_bf16 v[128:131], a[12:15], a[188:191], v[128:131]\n"
    "s_waitcnt lgkmcnt(4)\n"
    "s_xor_b32 s24, s39, 32768\n"
    "ds_read_b128 a[188:191], v11 offset:62336\n"
    "v_mfma_f32_16x16x32_bf16 v[132:135], a[12:15], a[176:179], v[132:135]\n"
    "v_min_u32 v224, -2, v224\n"
    "v_min_u32 v225, -2, v225\n"
    "v_mfma_f32_16x16x32_bf16 v[128:131], a[16:19], a[176:179], v[128:131]\n"
    "s_waitcnt lgkmcnt(4)\n"
    "ds_read_b128 a[176:179], v11 offset:62464\n"
    "v_min_u32 v226, -2, v226\n"
    "v_mfma_f32_16x16x32_bf16 v[132:135], a[16:19], a[192:195], v[132:135]\n"
    "v_min_u32 v227, -2, v227\n"
    "v_mfma_f32_16x16x32_bf16 v[128:131], a[20:23], a[192:195], v[128:131]\n"
    "s_waitcnt lgkmcnt(4)\n"
    "ds_read_b128 a[192:195], v11 offset:62592\n"
    "v_mfma_f32_16x16x32_bf16 v[132:135], a[20:23], a[180:183], v[132:135]\n"
    "buffer_store_dwordx4 v[224:227], v22, s[16:19], s24 offen sc1\n"
    "v_mfma_f32_16x16x32_bf16 v[128:131], a[24:27], a[180:183], v[128:131]\n"
    "s_waitcnt lgkmcnt(4)\n"
    "ds_read_b128 a[180:183], v11 offset:62720\n"
    "v_mfma_f32_16x16x32_bf16 v[132:135], a[24:27], a[196:199], v[132:135]\n"
    "v_mfma_f32_16x16x32_bf16 v[128:131], a[28:31], a[196:199], v[128:131]\n"
    "s_waitcnt lgkmcnt(4)\n"
    "ds_read_b128 a[196:199], v11 offset:62848\n"
    "v_mfma_f32_16x16x32_bf16 v[132:135], a[28:31], a[184:187], v[132:135]\n"
    "v_mfma_f32_16x16x32_bf16 v[128:131], a[32:35], a[184:187], v[128:131]\n"
    "s_waitcnt lgkmcnt(4)\n"
    "ds_read_b128 a[184:187], v11 offset:62976\n"
    "v_mfma_f32_16x16x32_bf16 v[132:135], a[32:35], a[188:191], v[132:135]\n"
    "v_mfma_f32_16x16x32_bf16 v[128:131], a[36:39], a[188:191], v[128:131]\n"
    "s_waitcnt lgkmcnt(4)\n"
    "ds_read_b128 a[188:191], v11 offset:63104\n"
    "v_mfma_f32_16x16x32_bf16 v[132:135], a[36:39], a[176:179], v[132:135]\n"
    "v_mfma_f32_16x16x32_bf16 v[128:131], a[40:43], a[176:179], v[128:131]\n"
    "s_waitcnt lgkmcnt(4)\n"
    "ds_read_b128 a[176:179], v12 offset:62208\n"
    "v_mfma_f32_16x16x32_bf16 v[132:135], a[40:43], a[192:195], v[132:135]\n"
    "ds_read_b128 a[200:203], v13 offset:62208\n"
    "v_mfma_f32_16x16x32_bf16 v[128:131], a[44:47], a[192:195], v[128:131]\n"
    "s_waitcnt lgkmcnt(5)\n"
    "ds_read_b128 a[192:195], v13 offset:62336\n"
    "v_mfma_f32_16x16x32_bf16 v[132:135], a[44:47], a[180:183], v[132:135]\n"
    "v_mfma_f32_16x16x32_bf16 v[128:131], a[48:51], a[180:183], v[128:131]\n"
    "s_waitcnt lgkmcnt(5)\n"
    "s_waitcnt vmcnt(3)\n"
    "ds_read_b128 a[180:183], v13 offset:62464\n"
    "v_mfma_f32_16x16x32_bf16 v[132:135], a[48:51], a[196:199], v[132:135]\n"
    "v_max3_u32 v240, v160, v161, v162\n"
    "v_max3_u32 v240, v240, v163, v164\n"
    "v_mfma_f32_16x16x32_bf16 v[128:131], a[52:55], a[196:199], v[128:131]\n"
    "s_waitcnt lgkmcnt(5)\n"
    "ds_read_b128 a[196:199], v13 offset:62592\n"
    "v_max3_u32 v240, v240, v165, v166\n"
    "v_mfma_f32_16x16x32_bf16 v[132:135], a[52:55], a[184:187], v[132:135]\n"
    "buffer_load_dwordx4 v[176:179], v22, s[16:19], s26 offen sc1\n"
    "v_mfma_f32_16x16x32_bf16 v[128:131], a[56:59], a[184:187], v[128:131]\n"
    "s_waitcnt lgkmcnt(5)\n"
    "ds_read_b128 a[184:187], v13 offset:62720\n"
    "v_max3_u32 v240, v240, v167, v168\n"
    "v_mfma_f32_16x16x32_bf16 v[132:135], a[56:59], a[188:191], v[132:135]\n"
    "v_max3_u32 v240, v240, v169, v170\n"
    "v_max3_u32 v240, v240, v171, v172\n"
    "v_mfma_f32_16x16x32_bf16 v[128:131], a[60:63], a[188:191], v[128:131]\n"
    "s_waitcnt lgkmcnt(5)\n"
    "ds_read_b128 a[188:191], v13 offset:62848\n"
    "v_max3_u32 v240, v240, v173, v174\n"
    "v_mfma_f32_16x16x32_bf16 v[132:135], a[60:63], a[176:179], v[132:135]\n"
    "s_waitcnt lgkmcnt(5)\n"
    "buffer_load_dwordx4 v[180:183], v22, s[16:19], s27 offen sc1\n"
    "v_mfma_f32_16x16x32_bf16 v[128:131], a[64:67], a[200:203], v[128:131]\n"
    "s_waitcnt lgkmcnt(4)\n"
    "ds_read_b128 a[176:179], v13 offset:62976\n"
    "v_max_u32 v240, v240, v175\n"
    "v_mfma_f32_16x16x32_bf16 v[132:135], a[64:67], a[192:195], v[132:135]\n"
    "v_cmp_eq_u32 vcc, -1, v240\n"
    "s_cbranch_vccz LE4ok_%=\n"
    "s_mov_b32 s43, 0x100000\n"
    "LE4lp_%=:\n"
    "buffer_load_dwordx4 v[160:163], v22, s[16:19], s28 offen sc1\n"
    "buffer_load_dwordx4 v[164:167], v22, s[16:19], s30 offen sc1\n"
    "buffer_load_dwordx4 v[168:171], v22, s[16:19], s29 offen sc1\n"
    "buffer_load_dwordx4 v[172:175], v22, s[16:19], s31 offen sc1\n"
    "s_waitcnt vmcnt(0)\n"
    "s_sub_u32 s43, s43, 1\n"
    "s_cbranch_scc1 LE4ok_%=\n"
    "v_max3_u32 v240, v160, v161, v162\n"
    "v_max3_u32 v240, v240, v163, v164\n"
    "v_max3_u32 v240, v240, v165, v166\n"
    "v_max3_u32 v240, v240, v167, v168\n"
    "v_max3_u32 v240, v240, v169, v170\n"
    "v_max3_u32 v240, v240, v171, v172\n"
    "v_max3_u32 v240, v240, v173, v174\n"
    "v_max_u32 v240, v240, v175\n"
    "v_cmp_eq_u32 vcc, -1, v240\n"
    "s_cbranch_vccnz LE4lp_%=\n"
    "LE4ok_%=:\n"
    "v_mfma_f32_16x16x32_bf16 v[128:131], a[68:71], a[192:195], v[128:131]\n"
    "s_waitcnt lgkmcnt(4)\n"
    "ds_read_b128 a[192:195], v13 offset:63104\n"
    "v_lshlrev_b32 v188, 16, v160\n"
    "v_mfma_f32_16x16x32_bf16 v[132:135], a[68:71], a[180:183], v[132:135]\n"
    "v_and_b32 v189, s48, v160\n"
    "v_lshlrev_b32 v190, 16, v161\n"
    "v_mfma_f32_16x16x32_bf16 v[128:131], a[72:75], a[180:183], v[128:131]\n"
    "s_waitcnt lgkmcnt(4)\n"
    "ds_read_b128 a[180:183], v14 offset:62208\n"
    "v_and_b32 v191, s48, v161\n"
    "v_mfma_f32_16x16x32_bf16 v[132:135], a[72:75], a[196:199], v[132:135]\n"
    "v_lshlrev_b32 v192, 16, v162\n"
    "v_and_b32 v193, s48, v162\n"
    "v_mfma_f32_16x16x32_bf16 v[128:131], a[76:79], a[196:199], v[128:131]\n"
    "s_waitcnt lgkmcnt(4)\n"
    "ds_read_b128 a[196:199], v14 offset:62336\n"
    "v_lshlrev_b32 v194, 16, v163\n"
    "v_mfma_f32_16x16x32_bf16 v[132:135], a[76:79], a[184:187], v[132:135]\n"
    "v_and_b32 v195, s48, v163\n"
    "v_lshlrev_b32 v240, 16, v164\n"
    "v_mfma_f32_16x16x32_bf16 v[128:131], a[80:83], a[184:187], v[128:131]\n"
    "s_waitcnt lgkmcnt(4)\n"
    "ds_read_b128 a[184:187], v14 offset:62464\n"
    "v_and_b32 v241, s48, v164\n"
    "v_mfma_f32_16x16x32_bf16 v[132:135], a[80:83], a[188:191], v[132:135]\n"
    "v_lshlrev_b32 v242, 16, v165\n"
    "v_and_b32 v243, s48, v165\n"
    "v_mfma_f32_16x16x32_bf16 v[128:131], a[84:87], a[188:191], v[128:131]\n"
    "s_waitcnt lgkmcnt(4)\n"
    "ds_read_b128 a[188:191], v14 offset:62592\n"
    "v_lshlrev_b32 v244, 16, v166\n"
    "v_mfma_f32_16x16x32_bf16 v[132:135], a[84:87], a[176:179], v[132:135]\n"
    "v_and_b32 v245, s48, v166\n"
    "v_lshlrev_b32 v246, 16, v167\n"
    "v_mfma_f32_16x16x32_bf16 v[128:131], a[88:91], a[176:179], v[128:131]\n"
    "s_waitcnt lgkmcnt(4)\n"
    "ds_read_b128 a[176:179], v14 offset:62720\n"
    "v_and_b32 v247, s48, v167\n"
    "v_mfma_f32_16x16x32_bf16 v[132:135], a[88:91], a[192:195], v[132:135]\n"
    "v_add_f32 v188, v188, v240\n"
    "v_add_f32 v189, v189, v241\n"
    "v_mfma_f32_16x16x32_bf16 v[128:131], a[92:95], a[192:195], v[128:131]\n"
    "s_waitcnt lgkmcnt(4)\n"
    "ds_read_b128 a[192:195], v14 offset:62848\n"
    "v_add_f32 v190, v190, v242\n"
    "v_mfma_f32_16x16x32_bf16 v[132:135], a[92:95], a[180:183], v[132:135]\n"
    "v_add_f32 v191, v191, v243\n"
    "v_add_f32 v192, v192, v244\n"
    "v_mfma_f32_16x16x32_bf16 v[128:131], a[96:99], a[180:183], v[128:131]\n"
    "s_waitcnt lgkmcnt(4)\n"
    "ds_read_b128 a[180:183], v14 offset:62976\n"
    "v_add_f32 v193, v193, v245\n"
    "v_mfma_f32_16x16x32_bf16 v[132:135], a[96:99], a[196:199], v[132:135]\n"
    "v_add_f32 v194, v194, v246\n"
    "v_add_f32 v195, v195, v247\n"
    "v_mfma_f32_16x16x32_bf16 v[128:131], a[100:103], a[196:199], v[128:131]\n"
    "s_waitcnt lgkmcnt(4)\n"
    "ds_read_b128 a[196:199], v14 offset:63104\n"
    "v_lshlrev_b32 v40, 16, v168\n"
    "v_mfma_f32_16x16x32_bf16 v[132:135], a[100:103], a[184:187], v[132:135]\n"
    "v_and_b32 v41, s48, v168\n"
    "v_lshlrev_b32 v42, 16, v169\n"
    "v_mfma_f32_16x16x32_bf16 v[128:131], a[104:107], a[184:187], v[128:131]\n"
    "s_waitcnt lgkmcnt(4)\n"
    "ds_read_b128 a[184:187], v15 offset:62208\n"
    "v_and_b32 v43, s48, v169\n"
    "v_mfma_f32_16x16x32_bf16 v[132:135], a[104:107], a[188:191], v[132:135]\n"
    "v_lshlrev_b32 v44, 16, v170\n"
    "v_and_b32 v45, s48, v170\n"
    "v_mfma_f32_16x16x32_bf16 v[128:131], a[108:111], a[188:191], v[128:131]\n"
    "s_waitcnt lgkmcnt(4)\n"
    "buffer_load_dwordx4 v[184:187], v22, s[16:19], s25 offen sc1\n"
    "v_mfma_f32_16x16x32_bf16 v[132:135], a[108:111], a[176:179], v[132:135]\n"
    "v_lshlrev_b32 v46, 16, v171\n"
    "v_and_b32 v47, s48, v171\n"
    "v_mfma_f32_16x16x32_bf16 v[128:131], a[112:115], a[176:179], v[128:131]\n"
    "s_waitcnt lgkmcnt(3)\n"
    "v_lshlrev_b32 v240, 16, v172\n"
    "v_and_b32 v241, s48, v172\n"
    "v_mfma_f32_16x16x32_bf16 v[132:135], a[112:115], a[192:195], v[132:135]\n"
    "v_lshlrev_b32 v242, 16, v173\n"
    "v_and_b32 v243, s48, v173\n"
    "v_mfma_f32_16x16x32_bf16 v[128:131], a[116:119], a[192:195], v[128:131]\n"
    "s_waitcnt lgkmcnt(2)\n"
    "v_lshlrev_b32 v244, 16, v174\n"
    "v_and_b32 v245, s48, v174\n"
    "v_mfma_f32_16x16x32_bf16 v[132:135], a[116:119], a[180:183], v[132:135]\n"
    "v_lshlrev_b32 v246, 16, v175\n"
    "v_and_b32 v247, s48, v175\n"
    "v_mfma_f32_16x16x32_bf16 v[128:131], a[120:123], a[180:183], v[128:131]\n"
    "s_waitcnt lgkmcnt(1)\n"
    "v_add_f32 v40, v40, v240\n"
    "v_add_f32 v41, v41, v241\n"
    "v_mfma_f32_16x16x32_bf16 v[132:135], a[120:123], a[196:199], v[132:135]\n"
    "v_add_f32 v42, v42, v242\n"
    "v_add_f32 v43, v43, v243\n"
    "v_mfma_f32_16x16x32_bf16 v[128:131], a[124:127], a[196:199], v[128:131]\n"
    "s_waitcnt lgkmcnt(0)\n"
    "v_add_f32 v44, v44, v244\n"
    "v_add_f32 v45, v45, v245\n"
    "v_mfma_f32_16x16x32_bf16 v[132:135], a[124:127], a[184:187], v[132:135]\n"
    "v_add_f32 v46, v46, v246\n"
    "v_add_f32 v47, v47, v247\n"
    "s_waitcnt vmcnt(1)\n"
    "v_max3_u32 v240, v176, v177, v178\n"
    "v_max3_u32 v240, v240, v179, v180\n"
    "v_max3_u32 v240, v240, v181, v182\n"
    "v_max_u32 v240, v240, v183\n"
    "v_cmp_eq_u32 vcc, -1, v240\n"
    "s_cbranch_vccz LM4ok_%=\n"
    "s_mov_b32 s43, 0x100000\n"
    "LM4lp_%=:\n"
    "buffer_load_dwordx4 v[176:179], v22, s[16:19], s26 offen sc1\n"
    "buffer_load_dwordx4 v[180:183], v22, s[16:19], s27 offen sc1\n"
    "s_waitcnt vmcnt(0)\n"
    "s_sub_u32 s43, s43, 1\n"
    "s_cbranch_scc1 LM4ok_%=\n"
    "v_max3_u32 v240, v176, v177, v178\n"
    "v_max3_u32 v240, v240, v179, v180\n"
    "v_max3_u32 v240, v240, v181, v182\n"
    "v_max_u32 v240, v240, v183\n"
    "v_cmp_eq_u32 vcc, -1, v240\n"
    "s_cbranch_vccnz LM4lp_%=\n"
    "LM4ok_%=:\n"
    "v_mfma_f32_16x16x32_bf16 v[128:131], v[236:239], v[176:179], v[128:131]\n"
    "v_mfma_f32_16x16x32_bf16 v[132:135], v[234:237], v[176:179], v[132:135]\n"
    "v_mfma_f32_16x16x32_bf16 v[40:43], v[236:239], v[180:183], v[40:43]\n"
    "v_mfma_f32_16x16x32_bf16 v[44:47], v[234:237], v[180:183], v[44:47]\n"
    "s_nop 4\n"
    "v_pk_add_f32 v[128:129], v[128:129], v[188:189]\n"
    "v_pk_add_f32 v[130:131], v[130:131], v[190:191]\n"
    "v_pk_add_f32 v[132:133], v[132:133], v[192:193]\n"
    "v_pk_add_f32 v[134:135], v[134:135], v[194:195]\n"
    "v_pk_mul_f32 v[240:241], v[128:129], s[46:47] op_sel_hi:[1,0]\n"
    "v_pk_mul_f32 v[242:243], v[130:131], s[46:47] op_sel_hi:[1,0]\n"
    "v_pk_mul_f32 v[244:245], v[132:133], s[46:47] op_sel_hi:[1,0]\n"
    "v_pk_mul_f32 v[246:247], v[134:135], s[46:47] op_sel_hi:[1,0]\n"
    "v_exp_f32 v240, -|v240|\n"
    "v_fma_f32 v248, -v128, v128, |v128|\n"
    "v_exp_f32 v241, -|v241|\n"
    "v_fma_f32 v249, -v129, v129, |v129|\n"
    "v_exp_f32 v242, -|v242|\n"
    "v_fma_f32 v250, -v130, v130, |v130|\n"
    "v_exp_f32 v243, -|v243|\n"
    "v_fma_f32 v251, -v131, v131, |v131|\n"
    "v_exp_f32 v244, -|v244|\n"
    "v_fma_f32 v252, -v132, v132, |v132|\n"
    "v_exp_f32 v245, -|v245|\n"
    "v_fma_f32 v253, -v133, v133, |v133|\n"
    "v_exp_f32 v246, -|v246|\n"
    "v_fma_f32 v254, -v134, v134, |v134|\n"
    "v_exp_f32 v247, -|v247|\n"
    "v_fma_f32 v255, -v135, v135, |v135|\n"
    "v_pk_fma_f32 v[168:169], v[240:241], 0.5, 0.5 op_sel_hi:[1,0,0] neg_lo:[1,0,0] neg_hi:[1,0,0]\n"
    "v_pk_fma_f32 v[170:171], v[242:243], 0.5, 0.5 op_sel_hi:[1,0,0] neg_lo:[1,0,0] neg_hi:[1,0,0]\n"
    "v_pk_fma_f32 v[172:173], v[244:245], 0.5, 0.5 op_sel_hi:[1,0,0] neg_lo:[1,0,0] neg_hi:[1,0,0]\n"
    "v_pk_fma_f32 v[174:175], v[246:247], 0.5, 0.5 op_sel_hi:[1,0,0] neg_lo:[1,0,0] neg_hi:[1,0,0]\n"
    "v_pk_fma_f32 v[240:241], v[240:241], 0.5, 0.5 op_sel_hi:[1,0,0]\n"
    "v_pk_fma_f32 v[242:243], v[242:243], 0.5, 0.5 op_sel_hi:[1,0,0]\n"
    "v_pk_fma_f32 v[244:245], v[244:245], 0.5, 0.5 op_sel_hi:[1,0,0]\n"
    "v_pk_fma_f32 v[246:247], v[246:247], 0.5, 0.5 op_sel_hi:[1,0,0]\n"
    "v_med3_f32 v248, v168, v248, |v128|\n"
    "v_med3_f32 v249, v169, v249, |v129|\n"
    "v_med3_f32 v250, v170, v250, |v130|\n"
    "v_med3_f32 v251, v171, v251, |v131|\n"
    "v_med3_f32 v252, v172, v252, |v132|\n"
    "v_med3_f32 v253, v173, v253, |v133|\n"
    "v_med3_f32 v254, v174, v254, |v134|\n"
    "v_med3_f32 v255, v175, v255, |v135|\n"
    "v_bfi_b32 v248, s47, v248, v128\n"
    "v_bfi_b32 v249, s47, v249, v129\n"
    "v_bfi_b32 v250, s47, v250, v130\n"
    "v_bfi_b32 v251, s47, v251, v131\n"
    "v_bfi_b32 v252, s47, v252, v132\n"
    "v_bfi_b32 v253, s47, v253, v133\n"
    "v_bfi_b32 v254, s47, v254, v134\n"
    "v_bfi_b32 v255, s47, v255, v135\n"
    "s_waitcnt vmcnt(0)\n"
    "v_max3_u32 v188, v184, v185, v186\n"
    "v_max_u32 v188, v188, v187\n"
    "v_cmp_eq_u32 vcc, -1, v188\n"
    "s_cbranch_vccz LL4ok_%=\n"
    "s_mov_b32 s43, 0x100000\n"
    "LL4lp_%=:\n"
    "buffer_load_dwordx4 v[184:187], v22, s[16:19], s25 offen sc1\n"
    "s_waitcnt vmcnt(0)\n"
    "s_sub_u32 s43, s43, 1\n"
    "s_cbranch_scc1 LL4ok_%=\n"
    "v_max3_u32 v188, v184, v185, v186\n"
    "v_max_u32 v188, v188, v187\n"
    "v_cmp_eq_u32 vcc, -1, v188\n"
    "s_cbranch_vccnz LL4lp_%=\n"
    "LL4ok_%=:\n"
    "v_lshlrev_b32 v188, 16, v184\n"
    "v_and_b32 v189, s48, v184\n"
    "v_lshlrev_b32 v190, 16, v185\n"
    "v_and_b32 v191, s48, v185\n"
    "v_lshlrev_b32 v192, 16, v186\n"
    "v_and_b32 v193, s48, v186\n"
    "v_lshlrev_b32 v194, 16, v187\n"
    "v_and_b32 v195, s48, v187\n"
    "v_pk_add_f32 v[40:41], v[40:41], v[188:189]\n"
    "v_pk_add_f32 v[42:43], v[42:43], v[190:191]\n"
    "v_pk_add_f32 v[44:45], v[44:45], v[192:193]\n"
    "v_pk_add_f32 v[46:47], v[46:47], v[194:195]\n"
    "v_pk_mul_f32 v[200:201], v[40:41], s[44:45] op_sel:[0,1] op_sel_hi:[1,1]\n"
    "v_pk_mul_f32 v[202:203], v[42:43], s[44:45] op_sel:[0,1] op_sel_hi:[1,1]\n"
    "v_pk_mul_f32 v[204:205], v[44:45], s[44:45] op_sel:[0,1] op_sel_hi:[1,1]\n"
    "v_pk_mul_f32 v[206:207], v[46:47], s[44:45] op_sel:[0,1] op_sel_hi:[1,1]\n"
    "v_exp_f32 v200, v200\n"
    "v_exp_f32 v201, v201\n"
    "v_exp_f32 v202, v202\n"
    "v_exp_f32 v203, v203\n"
    "v_exp_f32 v204, v204\n"
    "v_exp_f32 v205, v205\n"
    "v_exp_f32 v206, v206\n"
    "v_exp_f32 v207, v207\n"
    "v_pk_add_f32 v[200:201], v[200:201], 1.0 op_sel_hi:[1,0]\n"
    "v_pk_add_f32 v[202:203], v[202:203], 1.0 op_sel_hi:[1,0]\n"
    "v_pk_add_f32 v[204:205], v[204:205], 1.0 op_sel_hi:[1,0]\n"
    "v_pk_add_f32 v[206:207], v[206:207], 1.0 op_sel_hi:[1,0]\n"
    "v_pk_mul_f32 v[240:241], v[240:241], v[200:201]\n"
    "v_pk_mul_f32 v[242:243], v[242:243], v[202:203]\n"
    "v_pk_mul_f32 v[244:245], v[244:245], v[204:205]\n"
    "v_pk_mul_f32 v[246:247], v[246:247], v[206:207]\n"
    "v_rcp_f32 v240, v240\n"
    "v_rcp_f32 v241, v241\n"
    "v_rcp_f32 v242, v242\n"
    "v_rcp_f32 v243, v243\n"
    "v_rcp_f32 v244, v244\n"
    "v_rcp_f32 v245, v245\n"
    "v_rcp_f32 v246, v246\n"
    "v_rcp_f32 v247, v247\n"
    "v_pk_mul_f32 v[248:249], v[248:249], v[240:241]\n"
    "v_pk_mul_f32 v[250:251], v[250:251], v[242:243]\n"
    "v_pk_mul_f32 v[252:253], v[252:253], v[244:245]\n"
    "v_pk_mul_f32 v[254:255], v[254:255], v[246:247]\n"
    "v_cvt_pk_bf16_f32 v240, v248, v249\n"
    "v_cvt_pk_bf16_f32 v241, v250, v251\n"
    "v_cvt_pk_bf16_f32 v242, v252, v253\n"
    "v_cvt_pk_bf16_f32 v243, v254, v255\n"
    "buffer_store_dwordx2 v[240:241], v25, s[20:23], 0 offen\n"
    "buffer_store_dwordx2 v[242:243], v25, s[20:23], 0 offen offset:2048\n"
    "s_endpgm\n"
    ".fill (LV0_%=+30720-.)/4, 4, 0xbf800000\n"
    "LV5_%=:\n"
    "ds_read_b128 a[192:195], v10 offset:62720\n"
    "v_mfma_f32_16x16x32_bf16 v[168:171], v[192:195], a[212:215], v[168:171]\n"
    "s_waitcnt lgkmcnt(2)\n"
    "ds_read_b128 a[196:199], v10 offset:62848\n"
    "v_mfma_f32_16x16x32_bf16 v[172:175], v[192:195], a[184:187], v[172:175]\n"
    "ds_read_b128 a[184:187], v10 offset:62976\n"
    "v_mfma_f32_16x16x32_bf16 v[96:99], a[138:141], a[236:239], v[96:99]\n"
    "ds_read_b128 a[200:203], v10 offset:63104\n"
    "v_mfma_f32_16x16x32_bf16 v[100:103], a[138:141], a[228:231], v[100:103]\n"
    "s_nop 0\n"
    "v_add_f32 v80, v80, v168\n"
    "v_add_f32 v81, v81, v169\n"
    "v_mfma_f32_16x16x32_bf16 v[104:107], a[138:141], a[204:207], v[104:107]\n"
    "v_add_f32 v82, v82, v170\n"
    "v_add_f32 v83, v83, v171\n"
    "v_mfma_f32_16x16x32_bf16 v[108:111], a[138:141], a[220:223], v[108:111]\n"
    "v_add_f32 v84, v84, v168\n"
    "v_add_f32 v85, v85, v169\n"
    "v_mfma_f32_16x16x32_bf16 v[112:115], a[138:141], a[176:179], v[112:115]\n"
    "ds_read_b128 a[176:179], v11 offset:62208\n"
    "v_add_f32 v86, v86, v170\n"
    "v_mfma_f32_16x16x32_bf16 v[116:119], a[138:141], a[232:235], v[116:119]\n"
    "v_add_f32 v87, v87, v171\n"
    "v_add_f32 v88, v88, v172\n"
    "v_mfma_f32_16x16x32_bf16 v[120:123], a[138:141], a[180:183], v[120:123]\n"
    "s_waitcnt lgkmcnt(5)\n"
    "v_add_f32 v89, v89, v173\n"
    "v_add_f32 v90, v90, v174\n"
    "v_mfma_f32_16x16x32_bf16 v[124:127], a[138:141], a[188:191], v[124:127]\n"
    "s_waitcnt lgkmcnt(4)\n"
    "v_add_f32 v91, v91, v175\n"
    "v_add_f32 v92, v92, v172\n"
    "v_mfma_f32_16x16x32_bf16 v[144:147], a[0:3], a[192:195], v[80:83]\n"
    "s_waitcnt lgkmcnt(3)\n"
    "ds_read_b128 a[180:183], v11 offset:62336\n"
    "v_add_f32 v93, v93, v173\n"
    "v_mfma_f32_16x16x32_bf16 v[148:151], a[0:3], a[196:199], v[84:87]\n"
    "s_waitcnt lgkmcnt(3)\n"
    "v_add_f32 v94, v94, v174\n"
    "v_add_f32 v95, v95, v175\n"
    "v_mfma_f32_16x16x32_bf16 v[152:155], a[0:3], a[184:187], v[88:91]\n"
    "s_waitcnt lgkmcnt(2)\n"
    "v_add_f32 v64, v64, v160\n"
    "v_add_f32 v65, v65, v161\n"
    "v_mfma_f32_16x16x32_bf16 v[156:159], a[0:3], a[200:203], v[92:95]\n"
    "v_add_f32 v66, v66, v162\n"
    "v_add_f32 v67, v67, v163\n"
    "v_mfma_f32_16x16x32_bf16 v[144:147], a[4:7], a[196:199], v[144:147]\n"
    "ds_read_b128 a[188:191], v11 offset:62464\n"
    "v_add_f32 v68, v68, v160\n"
    "v_mfma_f32_16x16x32_bf16 v[148:151], a[4:7], a[184:187], v[148:151]\n"
    "v_add_f32 v69, v69, v161\n"
    "v_add_f32 v70, v70, v162\n"
    "v_mfma_f32_16x16x32_bf16 v[152:155], a[4:7], a[200:203], v[152:155]\n"
    "s_waitcnt lgkmcnt(2)\n"
    "v_add_f32 v71, v71, v163\n"
    "v_add_f32 v72, v72, v164\n"
    "v_mfma_f32_16x16x32_bf16 v[156:159], a[4:7], a[176:179], v[156:159]\n"
    "v_add_f32 v73, v73, v165\n"
    "v_add_f32 v74, v74, v166\n"
    "v_mfma_f32_16x16x32_bf16 v[144:147], a[8:11], a[184:187], v[144:147]\n"
    "ds_read_b128 a[184:187], v11 offset:62592\n"
    "v_add_f32 v75, v75, v167\n"
    "v_mfma_f32_16x16x32_bf16 v[148:151], a[8:11], a[200:203], v[148:151]\n"
    "v_add_f32 v76, v76, v164\n"
    "v_add_f32 v77, v77, v165\n"
    "v_mfma_f32_16x16x32_bf16 v[152:155], a[8:11], a[176:179], v[152:155]\n"
    "s_waitcnt lgkmcnt(2)\n"
    "v_add_f32 v78, v78, v166\n"
    "v_add_f32 v79, v79, v167\n"
    "v_mfma_f32_16x16x32_bf16 v[156:159], a[8:11], a[180:183], v[156:159]\n"
    "v_add_f32 v104, v104, v72\n"
    "v_add_f32 v105, v105, v73\n"
    "v_mfma_f32_16x16x32_bf16 v[144:147], a[12:15], a[200:203], v[144:147]\n"
    "ds_read_b128 a[192:195], v11 offset:62720\n"
    "v_add_f32 v106, v106, v74\n"
    "v_mfma_f32_16x16x32_bf16 v[148:151], a[12:15], a[176:179], v[148:151]\n"
    "v_add_f32 v107, v107, v75\n"
    "v_add_f32 v108, v108, v76\n"
    "v_mfma_f32_16x16x32_bf16 v[152:155], a[12:15], a[180:183], v[152:155]\n"
    "s_waitcnt lgkmcnt(2)\n"
    "v_add_f32 v109, v109, v77\n"
    "v_add_f32 v110, v110, v78\n"
    "v_mfma_f32_16x16x32_bf16 v[156:159], a[12:15], a[188:191], v[156:159]\n"
    "v_add_f32 v111, v111, v79\n"
    "v_cvt_pk_bf16_f32 v200, v104, v105\n"
    "v_mfma_f32_16x16x32_bf16 v[144:147], a[16:19], a[176:179], v[144:147]\n"
    "ds_read_b128 a[176:179], v11 offset:62848\n"
    "v_cvt_pk_bf16_f32 v201, v106, v107\n"
    "v_mfma_f32_16x16x32_bf16 v[148:151], a[16:19], a[180:183], v[148:151]\n"
    "v_cvt_pk_bf16_f32 v202, v108, v109\n"
    "v_cvt_pk_bf16_f32 v203, v110, v111\n"
    "v_mfma_f32_16x16x32_bf16 v[152:155], a[16:19], a[188:191], v[152:155]\n"
    "s_waitcnt lgkmcnt(2)\n"
    "s_xor_b32 s36, s39, 131072\n"
    "v_min_u32 v200, -2, v200\n"
    "v_mfma_f32_16x16x32_bf16 v[156:159], a[16:19], a[184:187], v[156:159]\n"
    "v_min_u32 v201, -2, v201\n"
    "v_min_u32 v202, -2, v202\n"
    "v_mfma_f32_16x16x32_bf16 v[144:147], a[20:23], a[180:183], v[144:147]\n"
    "ds_read_b128 a[180:183], v11 offset:62976\n"
    "v_min_u32 v203, -2, v203\n"
    "v_mfma_f32_16x16x32_bf16 v[148:151], a[20:23], a[188:191], v[148:151]\n"
    "v_add_f32 v96, v96, v64\n"
    "v_add_f32 v97, v97, v65\n"
    "v_mfma_f32_16x16x32_bf16 v[152:155], a[20:23], a[184:187], v[152:155]\n"
    "s_waitcnt lgkmcnt(2)\n"
    "v_add_f32 v98, v98, v66\n"
    "v_add_f32 v99, v99, v67\n"
    "v_mfma_f32_16x16x32_bf16 v[156:159], a[20:23], a[192:195], v[156:159]\n"
    "v_add_f32 v100, v100, v68\n"
    "v_add_f32 v101, v101, v69\n"
    "v_mfma_f32_16x16x32_bf16 v[144:147], a[24:27], a[188:191], v[144:147]\n"
    "ds_read_b128 a[188:191], v11 offset:63104\n"
    "v_add_f32 v102, v102, v70\n"
    "v_mfma_f32_16x16x32_bf16 v[148:151], a[24:27], a[184:187], v[148:151]\n"
    "v_add_f32 v103, v103, v71\n"
    "v_cvt_pk_bf16_f32 v204, v96, v97\n"
    "v_mfma_f32_16x16x32_bf16 v[152:155], a[24:27], a[192:195], v[152:155]\n"
    "s_waitcnt lgkmcnt(2)\n"
    "v_cvt_pk_bf16_f32 v205, v98, v99\n"
    "v_cvt_pk_bf16_f32 v206, v100, v101\n"
    "v_mfma_f32_16x16x32_bf16 v[156:159], a[24:27], a[176:179], v[156:159]\n"
    "v_cvt_pk_bf16_f32 v207, v102, v103\n"
    "v_mfma_f32_16x16x32_bf16 v[144:147], a[28:31], a[184:187], v[144:147]\n"
    "s_xor_b32 s36, s39, 163840\n"
    "ds_read_b128 a[184:187], v12 offset:62208\n"
    "v_min_u32 v204, -2, v204\n"
    "v_mfma_f32_16x16x32_bf16 v[148:151], a[28:31], a[192:195], v[148:151]\n"
    "v_min_u32 v205, -2, v205\n"
    "v_min_u32 v206, -2, v206\n"
    "v_mfma_f32_16x16x32_bf16 v[152:155], a[28:31], a[176:179], v[152:155]\n"
    "s_waitcnt lgkmcnt(2)\n"
    "v_min_u32 v207, -2, v207\n"
    "v_mfma_f32_16x16x32_bf16 v[156:159], a[28:31], a[180:183], v[156:159]\n"
    "s_waitcnt vmcnt(0)\n"
    "v_cmp_ne_u64 vcc, v[28:29], v[26:27]\n"
    "s_cbranch_vccz LF5ok_%=\n"
    "s_mov_b32 s43, 0x100000\n"
    "LF5lp_%=:\n"
    "buffer_load_dwordx2 v[28:29], v23, s[16:19], s40 offen offset:0 sc1\n"
    "s_waitcnt vmcnt(0)\n"
    "s_sub_u32 s43, s43, 1\n"
    "s_cbranch_scc1 LF5ok_%=\n"
    "v_cmp_ne_u64 vcc, v[28:29], v[26:27]\n"
    "s_cbranch_vccnz LF5lp_%=\n"
    "LF5ok_%=:\n"
    "v_mfma_f32_16x16x32_bf16 v[144:147], a[32:35], a[192:195], v[144:147]\n"
    "s_xor_b32 s36, s39, 131072\n"
    "ds_read_b128 a[192:195], v12 offset:62336\n"
    "v_mfma_f32_16x16x32_bf16 v[148:151], a[32:35], a[176:179], v[148:151]\n"
    "buffer_store_dwordx4 v[200:203], v22, s[16:19], s36 offen sc1\n"
    "v_mfma_f32_16x16x32_bf16 v[152:155], a[32:35], a[180:183], v[152:155]\n"
    "s_waitcnt lgkmcnt(2)\n"
    "s_xor_b32 s36, s39, 163840\n"
    "v_mfma_f32_16x16x32_bf16 v[156:159], a[32:35], a[188:191], v[156:159]\n"
    "v_mfma_f32_16x16x32_bf16 v[144:147], a[36:39], a[176:179], v[144:147]\n"
    "ds_read_b128 a[176:179], v12 offset:62464\n"
    "v_mfma_f32_16x16x32_bf16 v[148:151], a[36:39], a[180:183], v[148:151]\n"
    "buffer_store_dwordx4 v[204:207], v22, s[16:19], s36 offen sc1\n"
    "v_mfma_f32_16x16x32_bf16 v[152:155], a[36:39], a[188:191], v[152:155]\n"
    "s_waitcnt lgkmcnt(2)\n"
    "v_add_f32 v120, v120, v88\n"
    "v_add_f32 v121, v121, v89\n"
    "v_mfma_f32_16x16x32_bf16 v[156:159], a[36:39], a[184:187], v[156:159]\n"
    "v_add_f32 v122, v122, v90\n"
    "v_add_f32 v123, v123, v91\n"
    "v_mfma_f32_16x16x32_bf16 v[144:147], a[40:43], a[180:183], v[144:147]\n"
    "ds_read_b128 a[180:183], v12 offset:62592\n"
    "v_add_f32 v124, v124, v92\n"
    "v_mfma_f32_16x16x32_bf16 v[148:151], a[40:43], a[188:191], v[148:151]\n"
    "v_add_f32 v125, v125, v93\n"
    "v_add_f32 v126, v126, v94\n"
    "v_mfma_f32_16x16x32_bf16 v[152:155], a[40:43], a[184:187], v[152:155]\n"
    "s_waitcnt lgkmcnt(2)\n"
    "v_add_f32 v127, v127, v95\n"
    "v_cvt_pk_bf16_f32 v208, v120, v121\n"
    "v_mfma_f32_16x16x32_bf16 v[156:159], a[40:43], a[192:195], v[156:159]\n"
    "v_cvt_pk_bf16_f32 v209, v122, v123\n"
    "v_cvt_pk_bf16_f32 v210, v124, v125\n"
    "v_mfma_f32_16x16x32_bf16 v[144:147], a[44:47], a[188:191], v[144:147]\n"
    "ds_read_b128 a[188:191], v12 offset:62720\n"
    "v_cvt_pk_bf16_f32 v211, v126, v127\n"
    "v_mfma_f32_16x16x32_bf16 v[148:151], a[44:47], a[184:187], v[148:151]\n"
    "s_xor_b32 s36, s39, 196608\n"
    "v_min_u32 v208, -2, v208\n"
    "v_min_u32 v209, -2, v209\n"
    "v_mfma_f32_16x16x32_bf16 v[152:155], a[44:47], a[192:195], v[152:155]\n"
    "s_waitcnt lgkmcnt(2)\n"
    "v_min_u32 v210, -2, v210\n"
    "v_min_u32 v211, -2, v211\n"
    "v_mfma_f32_16x16x32_bf16 v[156:159], a[44:47], a[176:179], v[156:159]\n"
    "buffer_store_dwordx4 v[208:211], v22, s[16:19], s36 offen sc1\n"
    "v_mfma_f32_16x16x32_bf16 v[144:147], a[48:51], a[184:187], v[144:147]\n"
    "ds_read_b128 a[184:187], v12 offset:62848\n"
    "v_add_f32 v112, v112, v80\n"
    "v_mfma_f32_16x16x32_bf16 v[148:151], a[48:51], a[192:195], v[148:151]\n"
    "v_add_f32 v113, v113, v81\n"
    "v_add_f32 v114, v114, v82\n"
    "v_mfma_f32_16x16x32_bf16 v[152:155], a[48:51], a[176:179], v[152:155]\n"
    "s_waitcnt lgkmcnt(2)\n"
    "v_add_f32 v115, v115, v83\n"
    "v_add_f32 v116, v116, v84\n"
    "v_mfma_f32_16x16x32_bf16 v[156:159], a[48:51], a[180:183], v[156:159]\n"
    "v_add_f32 v117, v117, v85\n"
    "v_add_f32 v118, v118, v86\n"
    "v_mfma_f32_16x16x32_bf16 v[144:147], a[52:55], a[192:195], v[144:147]\n"
    "ds_read_b128 a[192:195], v12 offset:62976\n"
    "v_add_f32 v119, v119, v87\n"
    "v_mfma_f32_16x16x32_bf16 v[148:151], a[52:55], a[176:179], v[148:151]\n"
    "ds_read_b128 a[196:199], v13 offset:62720\n"
    "v_cvt_pk_bf16_f32 v212, v112, v113\n"
    "v_mfma_f32_16x16x32_bf16 v[152:155], a[52:55], a[180:183], v[152:155]\n"
    "s_waitcnt lgkmcnt(3)\n"
    "ds_read_b128 a[200:203], v13 offset:62848\n"
    "v_cvt_pk_bf16_f32 v213, v114, v115\n"
    "v_mfma_f32_16x16x32_bf16 v[156:159], a[52:55], a[188:191], v[156:159]\n"
    "ds_read_b128 a[204:207], v13 offset:62976\n"
    "v_cvt_pk_bf16_f32 v214, v116, v117\n"
    "v_mfma_f32_16x16x32_bf16 v[144:147], a[56:59], a[176:179], v[144:147]\n"
    "ds_read_b128 a[176:179], v13 offset:63104\n"
    "v_cvt_pk_bf16_f32 v215, v118, v119\n"
    "v_mfma_f32_16x16x32_bf16 v[148:151], a[56:59], a[180:183], v[148:151]\n"
    "s_xor_b32 s36, s39, 229376\n"
    "v_min_u32 v212, -2, v212\n"
    "v_min_u32 v213, -2, v213\n"
    "v_mfma_f32_16x16x32_bf16 v[152:155], a[56:59], a[188:191], v[152:155]\n"
    "s_waitcnt lgkmcnt(5)\n"
    "v_min_u32 v214, -2, v214\n"
    "v_min_u32 v215, -2, v215\n"
    "v_mfma_f32_16x16x32_bf16 v[156:159], a[56:59], a[184:187], v[156:159]\n"
    "buffer_store_dwordx4 v[212:215], v22, s[16:19], s36 offen sc1\n"
    "v_mfma_f32_16x16x32_bf16 v[144:147], a[60:63], a[180:183], v[144:147]\n"
    "ds_read_b128 a[180:183], v14 offset:62208\n"
    "v_mfma_f32_16x16x32_bf16 v[148:151], a[60:63], a[188:191], v[148:151]\n"
    "v_mfma_f32_16x16x32_bf16 v[152:155], a[60:63], a[184:187], v[152:155]\n"
    "s_waitcnt lgkmcnt(5)\n"
    "v_mfma_f32_16x16x32_bf16 v[156:159], a[60:63], a[192:195], v[156:159]\n"
    "s_waitcnt lgkmcnt(4)\n"
    "v_mfma_f32_16x16x32_bf16 v[144:147], a[64:67], a[196:199], v[144:147]\n"
    "s_waitcnt lgkmcnt(3)\n"
    "ds_read_b128 a[184:187], v14 offset:62336\n"
    "v_mfma_f32_16x16x32_bf16 v[148:151], a[64:67], a[200:203], v[148:151]\n"
    "s_waitcnt lgkmcnt(3)\n"
    "v_mfma_f32_16x16x32_bf16 v[152:155], a[64:67], a[204:207], v[152:155]\n"
    "s_waitcnt lgkmcnt(2)\n"
    "v_mfma_f32_16x16x32_bf16 v[156:159], a[64:67], a[176:179], v[156:159]\n"
    "v_mfma_f32_16x16x32_bf16 v[144:147], a[68:71], a[200:203], v[144:147]\n"
    "ds_read_b128 a[188:191], v14 offset:62464\n"
    "v_mfma_f32_16x16x32_bf16 v[148:151], a[68:71], a[204:207], v[148:151]\n"
    "v_mfma_f32_16x16x32_bf16 v[152:155], a[68:71], a[176:179], v[152:155]\n"
    "s_waitcnt lgkmcnt(2)\n"
    "v_mfma_f32_16x16x32_bf16 v[156:159], a[68:71], a[180:183], v[156:159]\n"
    "v_mfma_f32_16x16x32_bf16 v[144:147], a[72:75], a[204:207], v[144:147]\n"
    "ds_read_b128 a[192:195], v14 offset:62592\n"
    "v_mfma_f32_16x16x32_bf16 v[148:151], a[72:75], a[176:179], v[148:151]\n"
    "v_mfma_f32_16x16x32_bf16 v[152:155], a[72:75], a[180:183], v[152:155]\n"
    "s_waitcnt lgkmcnt(2)\n"
    "v_mfma_f32_16x16x32_bf16 v[156:159], a[72:75], a[184:187], v[156:159]\n"
    "v_mfma_f32_16x16x32_bf16 v[144:147], a[76:79], a[176:179], v[144:147]\n"
    "ds_read_b128 a[176:179], v14 offset:62720\n"
    "v_mfma_f32_16x16x32_bf16 v[148:151], a[76:79], a[180:183], v[148:151]\n"
    "v_mfma_f32_16x16x32_bf16 v[152:155], a[76:79], a[184:187], v[152:155]\n"
    "s_waitcnt lgkmcnt(2)\n"
    "v_mfma_f32_16x16x32_bf16 v[156:159], a[76:79], a[188:191], v[156:159]\n"
    "v_mfma_f32_16x16x32_bf16 v[144:147], a[80:83], a[180:183], v[144:147]\n"
    "ds_read_b128 a[180:183], v14 offset:62848\n"
    "v_mfma_f32_16x16x32_bf16 v[148:151], a[80:83], a[184:187], v[148:151]\n"
    "v_mfma_f32_16x16x32_bf16 v[152:155], a[80:83], a[188:191], v[152:155]\n"
    "s_waitcnt lgkmcnt(2)\n"
    "v_mfma_f32_16x16x32_bf16 v[156:159], a[80:83], a[192:195], v[156:159]\n"
    "v_mfma_f32_16x16x32_bf16 v[144:147], a[84:87], a[184:187], v[144:147]\n"
    "ds_read_b128 a[184:187], v14 offset:62976\n"
    "v_mfma_f32_16x16x32_bf16 v[148:151], a[84:87], a[188:191], v[148:151]\n"
    "v_mfma_f32_16x16x32_bf16 v[152:155], a[84:87], a[192:195], v[152:155]\n"
    "s_waitcnt lgkmcnt(2)\n"
    "v_mfma_f32_16x16x32_bf16 v[156:159], a[84:87], a[176:179], v[156:159]\n"
    "v_mfma_f32_16x16x32_bf16 v[144:147], a[88:91], a[188:191], v[144:147]\n"
    "ds_read_b128 a[188:191], v14 offset:63104\n"
    "v_mfma_f32_16x16x32_bf16 v[148:151], a[88:91], a[192:195], v[148:151]\n"
    "v_mfma_f32_16x16x32_bf16 v[152:155], a[88:91], a[176:179], v[152:155]\n"
    "s_waitcnt lgkmcnt(2)\n"
    "v_mfma_f32_16x16x32_bf16 v[156:159], a[88:91], a[180:183], v[156:159]\n"
    "v_mfma_f32_16x16x32_bf16 v[144:147], a[92:95], a[192:195], v[144:147]\n"
    "ds_read_b128 a[192:195], v15 offset:62208\n"
    "v_mfma_f32_16x16x32_bf16 v[148:151], a[92:95], a[176:179], v[148:151]\n"
    "v_mfma_f32_16x16x32_bf16 v[152:155], a[92:95], a[180:183], v[152:155]\n"
    "s_waitcnt lgkmcnt(2)\n"
    "v_mfma_f32_16x16x32_bf16 v[156:159], a[92:95], a[184:187], v[156:159]\n"
    "v_mfma_f32_16x16x32_bf16 v[144:147], a[96:99], a[176:179], v[144:147]\n"
    "ds_read_b128 a[176:179], v15 offset:62336\n"
    "v_mfma_f32_16x16x32_bf16 v[148:151], a[96:99], a[180:183], v[148:151]\n"
    "v_mfma_f32_16x16x32_bf16 v[152:155], a[96:99], a[184:187], v[152:155]\n"
    "s_waitcnt lgkmcnt(2)\n"
    "v_mfma_f32_16x16x32_bf16 v[156:159], a[96:99], a[188:191], v[156:159]\n"
    "v_mfma_f32_16x16x32_bf16 v[144:147], a[100:103], a[180:183], v[144:147]\n"
    "ds_read_b128 a[180:183], v15 offset:62464\n"
    "v_mfma_f32_16x16x32_bf16 v[148:151], a[100:103], a[184:187], v[148:151]\n"
    "v_mfma_f32_16x16x32_bf16 v[152:155], a[100:103], a[188:191], v[152:155]\n"
    "s_waitcnt lgkmcnt(2)\n"
    "v_mfma_f32_16x16x32_bf16 v[156:159], a[100:103], a[192:195], v[156:159]\n"
    "v_mfma_f32_16x16x32_bf16 v[144:147], a[104:107], a[184:187], v[144:147]\n"
    "ds_read_b128 a[184:187], v15 offset:62592\n"
    "v_mfma_f32_16x16x32_bf16 v[148:151], a[104:107], a[188:191], v[148:151]\n"
    "v_mfma_f32_16x16x32_bf16 v[152:155], a[104:107], a[192:195], v[152:155]\n"
    "s_waitcnt lgkmcnt(2)\n"
    "v_mfma_f32_16x16x32_bf16 v[156:159], a[104:107], a[176:179], v[156:159]\n"
    "v_mfma_f32_16x16x32_bf16 v[144:147], a[108:111], a[188:191], v[144:147]\n"
    "ds_read_b128 a[188:191], v15 offset:62720\n"
    "v_mfma_f32_16x16x32_bf16 v[148:151], a[108:111], a[192:195], v[148:151]\n"
    "v_mfma_f32_16x16x32_bf16 v[152:155], a[108:111], a[176:179], v[152:155]\n"
    "s_waitcnt lgkmcnt(2)\n"
    "v_mfma_f32_16x16x32_bf16 v[156:159], a[108:111], a[180:183], v[156:159]\n"
    "v_mfma_f32_16x16x32_bf16 v[144:147], a[112:115], a[192:195], v[144:147]\n"
    "ds_read_b128 a[192:195], v15 offset:62848\n"
    "v_mfma_f32_16x16x32_bf16 v[148:151], a[112:115], a[176:179], v[148:151]\n"
    "v_mfma_f32_16x16x32_bf16 v[152:155], a[112:115], a[180:183], v[152:155]\n"
    "s_waitcnt lgkmcnt(2)\n"
    "buffer_load_dwordx4 v[160:163], v22, s[16:19], s28 offen sc1\n"
    "v_mfma_f32_16x16x32_bf16 v[156:159], a[112:115], a[184:187], v[156:159]\n"
    "v_mfma_f32_16x16x32_bf16 v[144:147], a[116:119], a[176:179], v[144:147]\n"
    "ds_read_b128 a[176:179], v15 offset:62976\n"
    "v_mfma_f32_16x16x32_bf16 v[148:151], a[116:119], a[180:183], v[148:151]\n"
    "ds_read_b128 a[196:199], v10 offset:62208\n"
    "v_mfma_f32_16x16x32_bf16 v[152:155], a[116:119], a[184:187], v[152:155]\n"
    "s_waitcnt lgkmcnt(3)\n"
    "ds_read_b128 a[200:203], v10 offset:62336\n"
    "v_mfma_f32_16x16x32_bf16 v[156:159], a[116:119], a[188:191], v[156:159]\n"
    "buffer_load_dwordx4 v[164:167], v22, s[16:19], s30 offen sc1\n"
    "v_mfma_f32_16x16x32_bf16 v[144:147], a[120:123], a[180:183], v[144:147]\n"
    "ds_read_b128 a[180:183], v10 offset:62464\n"
    "v_mfma_f32_16x16x32_bf16 v[148:151], a[120:123], a[184:187], v[148:151]\n"
    "v_mfma_f32_16x16x32_bf16 v[152:155], a[120:123], a[188:191], v[152:155]\n"
    "s_waitcnt lgkmcnt(4)\n"
    "ds_read_b128 a[204:207], v10 offset:62592\n"
    "v_mfma_f32_16x16x32_bf16 v[156:159], a[120:123], a[192:195], v[156:159]\n"
    "buffer_load_dwordx4 v[168:171], v22, s[16:19], s29 offen sc1\n"
    "v_mfma_f32_16x16x32_bf16 v[144:147], a[124:127], a[184:187], v[144:147]\n"
    "ds_read_b128 a[184:187], v10 offset:62720\n"
    "v_mfma_f32_16x16x32_bf16 v[148:151], a[124:127], a[188:191], v[148:151]\n"
    "v_mfma_f32_16x16x32_bf16 v[152:155], a[124:127], a[192:195], v[152:155]\n"
    "s_waitcnt lgkmcnt(5)\n"
    "ds_read_b128 a[188:191], v10 offset:62848\n"
    "v_mfma_f32_16x16x32_bf16 v[156:159], a[124:127], a[176:179], v[156:159]\n"
    "s_waitcnt lgkmcnt(5)\n"
    "buffer_load_dwordx4 v[172:175], v22, s[16:19], s31 offen sc1\n"
    "v_mfma_f32_16x16x32_bf16 v[128:131], a[0:3], a[196:199], v[64:67]\n"
    "s_waitcnt lgkmcnt(4)\n"
    "ds_read_b128 a[176:179], v10 offset:62976\n"
    "v_mfma_f32_16x16x32_bf16 v[132:135], a[0:3], a[200:203], v[68:71]\n"
    "v_mfma_f32_16x16x32_bf16 v[128:131], a[4:7], a[200:203], v[128:131]\n"
    "s_waitcnt lgkmcnt(4)\n"
    "ds_read_b128 a[192:195], v10 offset:63104\n"
    "v_mfma_f32_16x16x32_bf16 v[132:135], a[4:7], a[180:183], v[132:135]\n"
    "v_cvt_pk_bf16_f32 v216, v152, v153\n"
    "v_cvt_pk_bf16_f32 v217, v154, v155\n"
    "v_mfma_f32_16x16x32_bf16 v[128:131], a[8:11], a[180:183], v[128:131]\n"
    "s_waitcnt lgkmcnt(4)\n"
    "ds_read_b128 a[180:183], v11 offset:62208\n"
    "v_cvt_pk_bf16_f32 v218, v156, v157\n"
    "v_mfma_f32_16x16x32_bf16 v[132:135], a[8:11], a[204:207], v[132:135]\n"
    "v_cvt_pk_bf16_f32 v219, v158, v159\n"
    "v_mfma_f32_16x16x32_bf16 v[128:131], a[12:15], a[204:207], v[128:131]\n"
    "s_waitcnt lgkmcnt(4)\n"
    "s_xor_b32 s24, s39, 65536\n"
    "ds_read_b128 a[196:199], v11 offset:62336\n"
    "v_mfma_f32_16x16x32_bf16 v[132:135], a[12:15], a[184:187], v[132:135]\n"
    "v_min_u32 v216, -2, v216\n"
    "v_min_u32 v217, -2, v217\n"
    "v_mfma_f32_16x16x32_bf16 v[128:131], a[16:19], a[184:187], v[128:131]\n"
    "s_waitcnt lgkmcnt(4)\n"
    "ds_read_b128 a[184:187], v11 offset:62464\n"
    "v_min_u32 v218, -2, v218\n"
    "v_mfma_f32_16x16x32_bf16 v[132:135], a[16:19], a[188:191], v[132:135]\n"
    "v_min_u32 v219, -2, v219\n"
    "v_mfma_f32_16x16x32_bf16 v[128:131], a[20:23], a[188:191], v[128:131]\n"
    "s_waitcnt lgkmcnt(4)\n"
    "ds_read_b128 a[188:191], v11 offset:62592\n"
    "v_mfma_f32_16x16x32_bf16 v[132:135], a[20:23], a[176:179], v[132:135]\n"
    "buffer_store_dwordx4 v[216:219], v22, s[16:19], s24 offen sc1\n"
    "v_mfma_f32_16x16x32_bf16 v[128:131], a[24:27], a[176:179], v[128:131]\n"
    "s_waitcnt lgkmcnt(4)\n"
    "ds_read_b128 a[176:179], v11 offset:62720\n"
    "v_cvt_pk_bf16_f32 v220, v144, v145\n"
    "v_mfma_f32_16x16x32_bf16 v[132:135], a[24:27], a[192:195], v[132:135]\n"
    "v_cvt_pk_bf16_f32 v221, v146, v147\n"
    "v_cvt_pk_bf16_f32 v222, v148, v149\n"
    "v_mfma_f32_16x16x32_bf16 v[128:131], a[28:31], a[192:195], v[128:131]\n"
    "s_waitcnt lgkmcnt(4)\n"
    "ds_read_b128 a[192:195], v11 offset:62848\n"
    "v_cvt_pk_bf16_f32 v223, v150, v151\n"
    "v_mfma_f32_16x16x32_bf16 v[132:135], a[28:31], a[180:183], v[132:135]\n"
    "s_xor_b32 s24, s39, 98304\n"
    "v_min_u32 v220, -2, v220\n"
    "v_min_u32 v221, -2, v221\n"
    "v_mfma_f32_16x16x32_bf16 v[128:131], a[32:35], a[180:183], v[128:131]\n"
    "s_waitcnt lgkmcnt(4)\n"
    "ds_read_b128 a[180:183], v11 offset:62976\n"
    "v_min_u32 v222, -2, v222\n"
    "v_mfma_f32_16x16x32_bf16 v[132:135], a[32:35], a[196:199], v[132:135]\n"
    "v_min_u32 v223, -2, v223\n"
    "v_mfma_f32_16x16x32_bf16 v[128:131], a[36:39], a[196:199], v[128:131]\n"
    "s_waitcnt lgkmcnt(4)\n"
    "ds_read_b128 a[196:199], v11 offset:63104\n"
    "v_mfma_f32_16x16x32_bf16 v[132:135], a[36:39], a[184:187], v[132:135]\n"
    "buffer_store_dwordx4 v[220:223], v22, s[16:19], s24 offen sc1\n"
    "v_mfma_f32_16x16x32_bf16 v[128:131], a[40:43], a[184:187], v[128:131]\n"
    "s_waitcnt lgkmcnt(4)\n"
    "ds_read_b128 a[184:187], v12 offset:62208\n"
    "v_mfma_f32_16x16x32_bf16 v[132:135], a[40:43], a[188:191], v[132:135]\n"
    "ds_read_b128 a[200:203], v13 offset:62208\n"
    "v_mfma_f32_16x16x32_bf16 v[128:131], a[44:47], a[188:191], v[128:131]\n"
    "s_waitcnt lgkmcnt(5)\n"
    "ds_read_b128 a[188:191], v13 offset:62336\n"
    "v_mfma_f32_16x16x32_bf16 v[132:135], a[44:47], a[176:179], v[132:135]\n"
    "v_mfma_f32_16x16x32_bf16 v[128:131], a[48:51], a[176:179], v[128:131]\n"
    "s_waitcnt lgkmcnt(5)\n"
    "ds_read_b128 a[176:179], v13 offset:62464\n"
    "v_mfma_f32_16x16x32_bf16 v[132:135], a[48:51], a[192:195], v[132:135]\n"
    "v_mfma_f32_16x16x32_bf16 v[128:131], a[52:55], a[192:195], v[128:131]\n"
    "s_waitcnt lgkmcnt(5)\n"
    "ds_read_b128 a[192:195], v13 offset:62592\n"
    "v_mfma_f32_16x16x32_bf16 v[132:135], a[52:55], a[180:183], v[132:135]\n"
    "v_mfma_f32_16x16x32_bf16 v[128:131], a[56:59], a[180:183], v[128:131]\n"
    "s_waitcnt lgkmcnt(5)\n"
    "ds_read_b128 a[180:183], v13 offset:62720\n"
    "v_mfma_f32_16x16x32_bf16 v[132:135], a[56:59], a[196:199], v[132:135]\n"
    "v_mfma_f32_16x16x32_bf16 v[128:131], a[60:63], a[196:199], v[128:131]\n"
    "s_waitcnt lgkmcnt(5)\n"
    "ds_read_b128 a[196:199], v13 offset:62848\n"
    "v_mfma_f32_16x16x32_bf16 v[132:135], a[60:63], a[184:187], v[132:135]\n"
    "s_waitcnt lgkmcnt(5)\n"
    "v_mfma_f32_16x16x32_bf16 v[128:131], a[64:67], a[200:203], v[128:131]\n"
    "s_waitcnt lgkmcnt(4)\n"
    "ds_read_b128 a[184:187], v13 offset:62976\n"
    "v_mfma_f32_16x16x32_bf16 v[132:135], a[64:67], a[188:191], v[132:135]\n"
    "v_mfma_f32_16x16x32_bf16 v[128:131], a[68:71], a[188:191], v[128:131]\n"
    "s_waitcnt lgkmcnt(4)\n"
    "ds_read_b128 a[188:191], v13 offset:63104\n"
    "v_mfma_f32_16x16x32_bf16 v[132:135], a[68:71], a[176:179], v[132:135]\n"
    "v_mfma_f32_16x16x32_bf16 v[128:131], a[72:75], a[176:179], v[128:131]\n"
    "s_waitcnt lgkmcnt(4)\n"
    "ds_read_b128 a[176:179], v14 offset:62208\n"
    "v_mfma_f32_16x16x32_bf16 v[132:135], a[72:75], a[192:195], v[132:135]\n"
    "v_mfma_f32_16x16x32_bf16 v[128:131], a[76:79], a[192:195], v[128:131]\n"
    "s_waitcnt lgkmcnt(4)\n"
    "ds_read_b128 a[192:195], v14 offset:62336\n"
    "v_mfma_f32_16x16x32_bf16 v[132:135], a[76:79], a[180:183], v[132:135]\n"
    "v_mfma_f32_16x16x32_bf16 v[128:131], a[80:83], a[180:183], v[128:131]\n"
    "s_waitcnt lgkmcnt(4)\n"
    "ds_read_b128 a[180:183], v14 offset:62464\n"
    "v_mfma_f32_16x16x32_bf16 v[132:135], a[80:83], a[196:199], v[132:135]\n"
    "v_mfma_f32_16x16x32_bf16 v[128:131], a[84:87], a[196:199], v[128:131]\n"
    "s_waitcnt lgkmcnt(4)\n"
    "ds_read_b128 a[196:199], v14 offset:62592\n"
    "v_mfma_f32_16x16x32_bf16 v[132:135], a[84:87], a[184:187], v[132:135]\n"
    "v_mfma_f32_16x16x32_bf16 v[128:131], a[88:91], a[184:187], v[128:131]\n"
    "s_waitcnt lgkmcnt(4)\n"
    "ds_read_b128 a[184:187], v14 offset:62720\n"
    "v_mfma_f32_16x16x32_bf16 v[132:135], a[88:91], a[188:191], v[132:135]\n"
    "v_mfma_f32_16x16x32_bf16 v[128:131], a[92:95], a[188:191], v[128:131]\n"
    "s_waitcnt lgkmcnt(4)\n"
    "ds_read_b128 a[188:191], v14 offset:62848\n"
    "v_mfma_f32_16x16x32_bf16 v[132:135], a[92:95], a[176:179], v[132:135]\n"
    "v_mfma_f32_16x16x32_bf16 v[128:131], a[96:99], a[176:179], v[128:131]\n"
    "s_waitcnt lgkmcnt(4)\n"
    "ds_read_b128 a[176:179], v14 offset:62976\n"
    "v_mfma_f32_16x16x32_bf16 v[132:135], a[96:99], a[192:195], v[132:135]\n"
    "v_mfma_f32_16x16x32_bf16 v[128:131], a[100:103], a[192:195], v[128:131]\n"
    "s_waitcnt lgkmcnt(4)\n"
    "ds_read_b128 a[192:195], v14 offset:63104\n"
    "v_mfma_f32_16x16x32_bf16 v[132:135], a[100:103], a[180:183], v[132:135]\n"
    "v_mfma_f32_16x16x32_bf16 v[128:131], a[104:107], a[180:183], v[128:131]\n"
    "s_waitcnt lgkmcnt(4)\n"
    "ds_read_b128 a[180:183], v15 offset:62208\n"
    "v_mfma_f32_16x16x32_bf16 v[132:135], a[104:107], a[196:199], v[132:135]\n"
    "ds_read_b128 a[200:203], v10 offset:62464\n"
    "v_mfma_f32_16x16x32_bf16 v[128:131], a[108:111], a[196:199], v[128:131]\n"
    "s_waitcnt lgkmcnt(5)\n"
    "ds_read_b128 a[196:199], v10 offset:62592\n"
    "v_mfma_f32_16x16x32_bf16 v[132:135], a[108:111], a[184:187], v[132:135]\n"
    "v_mfma_f32_16x16x32_bf16 v[128:131], a[112:115], a[184:187], v[128:131]\n"
    "s_waitcnt lgkmcnt(5)\n"
    "ds_read_b128 a[184:187], v10 offset:62720\n"
    "v_mfma_f32_16x16x32_bf16 v[132:135], a[112:115], a[188:191], v[132:135]\n"
    "v_mfma_f32_16x16x32_bf16 v[128:131], a[116:119], a[188:191], v[128:131]\n"
    "s_waitcnt lgkmcnt(5)\n"
    "ds_read_b128 a[188:191], v10 offset:62848\n"
    "v_mfma_f32_16x16x32_bf16 v[132:135], a[116:119], a[176:179], v[132:135]\n"
    "v_mfma_f32_16x16x32_bf16 v[128:131], a[120:123], a[176:179], v[128:131]\n"
    "s_waitcnt lgkmcnt(5)\n"
    "ds_read_b128 a[176:179], v10 offset:62976\n"
    "v_mfma_f32_16x16x32_bf16 v[132:135], a[120:123], a[192:195], v[132:135]\n"
    "v_mfma_f32_16x16x32_bf16 v[128:131], a[124:127], a[192:195], v[128:131]\n"
    "s_waitcnt lgkmcnt(5)\n"
    "ds_read_b128 a[192:195], v10 offset:63104\n"
    "v_mfma_f32_16x16x32_bf16 v[132:135], a[124:127], a[180:183], v[132:135]\n"
    "s_waitcnt lgkmcnt(5)\n"
    "v_mfma_f32_16x16x32_bf16 v[136:139], a[0:3], a[200:203], v[72:75]\n"
    "s_waitcnt lgkmcnt(4)\n"
    "ds_read_b128 a[180:183], v11 offset:62208\n"
    "v_mfma_f32_16x16x32_bf16 v[140:143], a[0:3], a[196:199], v[76:79]\n"
    "v_mfma_f32_16x16x32_bf16 v[136:139], a[4:7], a[196:199], v[136:139]\n"
    "s_waitcnt lgkmcnt(4)\n"
    "ds_read_b128 a[196:199], v11 offset:62336\n"
    "v_mfma_f32_16x16x32_bf16 v[140:143], a[4:7], a[184:187], v[140:143]\n"
    "v_cvt_pk_bf16_f32 v224, v128, v129\n"
    "v_cvt_pk_bf16_f32 v225, v130, v131\n"
    "v_mfma_f32_16x16x32_bf16 v[136:139], a[8:11], a[184:187], v[136:139]\n"
    "s_waitcnt lgkmcnt(4)\n"
    "ds_read_b128 a[184:187], v11 offset:62464\n"
    "v_cvt_pk_bf16_f32 v226, v132, v133\n"
    "v_mfma_f32_16x16x32_bf16 v[140:143], a[8:11], a[188:191], v[140:143]\n"
    "v_cvt_pk_bf16_f32 v227, v134, v135\n"
    "v_mfma_f32_16x16x32_bf16 v[136:139], a[12:15], a[188:191], v[136:139]\n"
    "s_waitcnt lgkmcnt(4)\n"
    "s_xor_b32 s24, s39, 32768\n"
    "ds_read_b128 a[188:191], v11 offset:62592\n"
    "v_mfma_f32_16x16x32_bf16 v[140:143], a[12:15], a[176:179], v[140:143]\n"
    "v_min_u32 v224, -2, v224\n"
    "v_min_u32 v225, -2, v225\n"
    "v_mfma_f32_16x16x32_bf16 v[136:139], a[16:19], a[176:179], v[136:139]\n"
    "s_waitcnt lgkmcnt(4)\n"
    "ds_read_b128 a[176:179], v11 offset:62720\n"
    "v_min_u32 v226, -2, v226\n"
    "v_mfma_f32_16x16x32_bf16 v[140:143], a[16:19], a[192:195], v[140:143]\n"
    "v_min_u32 v227, -2, v227\n"
    "v_mfma_f32_16x16x32_bf16 v[136:139], a[20:23], a[192:195], v[136:139]\n"
    "s_waitcnt lgkmcnt(4)\n"
    "ds_read_b128 a[192:195], v11 offset:62848\n"
    "v_mfma_f32_16x16x32_bf16 v[140:143], a[20:23], a[180:183], v[140:143]\n"
    "buffer_store_dwordx4 v[224:227], v22, s[16:19], s24 offen sc1\n"
    "v_mfma_f32_16x16x32_bf16 v[136:139], a[24:27], a[180:183], v[136:139]\n"
    "s_waitcnt lgkmcnt(4)\n"
    "ds_read_b128 a[180:183], v11 offset:62976\n"
    "v_mfma_f32_16x16x32_bf16 v[140:143], a[24:27], a[196:199], v[140:143]\n"
    "v_mfma_f32_16x16x32_bf16 v[136:139], a[28:31], a[196:199], v[136:139]\n"
    "s_waitcnt lgkmcnt(4)\n"
    "ds_read_b128 a[196:199], v11 offset:63104\n"
    "v_mfma_f32_16x16x32_bf16 v[140:143], a[28:31], a[184:187], v[140:143]\n"
    "v_mfma_f32_16x16x32_bf16 v[136:139], a[32:35], a[184:187], v[136:139]\n"
    "s_waitcnt lgkmcnt(4)\n"
    "ds_read_b128 a[184:187], v12 offset:62208\n"
    "v_mfma_f32_16x16x32_bf16 v[140:143], a[32:35], a[188:191], v[140:143]\n"
    "v_mfma_f32_16x16x32_bf16 v[136:139], a[36:39], a[188:191], v[136:139]\n"
    "s_waitcnt lgkmcnt(4)\n"
    "ds_read_b128 a[188:191], v12 offset:62336\n"
    "v_mfma_f32_16x16x32_bf16 v[140:143], a[36:39], a[176:179], v[140:143]\n"
    "v_mfma_f32_16x16x32_bf16 v[136:139], a[40:43], a[176:179], v[136:139]\n"
    "s_waitcnt lgkmcnt(4)\n"
    "ds_read_b128 a[176:179], v12 offset:62464\n"
    "v_mfma_f32_16x16x32_bf16 v[140:143], a[40:43], a[192:195], v[140:143]\n"
    "ds_read_b128 a[200:203], v13 offset:62464\n"
    "v_mfma_f32_16x16x32_bf16 v[136:139], a[44:47], a[192:195], v[136:139]\n"
    "s_waitcnt lgkmcnt(5)\n"
    "ds_read_b128 a[192:195], v13 offset:62592\n"
    "v_mfma_f32_16x16x32_bf16 v[140:143], a[44:47], a[180:183], v[140:143]\n"
    "v_mfma_f32_16x16x32_bf16 v[136:139], a[48:51], a[180:183], v[136:139]\n"
    "s_waitcnt lgkmcnt(5)\n"
    "s_waitcnt vmcnt(3)\n"
    "ds_read_b128 a[180:183], v13 offset:62720\n"
    "v_mfma_f32_16x16x32_bf16 v[140:143], a[48:51], a[196:199], v[140:143]\n"
    "v_max3_u32 v240, v160, v161, v162\n"
    "v_max3_u32 v240, v240, v163, v164\n"
    "v_mfma_f32_16x16x32_bf16 v[136:139], a[52:55], a[196:199], v[136:139]\n"
    "s_waitcnt lgkmcnt(5)\n"
    "ds_read_b128 a[196:199], v13 offset:62848\n"
    "v_max3_u32 v240, v240, v165, v166\n"
    "v_mfma_f32_16x16x32_bf16 v[140:143], a[52:55], a[184:187], v[140:143]\n"
    "buffer_load_dwordx4 v[176:179], v22, s[16:19], s26 offen sc1\n"
    "v_mfma_f32_16x16x32_bf16 v[136:139], a[56:59], a[184:187], v[136:139]\n"
    "s_waitcnt lgkmcnt(5)\n"
    "ds_read_b128 a[184:187], v13 offset:62976\n"
    "v_max3_u32 v240, v240, v167, v168\n"
    "v_mfma_f32_16x16x32_bf16 v[140:143], a[56:59], a[188:191], v[140:143]\n"
    "v_max3_u32 v240, v240, v169, v170\n"
    "v_max3_u32 v240, v240, v171, v172\n"
    "v_mfma_f32_16x16x32_bf16 v[136:139], a[60:63], a[188:191], v[136:139]\n"
    "s_waitcnt lgkmcnt(5)\n"
    "ds_read_b128 a[188:191], v13 offset:63104\n"
    "v_max3_u32 v240, v240, v173, v174\n"
    "v_mfma_f32_16x16x32_bf16 v[140:143], a[60:63], a[176:179], v[140:143]\n"
    "s_waitcnt lgkmcnt(5)\n"
    "buffer_load_dwordx4 v[180:183], v22, s[16:19], s27 offen sc1\n"
    "v_mfma_f32_16x16x32_bf16 v[136:139], a[64:67], a[200:203], v[136:139]\n"
    "s_waitcnt lgkmcnt(4)\n"
    "ds_read_b128 a[176:179], v14 offset:62208\n"
    "v_max_u32 v240, v240, v175\n"
    "v_mfma_f32_16x16x32_bf16 v[140:143], a[64:67], a[192:195], v[140:143]\n"
    "v_cmp_eq_u32 vcc, -1, v240\n"
    "s_cbranch_vccz LE5ok_%=\n"
    "s_mov_b32 s43, 0x100000\n"
    "LE5lp_%=:\n"
    "buffer_load_dwordx4 v[160:163], v22, s[16:19], s28 offen sc1\n"
    "buffer_load_dwordx4 v[164:167], v22, s[16:19], s30 offen sc1\n"
    "buffer_load_dwordx4 v[168:171], v22, s[16:19], s29 offen sc1\n"
    "buffer_load_dwordx4 v[172:175], v22, s[16:19], s31 offen sc1\n"
    "s_waitcnt vmcnt(0)\n"
    "s_sub_u32 s43, s43, 1\n"
    "s_cbranch_scc1 LE5ok_%=\n"
    "v_max3_u32 v240, v160, v161, v162\n"
    "v_max3_u32 v240, v240, v163, v164\n"
    "v_max3_u32 v240, v240, v165, v166\n"
    "v_max3_u32 v240, v240, v167, v168\n"
    "v_max3_u32 v240, v240, v169, v170\n"
    "v_max3_u32 v240, v240, v171, v172\n"
    "v_max3_u32 v240, v240, v173, v174\n"
    "v_max_u32 v240, v240, v175\n"
    "v_cmp_eq_u32 vcc, -1, v240\n"
    "s_cbranch_vccnz LE5lp_%=\n"
    "LE5ok_%=:\n"
    "v_mfma_f32_16x16x32_bf16 v[136:139], a[68:71], a[192:195], v[136:139]\n"
    "s_waitcnt lgkmcnt(4)\n"
    "ds_read_b128 a[192:195], v14 offset:62336\n"
    "v_lshlrev_b32 v188, 16, v160\n"
    "v_mfma_f32_16x16x32_bf16 v[140:143], a[68:71], a[180:183], v[140:143]\n"
    "v_and_b32 v189, s48, v160\n"
    "v_lshlrev_b32 v190, 16, v161\n"
    "v_mfma_f32_16x16x32_bf16 v[136:139], a[72:75], a[180:183], v[136:139]\n"
    "s_waitcnt lgkmcnt(4)\n"
    "ds_read_b128 a[180:183], v14 offset:62464\n"
    "v_and_b32 v191, s48, v161\n"
    "v_mfma_f32_16x16x32_bf16 v[140:143], a[72:75], a[196:199], v[140:143]\n"
    "v_lshlrev_b32 v192, 16, v162\n"
    "v_and_b32 v193, s48, v162\n"
    "v_mfma_f32_16x16x32_bf16 v[136:139], a[76:79], a[196:199], v[136:139]\n"
    "s_waitcnt lgkmcnt(4)\n"
    "ds_read_b128 a[196:199], v14 offset:62592\n"
    "v_lshlrev_b32 v194, 16, v163\n"
    "v_mfma_f32_16x16x32_bf16 v[140:143], a[76:79], a[184:187], v[140:143]\n"
    "v_and_b32 v195, s48, v163\n"
    "v_lshlrev_b32 v240, 16, v164\n"
    "v_mfma_f32_16x16x32_bf16 v[136:139], a[80:83], a[184:187], v[136:139]\n"
    "s_waitcnt lgkmcnt(4)\n"
    "ds_read_b128 a[184:187], v14 offset:62720\n"
    "v_and_b32 v241, s48, v164\n"
    "v_mfma_f32_16x16x32_bf16 v[140:143], a[80:83], a[188:191], v[140:143]\n"
    "v_lshlrev_b32 v242, 16, v165\n"
    "v_and_b32 v243, s48, v165\n"
    "v_mfma_f32_16x16x32_bf16 v[136:139], a[84:87], a[188:191], v[136:139]\n"
    "s_waitcnt lgkmcnt(4)\n"
    "ds_read_b128 a[188:191], v14 offset:62848\n"
    "v_lshlrev_b32 v244, 16, v166\n"
    "v_mfma_f32_16x16x32_bf16 v[140:143], a[84:87], a[176:179], v[140:143]\n"
    "v_and_b32 v245, s48, v166\n"
    "v_lshlrev_b32 v246, 16, v167\n"
    "v_mfma_f32_16x16x32_bf16 v[136:139], a[88:91], a[176:179], v[136:139]\n"
    "s_waitcnt lgkmcnt(4)\n"
    "ds_read_b128 a[176:179], v14 offset:62976\n"
    "v_and_b32 v247, s48, v167\n"
    "v_mfma_f32_16x16x32_bf16 v[140:143], a[88:91], a[192:195], v[140:143]\n"
    "v_add_f32 v188, v188, v240\n"
    "v_add_f32 v189, v189, v241\n"
    "v_mfma_f32_16x16x32_bf16 v[136:139], a[92:95], a[192:195], v[136:139]\n"
    "s_waitcnt lgkmcnt(4)\n"
    "ds_read_b128 a[192:195], v14 offset:63104\n"
    "v_add_f32 v190, v190, v242\n"
    "v_mfma_f32_16x16x32_bf16 v[140:143], a[92:95], a[180:183], v[140:143]\n"
    "v_add_f32 v191, v191, v243\n"
    "v_add_f32 v192, v192, v244\n"
    "v_mfma_f32_16x16x32_bf16 v[136:139], a[96:99], a[180:183], v[136:139]\n"
    "s_waitcnt lgkmcnt(4)\n"
    "ds_read_b128 a[180:183], v15 offset:62208\n"
    "v_add_f32 v193, v193, v245\n"
    "v_mfma_f32_16x16x32_bf16 v[140:143], a[96:99], a[196:199], v[140:143]\n"
    "v_add_f32 v194, v194, v246\n"
    "v_add_f32 v195, v195, v247\n"
    "v_mfma_f32_16x16x32_bf16 v[136:139], a[100:103], a[196:199], v[136:139]\n"
    "s_waitcnt lgkmcnt(4)\n"
    "ds_read_b128 a[196:199], v15 offset:62336\n"
    "v_lshlrev_b32 v40, 16, v168\n"
    "v_mfma_f32_16x16x32_bf16 v[140:143], a[100:103], a[184:187], v[140:143]\n"
    "v_and_b32 v41, s48, v168\n"
    "v_lshlrev_b32 v42, 16, v169\n"
    "v_mfma_f32_16x16x32_bf16 v[136:139], a[104:107], a[184:187], v[136:139]\n"
    "s_waitcnt lgkmcnt(4)\n"
    "ds_read_b128 a[184:187], v15 offset:62464\n"
    "v_and_b32 v43, s48, v169\n"
    "v_mfma_f32_16x16x32_bf16 v[140:143], a[104:107], a[188:191], v[140:143]\n"
    "v_lshlrev_b32 v44, 16, v170\n"
    "v_and_b32 v45, s48, v170\n"
    "v_mfma_f32_16x16x32_bf16 v[136:139], a[108:111], a[188:191], v[136:139]\n"
    "s_waitcnt lgkmcnt(4)\n"
    "buffer_load_dwordx4 v[184:187], v22, s[16:19], s25 offen sc1\n"
    "v_mfma_f32_16x16x32_bf16 v[140:143], a[108:111], a[176:179], v[140:143]\n"
    "v_lshlrev_b32 v46, 16, v171\n"
    "v_and_b32 v47, s48, v171\n"
    "v_mfma_f32_16x16x32_bf16 v[136:139], a[112:115], a[176:179], v[136:139]\n"
    "s_waitcnt lgkmcnt(3)\n"
    "v_lshlrev_b32 v240, 16, v172\n"
    "v_and_b32 v241, s48, v172\n"
    "v_mfma_f32_16x16x32_bf16 v[140:143], a[112:115], a[192:195], v[140:143]\n"
    "v_lshlrev_b32 v242, 16, v173\n"
    "v_and_b32 v243, s48, v173\n"
    "v_mfma_f32_16x16x32_bf16 v[136:139], a[116:119], a[192:195], v[136:139]\n"
    "s_waitcnt lgkmcnt(2)\n"
    "v_lshlrev_b32 v244, 16, v174\n"
    "v_and_b32 v245, s48, v174\n"
    "v_mfma_f32_16x16x32_bf16 v[140:143], a[116:119], a[180:183], v[140:143]\n"
    "v_lshlrev_b32 v246, 16, v175\n"
    "v_and_b32 v247, s48, v175\n"
    "v_mfma_f32_16x16x32_bf16 v[136:139], a[120:123], a[180:183], v[136:139]\n"
    "s_waitcnt lgkmcnt(1)\n"
    "v_add_f32 v40, v40, v240\n"
    "v_add_f32 v41, v41, v241\n"
    "v_mfma_f32_16x16x32_bf16 v[140:143], a[120:123], a[196:199], v[140:143]\n"
    "v_add_f32 v42, v42, v242\n"
    "v_add_f32 v43, v43, v243\n"
    "v_mfma_f32_16x16x32_bf16 v[136:139], a[124:127], a[196:199], v[136:139]\n"
    "s_waitcnt lgkmcnt(0)\n"
    "v_add_f32 v44, v44, v244\n"
    "v_add_f32 v45, v45, v245\n"
    "v_mfma_f32_16x16x32_bf16 v[140:143], a[124:127], a[184:187], v[140:143]\n"
    "v_add_f32 v46, v46, v246\n"
    "v_add_f32 v47, v47, v247\n"
    "s_waitcnt vmcnt(1)\n"
    "v_max3_u32 v240, v176, v177, v178\n"
    "v_max3_u32 v240, v240, v179, v180\n"
    "v_max3_u32 v240, v240, v181, v182\n"
    "v_max_u32 v240, v240, v183\n"
    "v_cmp_eq_u32 vcc, -1, v240\n"
    "s_cbranch_vccz LM5ok_%=\n"
    "s_mov_b32 s43, 0x100000\n"
    "LM5lp_%=:\n"
    "buffer_load_dwordx4 v[176:179], v22, s[16:19], s26 offen sc1\n"
    "buffer_load_dwordx4 v[180:183], v22, s[16:19], s27 offen sc1\n"
    "s_waitcnt vmcnt(0)\n"
    "s_sub_u32 s43, s43, 1\n"
    "s_cbranch_scc1 LM5ok_%=\n"
    "v_max3_u32 v240, v176, v177, v178\n"
    "v_max3_u32 v240, v240, v179, v180\n"
    "v_max3_u32 v240, v240, v181, v182\n"
    "v_max_u32 v240, v240, v183\n"
    "v_cmp_eq_u32 vcc, -1, v240\n"
    "s_cbranch_vccnz LM5lp_%=\n"
    "LM5ok_%=:\n"
    "v_mfma_f32_16x16x32_bf16 v[136:139], v[236:239], v[176:179], v[136:139]\n"
    "v_mfma_f32_16x16x32_bf16 v[140:143], v[234:237], v[176:179], v[140:143]\n"
    "v_mfma_f32_16x16x32_bf16 v[40:43], v[236:239], v[180:183], v[40:43]\n"
    "v_mfma_f32_16x16x32_bf16 v[44:47], v[234:237], v[180:183], v[44:47]\n"
    "s_nop 4\n"
    "v_pk_add_f32 v[136:137], v[136:137], v[188:189]\n"
    "v_pk_add_f32 v[138:139], v[138:139], v[190:191]\n"
    "v_pk_add_f32 v[140:141], v[140:141], v[192:193]\n"
    "v_pk_add_f32 v[142:143], v[142:143], v[194:195]\n"
    "v_pk_mul_f32 v[200:201], v[136:137], s[44:45] op_sel:[0,1] op_sel_hi:[1,1]\n"
    "v_pk_mul_f32 v[202:203], v[138:139], s[44:45] op_sel:[0,1] op_sel_hi:[1,1]\n"
    "v_pk_mul_f32 v[204:205], v[140:141], s[44:45] op_sel:[0,1] op_sel_hi:[1,1]\n"
    "v_pk_mul_f32 v[206:207], v[142:143], s[44:45] op_sel:[0,1] op_sel_hi:[1,1]\n"
    "v_exp_f32 v200, v200\n"
    "v_exp_f32 v201, v201\n"
    "v_exp_f32 v202, v202\n"
    "v_exp_f32 v203, v203\n"
    "v_exp_f32 v204, v204\n"
    "v_exp_f32 v205, v205\n"
    "v_exp_f32 v206, v206\n"
    "v_exp_f32 v207, v207\n"
    "v_pk_add_f32 v[200:201], v[200:201], 1.0 op_sel_hi:[1,0]\n"
    "v_pk_add_f32 v[202:203], v[202:203], 1.0 op_sel_hi:[1,0]\n"
    "v_pk_add_f32 v[204:205], v[204:205], 1.0 op_sel_hi:[1,0]\n"
    "v_pk_add_f32 v[206:207], v[206:207], 1.0 op_sel_hi:[1,0]\n"
    "s_waitcnt vmcnt(0)\n"
    "v_max3_u32 v188, v184, v185, v186\n"
    "v_max_u32 v188, v188, v187\n"
    "v_cmp_eq_u32 vcc, -1, v188\n"
    "s_cbranch_vccz LL5ok_%=\n"
    "s_mov_b32 s43, 0x100000\n"
    "LL5lp_%=:\n"
    "buffer_load_dwordx4 v[184:187], v22, s[16:19], s25 offen sc1\n"
    "s_waitcnt vmcnt(0)\n"
    "s_sub_u32 s43, s43, 1\n"
    "s_cbranch_scc1 LL5ok_%=\n"
    "v_max3_u32 v188, v184, v185, v186\n"
    "v_max_u32 v188, v188, v187\n"
    "v_cmp_eq_u32 vcc, -1, v188\n"
    "s_cbranch_vccnz LL5lp_%=\n"
    "LL5ok_%=:\n"
    "v_lshlrev_b32 v188, 16, v184\n"
    "v_and_b32 v189, s48, v184\n"
    "v_lshlrev_b32 v190, 16, v185\n"
    "v_and_b32 v191, s48, v185\n"
    "v_lshlrev_b32 v192, 16, v186\n"
    "v_and_b32 v193, s48, v186\n"
    "v_lshlrev_b32 v194, 16, v187\n"
    "v_and_b32 v195, s48, v187\n"
    "v_pk_add_f32 v[40:41], v[40:41], v[188:189]\n"
    "v_pk_add_f32 v[42:43], v[42:43], v[190:191]\n"
    "v_pk_add_f32 v[44:45], v[44:45], v[192:193]\n"
    "v_pk_add_f32 v[46:47], v[46:47], v[194:195]\n"
    "v_pk_mul_f32 v[240:241], v[40:41], s[46:47] op_sel_hi:[1,0]\n"
    "v_pk_mul_f32 v[242:243], v[42:43], s[46:47] op_sel_hi:[1,0]\n"
    "v_pk_mul_f32 v[244:245], v[44:45], s[46:47] op_sel_hi:[1,0]\n"
    "v_pk_mul_f32 v[246:247], v[46:47], s[46:47] op_sel_hi:[1,0]\n"
    "v_exp_f32 v240, -|v240|\n"
    "v_fma_f32 v248, -v40, v40, |v40|\n"
    "v_exp_f32 v241, -|v241|\n"
    "v_fma_f32 v249, -v41, v41, |v41|\n"
    "v_exp_f32 v242, -|v242|\n"
    "v_fma_f32 v250, -v42, v42, |v42|\n"
    "v_exp_f32 v243, -|v243|\n"
    "v_fma_f32 v251, -v43, v43, |v43|\n"
    "v_exp_f32 v244, -|v244|\n"
    "v_fma_f32 v252, -v44, v44, |v44|\n"
    "v_exp_f32 v245, -|v245|\n"
    "v_fma_f32 v253, -v45, v45, |v45|\n"
    "v_exp_f32 v246, -|v246|\n"
    "v_fma_f32 v254, -v46, v46, |v46|\n"
    "v_exp_f32 v247, -|v247|\n"
    "v_fma_f32 v255, -v47, v47, |v47|\n"
    "v_pk_fma_f32 v[168:169], v[240:241], 0.5, 0.5 op_sel_hi:[1,0,0] neg_lo:[1,0,0] neg_hi:[1,0,0]\n"
    "v_pk_fma_f32 v[170:171], v[242:243], 0.5, 0.5 op_sel_hi:[1,0,0] neg_lo:[1,0,0] neg_hi:[1,0,0]\n"
    "v_pk_fma_f32 v[172:173], v[244:245], 0.5, 0.5 op_sel_hi:[1,0,0] neg_lo:[1,0,0] neg_hi:[1,0,0]\n"
    "v_pk_fma_f32 v[174:175], v[246:247], 0.5, 0.5 op_sel_hi:[1,0,0] neg_lo:[1,0,0] neg_hi:[1,0,0]\n"
    "v_pk_fma_f32 v[240:241], v[240:241], 0.5, 0.5 op_sel_hi:[1,0,0]\n"
    "v_pk_fma_f32 v[242:243], v[242:243], 0.5, 0.5 op_sel_hi:[1,0,0]\n"
    "v_pk_fma_f32 v[244:245], v[244:245], 0.5, 0.5 op_sel_hi:[1,0,0]\n"
    "v_pk_fma_f32 v[246:247], v[246:247], 0.5, 0.5 op_sel_hi:[1,0,0]\n"
    "v_med3_f32 v248, v168, v248, |v40|\n"
    "v_med3_f32 v249, v169, v249, |v41|\n"
    "v_med3_f32 v250, v170, v250, |v42|\n"
    "v_med3_f32 v251, v171, v251, |v43|\n"
    "v_med3_f32 v252, v172, v252, |v44|\n"
    "v_med3_f32 v253, v173, v253, |v45|\n"
    "v_med3_f32 v254, v174, v254, |v46|\n"
    "v_med3_f32 v255, v175, v255, |v47|\n"
    "v_bfi_b32 v248, s47, v248, v40\n"
    "v_bfi_b32 v249, s47, v249, v41\n"
    "v_bfi_b32 v250, s47, v250, v42\n"
    "v_bfi_b32 v251, s47, v251, v43\n"
    "v_bfi_b32 v252, s47, v252, v44\n"
    "v_bfi_b32 v253, s47, v253, v45\n"
    "v_bfi_b32 v254, s47, v254, v46\n"
    "v_bfi_b32 v255, s47, v255, v47\n"
    "v_pk_mul_f32 v[240:241], v[240:241], v[200:201]\n"
    "v_pk_mul_f32 v[242:243], v[242:243], v[202:203]\n"
    "v_pk_mul_f32 v[244:245], v[244:245], v[204:205]\n"
    "v_pk_mul_f32 v[246:247], v[246:247], v[206:207]\n"
    "v_rcp_f32 v240, v240\n"
    "v_rcp_f32 v241, v241\n"
    "v_rcp_f32 v242, v242\n"
    "v_rcp_f32 v243, v243\n"
    "v_rcp_f32 v244, v244\n"
    "v_rcp_f32 v245, v245\n"
    "v_rcp_f32 v246, v246\n"
    "v_rcp_f32 v247, v247\n"
    "v_pk_mul_f32 v[248:249], v[248:249], v[240:241]\n"
    "v_pk_mul_f32 v[250:251], v[250:251], v[242:243]\n"
    "v_pk_mul_f32 v[252:253], v[252:253], v[244:245]\n"
    "v_pk_mul_f32 v[254:255], v[254:255], v[246:247]\n"
    "v_cvt_pk_bf16_f32 v240, v248, v249\n"
    "v_cvt_pk_bf16_f32 v241, v250, v251\n"
    "v_cvt_pk_bf16_f32 v242, v252, v253\n"
    "v_cvt_pk_bf16_f32 v243, v254, v255\n"
    "buffer_store_dwordx2 v[240:241], v25, s[20:23], 0 offen\n"
    "buffer_store_dwordx2 v[242:243], v25, s[20:23], 0 offen offset:2048\n"
    "s_endpgm\n"
    ".fill (LV0_%=+36864-.)/4, 4, 0xbf800000\n"
    "LV6_%=:\n"
    "ds_read_b128 a[192:195], v10 offset:62208\n"
    "v_mfma_f32_16x16x32_bf16 v[168:171], v[192:195], a[212:215], v[168:171]\n"
    "s_waitcnt lgkmcnt(2)\n"
    "ds_read_b128 a[196:199], v10 offset:62336\n"
    "v_mfma_f32_16x16x32_bf16 v[172:175], v[192:195], a[184:187], v[172:175]\n"
    "ds_read_b128 a[184:187], v10 offset:62464\n"
    "v_mfma_f32_16x16x32_bf16 v[96:99], a[138:141], a[236:239], v[96:99]\n"
    "ds_read_b128 a[200:203], v10 offset:62592\n"
    "v_mfma_f32_16x16x32_bf16 v[100:103], a[138:141], a[228:231], v[100:103]\n"
    "v_add_f32 v64, v64, v160\n"
    "v_add_f32 v65, v65, v161\n"
    "v_mfma_f32_16x16x32_bf16 v[104:107], a[138:141], a[204:207], v[104:107]\n"
    "v_add_f32 v66, v66, v162\n"
    "v_add_f32 v67, v67, v163\n"
    "v_mfma_f32_16x16x32_bf16 v[108:111], a[138:141], a[220:223], v[108:111]\n"
    "v_add_f32 v68, v68, v160\n"
    "v_add_f32 v69, v69, v161\n"
    "v_mfma_f32_16x16x32_bf16 v[112:115], a[138:141], a[176:179], v[112:115]\n"
    "ds_read_b128 a[176:179], v10 offset:62720\n"
    "v_add_f32 v70, v70, v162\n"
    "v_mfma_f32_16x16x32_bf16 v[116:119], a[138:141], a[232:235], v[116:119]\n"
    "v_add_f32 v71, v71, v163\n"
    "v_add_f32 v72, v72, v164\n"
    "v_mfma_f32_16x16x32_bf16 v[120:123], a[138:141], a[180:183], v[120:123]\n"
    "s_waitcnt lgkmcnt(5)\n"
    "v_add_f32 v73, v73, v165\n"
    "v_add_f32 v74, v74, v166\n"
    "v_mfma_f32_16x16x32_bf16 v[124:127], a[138:141], a[188:191], v[124:127]\n"
    "s_waitcnt lgkmcnt(4)\n"
    "v_add_f32 v75, v75, v167\n"
    "v_add_f32 v76, v76, v164\n"
    "v_mfma_f32_16x16x32_bf16 v[128:131], a[0:3], a[192:195], v[64:67]\n"
    "s_waitcnt lgkmcnt(3)\n"
    "ds_read_b128 a[180:183], v10 offset:62848\n"
    "v_add_f32 v77, v77, v165\n"
    "v_mfma_f32_16x16x32_bf16 v[132:135], a[0:3], a[196:199], v[68:71]\n"
    "s_waitcnt lgkmcnt(3)\n"
    "v_add_f32 v78, v78, v166\n"
    "v_add_f32 v79, v79, v167\n"
    "v_mfma_f32_16x16x32_bf16 v[136:139], a[0:3], a[184:187], v[72:75]\n"
    "s_waitcnt lgkmcnt(2)\n"
    "v_add_f32 v80, v80, v168\n"
    "v_add_f32 v81, v81, v169\n"
    "v_mfma_f32_16x16x32_bf16 v[140:143], a[0:3], a[200:203], v[76:79]\n"
    "v_add_f32 v82, v82, v170\n"
    "v_add_f32 v83, v83, v171\n"
    "v_mfma_f32_16x16x32_bf16 v[128:131], a[4:7], a[196:199], v[128:131]\n"
    "ds_read_b128 a[188:191], v10 offset:62976\n"
    "v_add_f32 v84, v84, v168\n"
    "v_mfma_f32_16x16x32_bf16 v[132:135], a[4:7], a[184:187], v[132:135]\n"
    "v_add_f32 v85, v85, v169\n"
    "v_add_f32 v86, v86, v170\n"
    "v_mfma_f32_16x16x32_bf16 v[136:139], a[4:7], a[200:203], v[136:139]\n"
    "s_waitcnt lgkmcnt(2)\n"
    "v_add_f32 v87, v87, v171\n"
    "v_add_f32 v88, v88, v172\n"
    "v_mfma_f32_16x16x32_bf16 v[140:143], a[4:7], a[176:179], v[140:143]\n"
    "v_add_f32 v89, v89, v173\n"
    "v_add_f32 v90, v90, v174\n"
    "v_mfma_f32_16x16x32_bf16 v[128:131], a[8:11], a[184:187], v[128:131]\n"
    "ds_read_b128 a[184:187], v10 offset:63104\n"
    "v_add_f32 v91, v91, v175\n"
    "v_mfma_f32_16x16x32_bf16 v[132:135], a[8:11], a[200:203], v[132:135]\n"
    "v_add_f32 v92, v92, v172\n"
    "v_add_f32 v93, v93, v173\n"
    "v_mfma_f32_16x16x32_bf16 v[136:139], a[8:11], a[176:179], v[136:139]\n"
    "s_waitcnt lgkmcnt(2)\n"
    "v_add_f32 v94, v94, v174\n"
    "v_add_f32 v95, v95, v175\n"
    "v_mfma_f32_16x16x32_bf16 v[140:143], a[8:11], a[180:183], v[140:143]\n"
    "v_add_f32 v112, v112, v80\n"
    "v_add_f32 v113, v113, v81\n"
    "v_mfma_f32_16x16x32_bf16 v[128:131], a[12:15], a[200:203], v[128:131]\n"
    "ds_read_b128 a[192:195], v11 offset:62208\n"
    "v_add_f32 v114, v114, v82\n"
    "v_mfma_f32_16x16x32_bf16 v[132:135], a[12:15], a[176:179], v[132:135]\n"
    "v_add_f32 v115, v115, v83\n"
    "v_add_f32 v116, v116, v84\n"
    "v_mfma_f32_16x16x32_bf16 v[136:139], a[12:15], a[180:183], v[136:139]\n"
    "s_waitcnt lgkmcnt(2)\n"
    "v_add_f32 v117, v117, v85\n"
    "v_add_f32 v118, v118, v86\n"
    "v_mfma_f32_16x16x32_bf16 v[140:143], a[12:15], a[188:191], v[140:143]\n"
    "v_add_f32 v119, v119, v87\n"
    "v_cvt_pk_bf16_f32 v200, v112, v113\n"
    "v_mfma_f32_16x16x32_bf16 v[128:131], a[16:19], a[176:179], v[128:131]\n"
    "ds_read_b128 a[176:179], v11 offset:62336\n"
    "v_cvt_pk_bf16_f32 v201, v114, v115\n"
    "v_mfma_f32_16x16x32_bf16 v[132:135], a[16:19], a[180:183], v[132:135]\n"
    "v_cvt_pk_bf16_f32 v202, v116, v117\n"
    "v_cvt_pk_bf16_f32 v203, v118, v119\n"
    "v_mfma_f32_16x16x32_bf16 v[136:139], a[16:19], a[188:191], v[136:139]\n"
    "s_waitcnt lgkmcnt(2)\n"
    "s_xor_b32 s36, s39, 131072\n"
    "v_min_u32 v200, -2, v200\n"
    "v_mfma_f32_16x16x32_bf16 v[140:143], a[16:19], a[184:187], v[140:143]\n"
    "v_min_u32 v201, -2, v201\n"
    "v_min_u32 v202, -2, v202\n"
    "v_mfma_f32_16x16x32_bf16 v[128:131], a[20:23], a[180:183], v[128:131]\n"
    "ds_read_b128 a[180:183], v11 offset:62464\n"
    "v_min_u32 v203, -2, v203\n"
    "v_mfma_f32_16x16x32_bf16 v[132:135], a[20:23], a[188:191], v[132:135]\n"
    "v_add_f32 v120, v120, v88\n"
    "v_add_f32 v121, v121, v89\n"
    "v_mfma_f32_16x16x32_bf16 v[136:139], a[20:23], a[184:187], v[136:139]\n"
    "s_waitcnt lgkmcnt(2)\n"
    "v_add_f32 v122, v122, v90\n"
    "v_add_f32 v123, v123, v91\n"
    "v_mfma_f32_16x16x32_bf16 v[140:143], a[20:23], a[192:195], v[140:143]\n"
    "v_add_f32 v124, v124, v92\n"
    "v_add_f32 v125, v125, v93\n"
    "v_mfma_f32_16x16x32_bf16 v[128:131], a[24:27], a[188:191], v[128:131]\n"
    "ds_read_b128 a[188:191], v11 offset:62592\n"
    "v_add_f32 v126, v126, v94\n"
    "v_mfma_f32_16x16x32_bf16 v[132:135], a[24:27], a[184:187], v[132:135]\n"
    "v_add_f32 v127, v127, v95\n"
    "v_cvt_pk_bf16_f32 v204, v120, v121\n"
    "v_mfma_f32_16x16x32_bf16 v[136:139], a[24:27], a[192:195], v[136:139]\n"
    "s_waitcnt lgkmcnt(2)\n"
    "v_cvt_pk_bf16_f32 v205, v122, v123\n"
    "v_cvt_pk_bf16_f32 v206, v124, v125\n"
    "v_mfma_f32_16x16x32_bf16 v[140:143], a[24:27], a[176:179], v[140:143]\n"
    "v_cvt_pk_bf16_f32 v207, v126, v127\n"
    "v_mfma_f32_16x16x32_bf16 v[128:131], a[28:31], a[184:187], v[128:131]\n"
    "s_xor_b32 s36, s39, 163840\n"
    "ds_read_b128 a[184:187], v11 offset:62720\n"
    "v_min_u32 v204, -2, v204\n"
    "v_mfma_f32_16x16x32_bf16 v[132:135], a[28:31], a[192:195], v[132:135]\n"
    "v_min_u32 v205, -2, v205\n"
    "v_min_u32 v206, -2, v206\n"
    "v_mfma_f32_16x16x32_bf16 v[136:139], a[28:31], a[176:179], v[136:139]\n"
    "s_waitcnt lgkmcnt(2)\n"
    "v_min_u32 v207, -2, v207\n"
    "v_mfma_f32_16x16x32_bf16 v[140:143], a[28:31], a[180:183], v[140:143]\n"
    "s_waitcnt vmcnt(0)\n"
    "v_cmp_ne_u64 vcc, v[28:29], v[26:27]\n"
    "s_cbranch_vccz LF6ok_%=\n"
    "s_mov_b32 s43, 0x100000\n"
    "LF6lp_%=:\n"
    "buffer_load_dwordx2 v[28:29], v23, s[16:19], s40 offen offset:0 sc1\n"
    "s_waitcnt vmcnt(0)\n"
    "s_sub_u32 s43, s43, 1\n"
    "s_cbranch_scc1 LF6ok_%=\n"
    "v_cmp_ne_u64 vcc, v[28:29], v[26:27]\n"
    "s_cbranch_vccnz LF6lp_%=\n"
    "LF6ok_%=:\n"
    "v_mfma_f32_16x16x32_bf16 v[128:131], a[32:35], a[192:195], v[128:131]\n"
    "s_xor_b32 s36, s39, 131072\n"
    "ds_read_b128 a[192:195], v11 offset:62848\n"
    "v_mfma_f32_16x16x32_bf16 v[132:135], a[32:35], a[176:179], v[132:135]\n"
    "buffer_store_dwordx4 v[200:203], v22, s[16:19], s36 offen sc1\n"
    "v_mfma_f32_16x16x32_bf16 v[136:139], a[32:35], a[180:183], v[136:139]\n"
    "s_waitcnt lgkmcnt(2)\n"
    "s_xor_b32 s36, s39, 163840\n"
    "v_mfma_f32_16x16x32_bf16 v[140:143], a[32:35], a[188:191], v[140:143]\n"
    "v_mfma_f32_16x16x32_bf16 v[128:131], a[36:39], a[176:179], v[128:131]\n"
    "ds_read_b128 a[176:179], v11 offset:62976\n"
    "v_mfma_f32_16x16x32_bf16 v[132:135], a[36:39], a[180:183], v[132:135]\n"
    "buffer_store_dwordx4 v[204:207], v22, s[16:19], s36 offen sc1\n"
    "v_mfma_f32_16x16x32_bf16 v[136:139], a[36:39], a[188:191], v[136:139]\n"
    "s_waitcnt lgkmcnt(2)\n"
    "v_add_f32 v96, v96, v64\n"
    "v_add_f32 v97, v97, v65\n"
    "v_mfma_f32_16x16x32_bf16 v[140:143], a[36:39], a[184:187], v[140:143]\n"
    "v_add_f32 v98, v98, v66\n"
    "v_add_f32 v99, v99, v67\n"
    "v_mfma_f32_16x16x32_bf16 v[128:131], a[40:43], a[180:183], v[128:131]\n"
    "ds_read_b128 a[180:183], v11 offset:63104\n"
    "v_add_f32 v100, v100, v68\n"
    "v_mfma_f32_16x16x32_bf16 v[132:135], a[40:43], a[188:191], v[132:135]\n"
    "v_add_f32 v101, v101, v69\n"
    "v_add_f32 v102, v102, v70\n"
    "v_mfma_f32_16x16x32_bf16 v[136:139], a[40:43], a[184:187], v[136:139]\n"
    "s_waitcnt lgkmcnt(2)\n"
    "v_add_f32 v103, v103, v71\n"
    "v_cvt_pk_bf16_f32 v208, v96, v97\n"
    "v_mfma_f32_16x16x32_bf16 v[140:143], a[40:43], a[192:195], v[140:143]\n"
    "v_cvt_pk_bf16_f32 v209, v98, v99\n"
    "v_cvt_pk_bf16_f32 v210, v100, v101\n"
    "v_mfma_f32_16x16x32_bf16 v[128:131], a[44:47], a[188:191], v[128:131]\n"
    "ds_read_b128 a[188:191], v12 offset:62208\n"
    "v_cvt_pk_bf16_f32 v211, v102, v103\n"
    "v_mfma_f32_16x16x32_bf16 v[132:135], a[44:47], a[184:187], v[132:135]\n"
    "s_xor_b32 s36, s39, 196608\n"
    "v_min_u32 v208, -2, v208\n"
    "v_min_u32 v209, -2, v209\n"
    "v_mfma_f32_16x16x32_bf16 v[136:139], a[44:47], a[192:195], v[136:139]\n"
    "s_waitcnt lgkmcnt(2)\n"
    "v_min_u32 v210, -2, v210\n"
    "v_min_u32 v211, -2, v211\n"
    "v_mfma_f32_16x16x32_bf16 v[140:143], a[44:47], a[176:179], v[140:143]\n"
    "buffer_store_dwordx4 v[208:211], v22, s[16:19], s36 offen sc1\n"
    "v_mfma_f32_16x16x32_bf16 v[128:131], a[48:51], a[184:187], v[128:131]\n"
    "ds_read_b128 a[184:187], v12 offset:62336\n"
    "v_add_f32 v104, v104, v72\n"
    "v_mfma_f32_16x16x32_bf16 v[132:135], a[48:51], a[192:195], v[132:135]\n"
    "v_add_f32 v105, v105, v73\n"
    "v_add_f32 v106, v106, v74\n"
    "v_mfma_f32_16x16x32_bf16 v[136:139], a[48:51], a[176:179], v[136:139]\n"
    "s_waitcnt lgkmcnt(2)\n"
    "v_add_f32 v107, v107, v75\n"
    "v_add_f32 v108, v108, v76\n"
    "v_mfma_f32_16x16x32_bf16 v[140:143], a[48:51], a[180:183], v[140:143]\n"
    "v_add_f32 v109, v109, v77\n"
    "v_add_f32 v110, v110, v78\n"
    "v_mfma_f32_16x16x32_bf16 v[128:131], a[52:55], a[192:195], v[128:131]\n"
    "ds_read_b128 a[192:195], v12 offset:62464\n"
    "v_add_f32 v111, v111, v79\n"
    "v_mfma_f32_16x16x32_bf16 v[132:135], a[52:55], a[176:179], v[132:135]\n"
    "ds_read_b128 a[196:199], v13 offset:62208\n"
    "v_cvt_pk_bf16_f32 v212, v104, v105\n"
    "v_mfma_f32_16x16x32_bf16 v[136:139], a[52:55], a[180:183], v[136:139]\n"
    "s_waitcnt lgkmcnt(3)\n"
    "ds_read_b128 a[200:203], v13 offset:62336\n"
    "v_cvt_pk_bf16_f32 v213, v106, v107\n"
    "v_mfma_f32_16x16x32_bf16 v[140:143], a[52:55], a[188:191], v[140:143]\n"
    "ds_read_b128 a[204:207], v13 offset:62464\n"
    "v_cvt_pk_bf16_f32 v214, v108, v109\n"
    "v_mfma_f32_16x16x32_bf16 v[128:131], a[56:59], a[176:179], v[128:131]\n"
    "ds_read_b128 a[176:179], v13 offset:62592\n"
    "v_cvt_pk_bf16_f32 v215, v110, v111\n"
    "v_mfma_f32_16x16x32_bf16 v[132:135], a[56:59], a[180:183], v[132:135]\n"
    "s_xor_b32 s36, s39, 229376\n"
    "v_min_u32 v212, -2, v212\n"
    "v_min_u32 v213, -2, v213\n"
    "v_mfma_f32_16x16x32_bf16 v[136:139], a[56:59], a[188:191], v[136:139]\n"
    "s_waitcnt lgkmcnt(5)\n"
    "v_min_u32 v214, -2, v214\n"
    "v_min_u32 v215, -2, v215\n"
    "v_mfma_f32_16x16x32_bf16 v[140:143], a[56:59], a[184:187], v[140:143]\n"
    "buffer_store_dwordx4 v[212:215], v22, s[16:19], s36 offen sc1\n"
    "v_mfma_f32_16x16x32_bf16 v[128:131], a[60:63], a[180:183], v[128:131]\n"
    "ds_read_b128 a[180:183], v13 offset:62720\n"
    "v_mfma_f32_16x16x32_bf16 v[132:135], a[60:63], a[188:191], v[132:135]\n"
    "v_mfma_f32_16x16x32_bf16 v[136:139], a[60:63], a[184:187], v[136:139]\n"
    "s_waitcnt lgkmcnt(5)\n"
    "v_mfma_f32_16x16x32_bf16 v[140:143], a[60:63], a[192:195], v[140:143]\n"
    "s_waitcnt lgkmcnt(4)\n"
    "v_mfma_f32_16x16x32_bf16 v[128:131], a[64:67], a[196:199], v[128:131]\n"
    "s_waitcnt lgkmcnt(3)\n"
    "ds_read_b128 a[184:187], v13 offset:62848\n"
    "v_mfma_f32_16x16x32_bf16 v[132:135], a[64:67], a[200:203], v[132:135]\n"
    "s_waitcnt lgkmcnt(3)\n"
    "v_mfma_f32_16x16x32_bf16 v[136:139], a[64:67], a[204:207], v[136:139]\n"
    "s_waitcnt lgkmcnt(2)\n"
    "v_mfma_f32_16x16x32_bf16 v[140:143], a[64:67], a[176:179], v[140:143]\n"
    "v_mfma_f32_16x16x32_bf16 v[128:131], a[68:71], a[200:203], v[128:131]\n"
    "ds_read_b128 a[188:191], v13 offset:62976\n"
    "v_mfma_f32_16x16x32_bf16 v[132:135], a[68:71], a[204:207], v[132:135]\n"
    "v_mfma_f32_16x16x32_bf16 v[136:139], a[68:71], a[176:179], v[136:139]\n"
    "s_waitcnt lgkmcnt(2)\n"
    "v_mfma_f32_16x16x32_bf16 v[140:143], a[68:71], a[180:183], v[140:143]\n"
    "v_mfma_f32_16x16x32_bf16 v[128:131], a[72:75], a[204:207], v[128:131]\n"
    "ds_read_b128 a[192:195], v13 offset:63104\n"
    "v_mfma_f32_16x16x32_bf16 v[132:135], a[72:75], a[176:179], v[132:135]\n"
    "v_mfma_f32_16x16x32_bf16 v[136:139], a[72:75], a[180:183], v[136:139]\n"
    "s_waitcnt lgkmcnt(2)\n"
    "v_mfma_f32_16x16x32_bf16 v[140:143], a[72:75], a[184:187], v[140:143]\n"
    "v_mfma_f32_16x16x32_bf16 v[128:131], a[76:79], a[176:179], v[128:131]\n"
    "ds_read_b128 a[176:179], v14 offset:62208\n"
    "v_mfma_f32_16x16x32_bf16 v[132:135], a[76:79], a[180:183], v[132:135]\n"
    "v_mfma_f32_16x16x32_bf16 v[136:139], a[76:79], a[184:187], v[136:139]\n"
    "s_waitcnt lgkmcnt(2)\n"
    "v_mfma_f32_16x16x32_bf16 v[140:143], a[76:79], a[188:191], v[140:143]\n"
    "v_mfma_f32_16x16x32_bf16 v[128:131], a[80:83], a[180:183], v[128:131]\n"
    "ds_read_b128 a[180:183], v14 offset:62336\n"
    "v_mfma_f32_16x16x32_bf16 v[132:135], a[80:83], a[184:187], v[132:135]\n"
    "v_mfma_f32_16x16x32_bf16 v[136:139], a[80:83], a[188:191], v[136:139]\n"
    "s_waitcnt lgkmcnt(2)\n"
    "v_mfma_f32_16x16x32_bf16 v[140:143], a[80:83], a[192:195], v[140:143]\n"
    "v_mfma_f32_16x16x32_bf16 v[128:131], a[84:87], a[184:187], v[128:131]\n"
    "ds_read_b128 a[184:187], v14 offset:62464\n"
    "v_mfma_f32_16x16x32_bf16 v[132:135], a[84:87], a[188:191], v[132:135]\n"
    "v_mfma_f32_16x16x32_bf16 v[136:139], a[84:87], a[192:195], v[136:139]\n"
    "s_waitcnt lgkmcnt(2)\n"
    "v_mfma_f32_16x16x32_bf16 v[140:143], a[84:87], a[176:179], v[140:143]\n"
    "v_mfma_f32_16x16x32_bf16 v[128:131], a[88:91], a[188:191], v[128:131]\n"
    "ds_read_b128 a[188:191], v14 offset:62592\n"
    "v_mfma_f32_16x16x32_bf16 v[132:135], a[88:91], a[192:195], v[132:135]\n"
    "v_mfma_f32_16x16x32_bf16 v[136:139], a[88:91], a[176:179], v[136:139]\n"
    "s_waitcnt lgkmcnt(2)\n"
    "v_mfma_f32_16x16x32_bf16 v[140:143], a[88:91], a[180:183], v[140:143]\n"
    "v_mfma_f32_16x16x32_bf16 v[128:131], a[92:95], a[192:195], v[128:131]\n"
    "ds_read_b128 a[192:195], v14 offset:62720\n"
    "v_mfma_f32_16x16x32_bf16 v[132:135], a[92:95], a[176:179], v[132:135]\n"
    "v_mfma_f32_16x16x32_bf16 v[136:139], a[92:95], a[180:183], v[136:139]\n"
    "s_waitcnt lgkmcnt(2)\n"
    "v_mfma_f32_16x16x32_bf16 v[140:143], a[92:95], a[184:187], v[140:143]\n"
    "v_mfma_f32_16x16x32_bf16 v[128:131], a[96:99], a[176:179], v[128:131]\n"
    "ds_read_b128 a[176:179], v14 offset:62848\n"
    "v_mfma_f32_16x16x32_bf16 v[132:135], a[96:99], a[180:183], v[132:135]\n"
    "v_mfma_f32_16x16x32_bf16 v[136:139], a[96:99], a[184:187], v[136:139]\n"
    "s_waitcnt lgkmcnt(2)\n"
    "v_mfma_f32_16x16x32_bf16 v[140:143], a[96:99], a[188:191], v[140:143]\n"
    "v_mfma_f32_16x16x32_bf16 v[128:131], a[100:103], a[180:183], v[128:131]\n"
    "ds_read_b128 a[180:183], v14 offset:62976\n"
    "v_mfma_f32_16x16x32_bf16 v[132:135], a[100:103], a[184:187], v[132:135]\n"
    "v_mfma_f32_16x16x32_bf16 v[136:139], a[100:103], a[188:191], v[136:139]\n"
    "s_waitcnt lgkmcnt(2)\n"
    "v_mfma_f32_16x16x32_bf16 v[140:143], a[100:103], a[192:195], v[140:143]\n"
    "v_mfma_f32_16x16x32_bf16 v[128:131], a[104:107], a[184:187], v[128:131]\n"
    "ds_read_b128 a[184:187], v14 offset:63104\n"
    "v_mfma_f32_16x16x32_bf16 v[132:135], a[104:107], a[188:191], v[132:135]\n"
    "v_mfma_f32_16x16x32_bf16 v[136:139], a[104:107], a[192:195], v[136:139]\n"
    "s_waitcnt lgkmcnt(2)\n"
    "v_mfma_f32_16x16x32_bf16 v[140:143], a[104:107], a[176:179], v[140:143]\n"
    "v_mfma_f32_16x16x32_bf16 v[128:131], a[108:111], a[188:191], v[128:131]\n"
    "ds_read_b128 a[188:191], v15 offset:62208\n"
    "v_mfma_f32_16x16x32_bf16 v[132:135], a[108:111], a[192:195], v[132:135]\n"
    "v_mfma_f32_16x16x32_bf16 v[136:139], a[108:111], a[176:179], v[136:139]\n"
    "s_waitcnt lgkmcnt(2)\n"
    "v_mfma_f32_16x16x32_bf16 v[140:143], a[108:111], a[180:183], v[140:143]\n"
    "v_mfma_f32_16x16x32_bf16 v[128:131], a[112:115], a[192:195], v[128:131]\n"
    "ds_read_b128 a[192:195], v15 offset:62336\n"
    "v_mfma_f32_16x16x32_bf16 v[132:135], a[112:115], a[176:179], v[132:135]\n"
    "v_mfma_f32_16x16x32_bf16 v[136:139], a[112:115], a[180:183], v[136:139]\n"
    "s_waitcnt lgkmcnt(2)\n"
    "buffer_load_dwordx4 v[160:163], v22, s[16:19], s28 offen sc1\n"
    "v_mfma_f32_16x16x32_bf16 v[140:143], a[112:115], a[184:187], v[140:143]\n"
    "v_mfma_f32_16x16x32_bf16 v[128:131], a[116:119], a[176:179], v[128:131]\n"
    "ds_read_b128 a[176:179], v15 offset:62464\n"
    "v_mfma_f32_16x16x32_bf16 v[132:135], a[116:119], a[180:183], v[132:135]\n"
    "ds_read_b128 a[196:199], v10 offset:62976\n"
    "v_mfma_f32_16x16x32_bf16 v[136:139], a[116:119], a[184:187], v[136:139]\n"
    "s_waitcnt lgkmcnt(3)\n"
    "ds_read_b128 a[200:203], v10 offset:63104\n"
    "v_mfma_f32_16x16x32_bf16 v[140:143], a[116:119], a[188:191], v[140:143]\n"
    "buffer_load_dwordx4 v[164:167], v22, s[16:19], s30 offen sc1\n"
    "v_mfma_f32_16x16x32_bf16 v[128:131], a[120:123], a[180:183], v[128:131]\n"
    "ds_read_b128 a[180:183], v11 offset:62208\n"
    "v_mfma_f32_16x16x32_bf16 v[132:135], a[120:123], a[184:187], v[132:135]\n"
    "v_mfma_f32_16x16x32_bf16 v[136:139], a[120:123], a[188:191], v[136:139]\n"
    "s_waitcnt lgkmcnt(4)\n"
    "ds_read_b128 a[204:207], v11 offset:62336\n"
    "v_mfma_f32_16x16x32_bf16 v[140:143], a[120:123], a[192:195], v[140:143]\n"
    "buffer_load_dwordx4 v[168:171], v22, s[16:19], s29 offen sc1\n"
    "v_mfma_f32_16x16x32_bf16 v[128:131], a[124:127], a[184:187], v[128:131]\n"
    "ds_read_b128 a[184:187], v11 offset:62464\n"
    "v_mfma_f32_16x16x32_bf16 v[132:135], a[124:127], a[188:191], v[132:135]\n"
    "v_mfma_f32_16x16x32_bf16 v[136:139], a[124:127], a[192:195], v[136:139]\n"
    "s_waitcnt lgkmcnt(5)\n"
    "ds_read_b128 a[188:191], v11 offset:62592\n"
    "v_mfma_f32_16x16x32_bf16 v[140:143], a[124:127], a[176:179], v[140:143]\n"
    "s_waitcnt lgkmcnt(5)\n"
    "buffer_load_dwordx4 v[172:175], v22, s[16:19], s31 offen sc1\n"
    "v_mfma_f32_16x16x32_bf16 v[152:155], a[0:3], a[196:199], v[88:91]\n"
    "s_waitcnt lgkmcnt(4)\n"
    "ds_read_b128 a[176:179], v11 offset:62720\n"
    "v_mfma_f32_16x16x32_bf16 v[156:159], a[0:3], a[200:203], v[92:95]\n"
    "v_mfma_f32_16x16x32_bf16 v[152:155], a[4:7], a[200:203], v[152:155]\n"
    "s_waitcnt lgkmcnt(4)\n"
    "ds_read_b128 a[192:195], v11 offset:62848\n"
    "v_mfma_f32_16x16x32_bf16 v[156:159], a[4:7], a[180:183], v[156:159]\n"
    "v_cvt_pk_bf16_f32 v216, v128, v129\n"
    "v_cvt_pk_bf16_f32 v217, v130, v131\n"
    "v_mfma_f32_16x16x32_bf16 v[152:155], a[8:11], a[180:183], v[152:155]\n"
    "s_waitcnt lgkmcnt(4)\n"
    "ds_read_b128 a[180:183], v11 offset:62976\n"
    "v_cvt_pk_bf16_f32 v218, v132, v133\n"
    "v_mfma_f32_16x16x32_bf16 v[156:159], a[8:11], a[204:207], v[156:159]\n"
    "v_cvt_pk_bf16_f32 v219, v134, v135\n"
    "v_mfma_f32_16x16x32_bf16 v[152:155], a[12:15], a[204:207], v[152:155]\n"
    "s_waitcnt lgkmcnt(4)\n"
    "s_xor_b32 s24, s39, 65536\n"
    "ds_read_b128 a[196:199], v11 offset:63104\n"
    "v_mfma_f32_16x16x32_bf16 v[156:159], a[12:15], a[184:187], v[156:159]\n"
    "v_min_u32 v216, -2, v216\n"
    "v_min_u32 v217, -2, v217\n"
    "v_mfma_f32_16x16x32_bf16 v[152:155], a[16:19], a[184:187], v[152:155]\n"
    "s_waitcnt lgkmcnt(4)\n"
    "ds_read_b128 a[184:187], v12 offset:62208\n"
    "v_min_u32 v218, -2, v218\n"
    "v_mfma_f32_16x16x32_bf16 v[156:159], a[16:19], a[188:191], v[156:159]\n"
    "v_min_u32 v219, -2, v219\n"
    "v_mfma_f32_16x16x32_bf16 v[152:155], a[20:23], a[188:191], v[152:155]\n"
    "s_waitcnt lgkmcnt(4)\n"
    "ds_read_b128 a[188:191], v12 offset:62336\n"
    "v_mfma_f32_16x16x32_bf16 v[156:159], a[20:23], a[176:179], v[156:159]\n"
    "buffer_store_dwordx4 v[216:219], v22, s[16:19], s24 offen sc1\n"
    "v_mfma_f32_16x16x32_bf16 v[152:155], a[24:27], a[176:179], v[152:155]\n"
    "s_waitcnt lgkmcnt(4)\n"
    "ds_read_b128 a[176:179], v12 offset:62464\n"
    "v_cvt_pk_bf16_f32 v220, v136, v137\n"
    "v_mfma_f32_16x16x32_bf16 v[156:159], a[24:27], a[192:195], v[156:159]\n"
    "v_cvt_pk_bf16_f32 v221, v138, v139\n"
    "v_cvt_pk_bf16_f32 v222, v140, v141\n"
    "v_mfma_f32_16x16x32_bf16 v[152:155], a[28:31], a[192:195], v[152:155]\n"
    "s_waitcnt lgkmcnt(4)\n"
    "ds_read_b128 a[192:195], v12 offset:62592\n"
    "v_cvt_pk_bf16_f32 v223, v142, v143\n"
    "v_mfma_f32_16x16x32_bf16 v[156:159], a[28:31], a[180:183], v[156:159]\n"
    "s_xor_b32 s24, s39, 98304\n"
    "v_min_u32 v220, -2, v220\n"
    "v_min_u32 v221, -2, v221\n"
    "v_mfma_f32_16x16x32_bf16 v[152:155], a[32:35], a[180:183], v[152:155]\n"
    "s_waitcnt lgkmcnt(4)\n"
    "ds_read_b128 a[180:183], v12 offset:62720\n"
    "v_min_u32 v222, -2, v222\n"
    "v_mfma_f32_16x16x32_bf16 v[156:159], a[32:35], a[196:199], v[156:159]\n"
    "v_min_u32 v223, -2, v223\n"
    "v_mfma_f32_16x16x32_bf16 v[152:155], a[36:39], a[196:199], v[152:155]\n"
    "s_waitcnt lgkmcnt(4)\n"
    "ds_read_b128 a[196:199], v12 offset:62848\n"
    "v_mfma_f32_16x16x32_bf16 v[156:159], a[36:39], a[184:187], v[156:159]\n"
    "buffer_store_dwordx4 v[220:223], v22, s[16:19], s24 offen sc1\n"
    "v_mfma_f32_16x16x32_bf16 v[152:155], a[40:43], a[184:187], v[152:155]\n"
    "s_waitcnt lgkmcnt(4)\n"
    "ds_read_b128 a[184:187], v12 offset:62976\n"
    "v_mfma_f32_16x16x32_bf16 v[156:159], a[40:43], a[188:191], v[156:159]\n"
    "ds_read_b128 a[200:203], v13 offset:62976\n"
    "v_mfma_f32_16x16x32_bf16 v[152:155], a[44:47], a[188:191], v[152:155]\n"
    "s_waitcnt lgkmcnt(5)\n"
    "ds_read_b128 a[188:191], v13 offset:63104\n"
    "v_mfma_f32_16x16x32_bf16 v[156:159], a[44:47], a[176:179], v[156:159]\n"
    "v_mfma_f32_16x16x32_bf16 v[152:155], a[48:51], a[176:179], v[152:155]\n"
    "s_waitcnt lgkmcnt(5)\n"
    "ds_read_b128 a[176:179], v14 offset:62208\n"
    "v_mfma_f32_16x16x32_bf16 v[156:159], a[48:51], a[192:195], v[156:159]\n"
    "v_mfma_f32_16x16x32_bf16 v[152:155], a[52:55], a[192:195], v[152:155]\n"
    "s_waitcnt lgkmcnt(5)\n"
    "ds_read_b128 a[192:195], v14 offset:62336\n"
    "v_mfma_f32_16x16x32_bf16 v[156:159], a[52:55], a[180:183], v[156:159]\n"
    "v_mfma_f32_16x16x32_bf16 v[152:155], a[56:59], a[180:183], v[152:155]\n"
    "s_waitcnt lgkmcnt(5)\n"
    "ds_read_b128 a[180:183], v14 offset:62464\n"
    "v_mfma_f32_16x16x32_bf16 v[156:159], a[56:59], a[196:199], v[156:159]\n"
    "v_mfma_f32_16x16x32_bf16 v[152:155], a[60:63], a[196:199], v[152:155]\n"
    "s_waitcnt lgkmcnt(5)\n"
    "ds_read_b128 a[196:199], v14 offset:62592\n"
    "v_mfma_f32_16x16x32_bf16 v[156:159], a[60:63], a[184:187], v[156:159]\n"
    "s_waitcnt lgkmcnt(5)\n"
    "v_mfma_f32_16x16x32_bf16 v[152:155], a[64:67], a[200:203], v[152:155]\n"
    "s_waitcnt lgkmcnt(4)\n"
    "ds_read_b128 a[184:187], v14 offset:62720\n"
    "v_mfma_f32_16x16x32_bf16 v[156:159], a[64:67], a[188:191], v[156:159]\n"
    "v_mfma_f32_16x16x32_bf16 v[152:155], a[68:71], a[188:191], v[152:155]\n"
    "s_waitcnt lgkmcnt(4)\n"
    "ds_read_b128 a[188:191], v14 offset:62848\n"
    "v_mfma_f32_16x16x32_bf16 v[156:159], a[68:71], a[176:179], v[156:159]\n"
    "v_mfma_f32_16x16x32_bf16 v[152:155], a[72:75], a[176:179], v[152:155]\n"
    "s_waitcnt lgkmcnt(4)\n"
    "ds_read_b128 a[176:179], v14 offset:62976\n"
    "v_mfma_f32_16x16x32_bf16 v[156:159], a[72:75], a[192:195], v[156:159]\n"
    "v_mfma_f32_16x16x32_bf16 v[152:155], a[76:79], a[192:195], v[152:155]\n"
    "s_waitcnt lgkmcnt(4)\n"
    "ds_read_b128 a[192:195], v14 offset:63104\n"
    "v_mfma_f32_16x16x32_bf16 v[156:159], a[76:79], a[180:183], v[156:159]\n"
    "v_mfma_f32_16x16x32_bf16 v[152:155], a[80:83], a[180:183], v[152:155]\n"
    "s_waitcnt lgkmcnt(4)\n"
    "ds_read_b128 a[180:183], v15 offset:62208\n"
    "v_mfma_f32_16x16x32_bf16 v[156:159], a[80:83], a[196:199], v[156:159]\n"
    "v_mfma_f32_16x16x32_bf16 v[152:155], a[84:87], a[196:199], v[152:155]\n"
    "s_waitcnt lgkmcnt(4)\n"
    "ds_read_b128 a[196:199], v15 offset:62336\n"
    "v_mfma_f32_16x16x32_bf16 v[156:159], a[84:87], a[184:187], v[156:159]\n"
    "v_mfma_f32_16x16x32_bf16 v[152:155], a[88:91], a[184:187], v[152:155]\n"
    "s_waitcnt lgkmcnt(4)\n"
    "ds_read_b128 a[184:187], v15 offset:62464\n"
    "v_mfma_f32_16x16x32_bf16 v[156:159], a[88:91], a[188:191], v[156:159]\n"
    "v_mfma_f32_16x16x32_bf16 v[152:155], a[92:95], a[188:191], v[152:155]\n"
    "s_waitcnt lgkmcnt(4)\n"
    "ds_read_b128 a[188:191], v15 offset:62592\n"
    "v_mfma_f32_16x16x32_bf16 v[156:159], a[92:95], a[176:179], v[156:159]\n"
    "v_mfma_f32_16x16x32_bf16 v[152:155], a[96:99], a[176:179], v[152:155]\n"
    "s_waitcnt lgkmcnt(4)\n"
    "ds_read_b128 a[176:179], v15 offset:62720\n"
    "v_mfma_f32_16x16x32_bf16 v[156:159], a[96:99], a[192:195], v[156:159]\n"
    "v_mfma_f32_16x16x32_bf16 v[152:155], a[100:103], a[192:195], v[152:155]\n"
    "s_waitcnt lgkmcnt(4)\n"
    "ds_read_b128 a[192:195], v15 offset:62848\n"
    "v_mfma_f32_16x16x32_bf16 v[156:159], a[100:103], a[180:183], v[156:159]\n"
    "v_mfma_f32_16x16x32_bf16 v[152:155], a[104:107], a[180:183], v[152:155]\n"
    "s_waitcnt lgkmcnt(4)\n"
    "ds_read_b128 a[180:183], v15 offset:62976\n"
    "v_mfma_f32_16x16x32_bf16 v[156:159], a[104:107], a[196:199], v[156:159]\n"
    "ds_read_b128 a[200:203], v10 offset:62720\n"
    "v_mfma_f32_16x16x32_bf16 v[152:155], a[108:111], a[196:199], v[152:155]\n"
    "s_waitcnt lgkmcnt(5)\n"
    "ds_read_b128 a[196:199], v10 offset:62848\n"
    "v_mfma_f32_16x16x32_bf16 v[156:159], a[108:111], a[184:187], v[156:159]\n"
    "v_mfma_f32_16x16x32_bf16 v[152:155], a[112:115], a[184:187], v[152:155]\n"
    "s_waitcnt lgkmcnt(5)\n"
    "ds_read_b128 a[184:187], v10 offset:62976\n"
    "v_mfma_f32_16x16x32_bf16 v[156:159], a[112:115], a[188:191], v[156:159]\n"
    "v_mfma_f32_16x16x32_bf16 v[152:155], a[116:119], a[188:191], v[152:155]\n"
    "s_waitcnt lgkmcnt(5)\n"
    "ds_read_b128 a[188:191], v10 offset:63104\n"
    "v_mfma_f32_16x16x32_bf16 v[156:159], a[116:119], a[176:179], v[156:159]\n"
    "v_mfma_f32_16x16x32_bf16 v[152:155], a[120:123], a[176:179], v[152:155]\n"
    "s_waitcnt lgkmcnt(5)\n"
    "ds_read_b128 a[176:179], v11 offset:62208\n"
    "v_mfma_f32_16x16x32_bf16 v[156:159], a[120:123], a[192:195], v[156:159]\n"
    "v_mfma_f32_16x16x32_bf16 v[152:155], a[124:127], a[192:195], v[152:155]\n"
    "s_waitcnt lgkmcnt(5)\n"
    "ds_read_b128 a[192:195], v11 offset:62336\n"
    "v_mfma_f32_16x16x32_bf16 v[156:159], a[124:127], a[180:183], v[156:159]\n"
    "s_waitcnt lgkmcnt(5)\n"
    "v_mfma_f32_16x16x32_bf16 v[144:147], a[0:3], a[200:203], v[80:83]\n"
    "s_waitcnt lgkmcnt(4)\n"
    "ds_read_b128 a[180:183], v11 offset:62464\n"
    "v_mfma_f32_16x16x32_bf16 v[148:151], a[0:3], a[196:199], v[84:87]\n"
    "v_mfma_f32_16x16x32_bf16 v[144:147], a[4:7], a[196:199], v[144:147]\n"
    "s_waitcnt lgkmcnt(4)\n"
    "ds_read_b128 a[196:199], v11 offset:62592\n"
    "v_mfma_f32_16x16x32_bf16 v[148:151], a[4:7], a[184:187], v[148:151]\n"
    "v_cvt_pk_bf16_f32 v224, v152, v153\n"
    "v_cvt_pk_bf16_f32 v225, v154, v155\n"
    "v_mfma_f32_16x16x32_bf16 v[144:147], a[8:11], a[184:187], v[144:147]\n"
    "s_waitcnt lgkmcnt(4)\n"
    "ds_read_b128 a[184:187], v11 offset:62720\n"
    "v_cvt_pk_bf16_f32 v226, v156, v157\n"
    "v_mfma_f32_16x16x32_bf16 v[148:151], a[8:11], a[188:191], v[148:151]\n"
    "v_cvt_pk_bf16_f32 v227, v158, v159\n"
    "v_mfma_f32_16x16x32_bf16 v[144:147], a[12:15], a[188:191], v[144:147]\n"
    "s_waitcnt lgkmcnt(4)\n"
    "s_xor_b32 s24, s39, 32768\n"
    "ds_read_b128 a[188:191], v11 offset:62848\n"
    "v_mfma_f32_16x16x32_bf16 v[148:151], a[12:15], a[176:179], v[148:151]\n"
    "v_min_u32 v224, -2, v224\n"
    "v_min_u32 v225, -2, v225\n"
    "v_mfma_f32_16x16x32_bf16 v[144:147], a[16:19], a[176:179], v[144:147]\n"
    "s_waitcnt lgkmcnt(4)\n"
    "ds_read_b128 a[176:179], v11 offset:62976\n"
    "v_min_u32 v226, -2, v226\n"
    "v_mfma_f32_16x16x32_bf16 v[148:151], a[16:19], a[192:195], v[148:151]\n"
    "v_min_u32 v227, -2, v227\n"
    "v_mfma_f32_16x16x32_bf16 v[144:147], a[20:23], a[192:195], v[144:147]\n"
    "s_waitcnt lgkmcnt(4)\n"
    "ds_read_b128 a[192:195], v11 offset:63104\n"
    "v_mfma_f32_16x16x32_bf16 v[148:151], a[20:23], a[180:183], v[148:151]\n"
    "buffer_store_dwordx4 v[224:227], v22, s[16:19], s24 offen sc1\n"
    "v_mfma_f32_16x16x32_bf16 v[144:147], a[24:27], a[180:183], v[144:147]\n"
    "s_waitcnt lgkmcnt(4)\n"
    "ds_read_b128 a[180:183], v12 offset:62208\n"
    "v_mfma_f32_16x16x32_bf16 v[148:151], a[24:27], a[196:199], v[148:151]\n"
    "v_mfma_f32_16x16x32_bf16 v[144:147], a[28:31], a[196:199], v[144:147]\n"
    "s_waitcnt lgkmcnt(4)\n"
    "ds_read_b128 a[196:199], v12 offset:62336\n"
    "v_mfma_f32_16x16x32_bf16 v[148:151], a[28:31], a[184:187], v[148:151]\n"
    "v_mfma_f32_16x16x32_bf16 v[144:147], a[32:35], a[184:187], v[144:147]\n"
    "s_waitcnt lgkmcnt(4)\n"
    "ds_read_b128 a[184:187], v12 offset:62464\n"
    "v_mfma_f32_16x16x32_bf16 v[148:151], a[32:35], a[188:191], v[148:151]\n"
    "v_mfma_f32_16x16x32_bf16 v[144:147], a[36:39], a[188:191], v[144:147]\n"
    "s_waitcnt lgkmcnt(4)\n"
    "ds_read_b128 a[188:191], v12 offset:62592\n"
    "v_mfma_f32_16x16x32_bf16 v[148:151], a[36:39], a[176:179], v[148:151]\n"
    "v_mfma_f32_16x16x32_bf16 v[144:147], a[40:43], a[176:179], v[144:147]\n"
    "s_waitcnt lgkmcnt(4)\n"
    "ds_read_b128 a[176:179], v12 offset:62720\n"
    "v_mfma_f32_16x16x32_bf16 v[148:151], a[40:43], a[192:195], v[148:151]\n"
    "ds_read_b128 a[200:203], v13 offset:62720\n"
    "v_mfma_f32_16x16x32_bf16 v[144:147], a[44:47], a[192:195], v[144:147]\n"
    "s_waitcnt lgkmcnt(5)\n"
    "ds_read_b128 a[192:195], v13 offset:62848\n"
    "v_mfma_f32_16x16x32_bf16 v[148:151], a[44:47], a[180:183], v[148:151]\n"
    "v_mfma_f32_16x16x32_bf16 v[144:147], a[48:51], a[180:183], v[144:147]\n"
    "s_waitcnt lgkmcnt(5)\n"
    "s_waitcnt vmcnt(3)\n"
    "ds_read_b128 a[180:183], v13 offset:62976\n"
    "v_mfma_f32_16x16x32_bf16 v[148:151], a[48:51], a[196:199], v[148:151]\n"
    "v_max3_u32 v240, v160, v161, v162\n"
    "v_max3_u32 v240, v240, v163, v164\n"
    "v_mfma_f32_16x16x32_bf16 v[144:147], a[52:55], a[196:199], v[144:147]\n"
    "s_waitcnt lgkmcnt(5)\n"
    "ds_read_b128 a[196:199], v13 offset:63104\n"
    "v_max3_u32 v240, v240, v165, v166\n"
    "v_mfma_f32_16x16x32_bf16 v[148:151], a[52:55], a[184:187], v[148:151]\n"
    "buffer_load_dwordx4 v[176:179], v22, s[16:19], s26 offen sc1\n"
    "v_mfma_f32_16x16x32_bf16 v[144:147], a[56:59], a[184:187], v[144:147]\n"
    "s_waitcnt lgkmcnt(5)\n"
    "ds_read_b128 a[184:187], v14 offset:62208\n"
    "v_max3_u32 v240, v240, v167, v168\n"
    "v_mfma_f32_16x16x32_bf16 v[148:151], a[56:59], a[188:191], v[148:151]\n"
    "v_max3_u32 v240, v240, v169, v170\n"
    "v_max3_u32 v240, v240, v171, v172\n"
    "v_mfma_f32_16x16x32_bf16 v[144:147], a[60:63], a[188:191], v[144:147]\n"
    "s_waitcnt lgkmcnt(5)\n"
    "ds_read_b128 a[188:191], v14 offset:62336\n"
    "v_max3_u32 v240, v240, v173, v174\n"
    "v_mfma_f32_16x16x32_bf16 v[148:151], a[60:63], a[176:179], v[148:151]\n"
    "s_waitcnt lgkmcnt(5)\n"
    "buffer_load_dwordx4 v[180:183], v22, s[16:19], s27 offen sc1\n"
    "v_mfma_f32_16x16x32_bf16 v[144:147], a[64:67], a[200:203], v[144:147]\n"
    "s_waitcnt lgkmcnt(4)\n"
    "ds_read_b128 a[176:179], v14 offset:62464\n"
    "v_max_u32 v240, v240, v175\n"
    "v_mfma_f32_16x16x32_bf16 v[148:151], a[64:67], a[192:195], v[148:151]\n"
    "v_cmp_eq_u32 vcc, -1, v240\n"
    "s_cbranch_vccz LE6ok_%=\n"
    "s_mov_b32 s43, 0x100000\n"
    "LE6lp_%=:\n"
    "buffer_load_dwordx4 v[160:163], v22, s[16:19], s28 offen sc1\n"
    "buffer_load_dwordx4 v[164:167], v22, s[16:19], s30 offen sc1\n"
    "buffer_load_dwordx4 v[168:171], v22, s[16:19], s29 offen sc1\n"
    "buffer_load_dwordx4 v[172:175], v22, s[16:19], s31 offen sc1\n"
    "s_waitcnt vmcnt(0)\n"
    "s_sub_u32 s43, s43, 1\n"
    "s_cbranch_scc1 LE6ok_%=\n"
    "v_max3_u32 v240, v160, v161, v162\n"
    "v_max3_u32 v240, v240, v163, v164\n"
    "v_max3_u32 v240, v240, v165, v166\n"
    "v_max3_u32 v240, v240, v167, v168\n"
    "v_max3_u32 v240, v240, v169, v170\n"
    "v_max3_u32 v240, v240, v171, v172\n"
    "v_max3_u32 v240, v240, v173, v174\n"
    "v_max_u32 v240, v240, v175\n"
    "v_cmp_eq_u32 vcc, -1, v240\n"
    "s_cbranch_vccnz LE6lp_%=\n"
    "LE6ok_%=:\n"
    "v_mfma_f32_16x16x32_bf16 v[144:147], a[68:71], a[192:195], v[144:147]\n"
    "s_waitcnt lgkmcnt(4)\n"
    "ds_read_b128 a[192:195], v14 offset:62592\n"
    "v_lshlrev_b32 v188, 16, v160\n"
    "v_mfma_f32_16x16x32_bf16 v[148:151], a[68:71], a[180:183], v[148:151]\n"
    "v_and_b32 v189, s48, v160\n"
    "v_lshlrev_b32 v190, 16, v161\n"
    "v_mfma_f32_16x16x32_bf16 v[144:147], a[72:75], a[180:183], v[144:147]\n"
    "s_waitcnt lgkmcnt(4)\n"
    "ds_read_b128 a[180:183], v14 offset:62720\n"
    "v_and_b32 v191, s48, v161\n"
    "v_mfma_f32_16x16x32_bf16 v[148:151], a[72:75], a[196:199], v[148:151]\n"
    "v_lshlrev_b32 v192, 16, v162\n"
    "v_and_b32 v193, s48, v162\n"
    "v_mfma_f32_16x16x32_bf16 v[144:147], a[76:79], a[196:199], v[144:147]\n"
    "s_waitcnt lgkmcnt(4)\n"
    "ds_read_b128 a[196:199], v14 offset:62848\n"
    "v_lshlrev_b32 v194, 16, v163\n"
    "v_mfma_f32_16x16x32_bf16 v[148:151], a[76:79], a[184:187], v[148:151]\n"
    "v_and_b32 v195, s48, v163\n"
    "v_lshlrev_b32 v240, 16, v164\n"
    "v_mfma_f32_16x16x32_bf16 v[144:147], a[80:83], a[184:187], v[144:147]\n"
    "s_waitcnt lgkmcnt(4)\n"
    "ds_read_b128 a[184:187], v14 offset:62976\n"
    "v_and_b32 v241, s48, v164\n"
    "v_mfma_f32_16x16x32_bf16 v[148:151], a[80:83], a[188:191], v[148:151]\n"
    "v_lshlrev_b32 v242, 16, v165\n"
    "v_and_b32 v243, s48, v165\n"
    "v_mfma_f32_16x16x32_bf16 v[144:147], a[84:87], a[188:191], v[144:147]\n"
    "s_waitcnt lgkmcnt(4)\n"
    "ds_read_b128 a[188:191], v14 offset:63104\n"
    "v_lshlrev_b32 v244, 16, v166\n"
    "v_mfma_f32_16x16x32_bf16 v[148:151], a[84:87], a[176:179], v[148:151]\n"
    "v_and_b32 v245, s48, v166\n"
    "v_lshlrev_b32 v246, 16, v167\n"
    "v_mfma_f32_16x16x32_bf16 v[144:147], a[88:91], a[176:179], v[144:147]\n"
    "s_waitcnt lgkmcnt(4)\n"
    "ds_read_b128 a[176:179], v15 offset:62208\n"
    "v_and_b32 v247, s48, v167\n"
    "v_mfma_f32_16x16x32_bf16 v[148:151], a[88:91], a[192:195], v[148:151]\n"
    "v_add_f32 v188, v188, v240\n"
    "v_add_f32 v189, v189, v241\n"
    "v_mfma_f32_16x16x32_bf16 v[144:147], a[92:95], a[192:195], v[144:147]\n"
    "s_waitcnt lgkmcnt(4)\n"
    "ds_read_b128 a[192:195], v15 offset:62336\n"
    "v_add_f32 v190, v190, v242\n"
    "v_mfma_f32_16x16x32_bf16 v[148:151], a[92:95], a[180:183], v[148:151]\n"
    "v_add_f32 v191, v191, v243\n"
    "v_add_f32 v192, v192, v244\n"
    "v_mfma_f32_16x16x32_bf16 v[144:147], a[96:99], a[180:183], v[144:147]\n"
    "s_waitcnt lgkmcnt(4)\n"
    "ds_read_b128 a[180:183], v15 offset:62464\n"
    "v_add_f32 v193, v193, v245\n"
    "v_mfma_f32_16x16x32_bf16 v[148:151], a[96:99], a[196:199], v[148:151]\n"
    "v_add_f32 v194, v194, v246\n"
    "v_add_f32 v195, v195, v247\n"
    "v_mfma_f32_16x16x32_bf16 v[144:147], a[100:103], a[196:199], v[144:147]\n"
    "s_waitcnt lgkmcnt(4)\n"
    "ds_read_b128 a[196:199], v15 offset:62592\n"
    "v_lshlrev_b32 v40, 16, v168\n"
    "v_mfma_f32_16x16x32_bf16 v[148:151], a[100:103], a[184:187], v[148:151]\n"
    "v_and_b32 v41, s48, v168\n"
    "v_lshlrev_b32 v42, 16, v169\n"
    "v_mfma_f32_16x16x32_bf16 v[144:147], a[104:107], a[184:187], v[144:147]\n"
    "s_waitcnt lgkmcnt(4)\n"
    "ds_read_b128 a[184:187], v15 offset:62720\n"
    "v_and_b32 v43, s48, v169\n"
    "v_mfma_f32_16x16x32_bf16 v[148:151], a[104:107], a[188:191], v[148:151]\n"
    "v_lshlrev_b32 v44, 16, v170\n"
    "v_and_b32 v45, s48, v170\n"
    "v_mfma_f32_16x16x32_bf16 v[144:147], a[108:111], a[188:191], v[144:147]\n"
    "s_waitcnt lgkmcnt(4)\n"
    "buffer_load_dwordx4 v[184:187], v22, s[16:19], s25 offen sc1\n"
    "v_mfma_f32_16x16x32_bf16 v[148:151], a[108:111], a[176:179], v[148:151]\n"
    "v_lshlrev_b32 v46, 16, v171\n"
    "v_and_b32 v47, s48, v171\n"
    "v_mfma_f32_16x16x32_bf16 v[144:147], a[112:115], a[176:179], v[144:147]\n"
    "s_waitcnt lgkmcnt(3)\n"
    "v_lshlrev_b32 v240, 16, v172\n"
    "v_and_b32 v241, s48, v172\n"
    "v_mfma_f32_16x16x32_bf16 v[148:151], a[112:115], a[192:195], v[148:151]\n"
    "v_lshlrev_b32 v242, 16, v173\n"
    "v_and_b32 v243, s48, v173\n"
    "v_mfma_f32_16x16x32_bf16 v[144:147], a[116:119], a[192:195], v[144:147]\n"
    "s_waitcnt lgkmcnt(2)\n"
    "v_lshlrev_b32 v244, 16, v174\n"
    "v_and_b32 v245, s48, v174\n"
    "v_mfma_f32_16x16x32_bf16 v[148:151], a[116:119], a[180:183], v[148:151]\n"
    "v_lshlrev_b32 v246, 16, v175\n"
    "v_and_b32 v247, s48, v175\n"
    "v_mfma_f32_16x16x32_bf16 v[144:147], a[120:123], a[180:183], v[144:147]\n"
    "s_waitcnt lgkmcnt(1)\n"
    "v_add_f32 v40, v40, v240\n"
    "v_add_f32 v41, v41, v241\n"
    "v_mfma_f32_16x16x32_bf16 v[148:151], a[120:123], a[196:199], v[148:151]\n"
    "v_add_f32 v42, v42, v242\n"
    "v_add_f32 v43, v43, v243\n"
    "v_mfma_f32_16x16x32_bf16 v[144:147], a[124:127], a[196:199], v[144:147]\n"
    "s_waitcnt lgkmcnt(0)\n"
    "v_add_f32 v44, v44, v244\n"
    "v_add_f32 v45, v45, v245\n"
    "v_mfma_f32_16x16x32_bf16 v[148:151], a[124:127], a[184:187], v[148:151]\n"
    "v_add_f32 v46, v46, v246\n"
    "v_add_f32 v47, v47, v247\n"
    "s_waitcnt vmcnt(1)\n"
    "v_max3_u32 v240, v176, v177, v178\n"
    "v_max3_u32 v240, v240, v179, v180\n"
    "v_max3_u32 v240, v240, v181, v182\n"
    "v_max_u32 v240, v240, v183\n"
    "v_cmp_eq_u32 vcc, -1, v240\n"
    "s_cbranch_vccz LM6ok_%=\n"
    "s_mov_b32 s43, 0x100000\n"
    "LM6lp_%=:\n"
    "buffer_load_dwordx4 v[176:179], v22, s[16:19], s26 offen sc1\n"
    "buffer_load_dwordx4 v[180:183], v22, s[16:19], s27 offen sc1\n"
    "s_waitcnt vmcnt(0)\n"
    "s_sub_u32 s43, s43, 1\n"
    "s_cbranch_scc1 LM6ok_%=\n"
    "v_max3_u32 v240, v176, v177, v178\n"
    "v_max3_u32 v240, v240, v179, v180\n"
    "v_max3_u32 v240, v240, v181, v182\n"
    "v_max_u32 v240, v240, v183\n"
    "v_cmp_eq_u32 vcc, -1, v240\n"
    "s_cbranch_vccnz LM6lp_%=\n"
    "LM6ok_%=:\n"
    "v_mfma_f32_16x16x32_bf16 v[144:147], v[236:239], v[176:179], v[144:147]\n"
    "v_mfma_f32_16x16x32_bf16 v[148:151], v[234:237], v[176:179], v[148:151]\n"
    "v_mfma_f32_16x16x32_bf16 v[40:43], v[236:239], v[180:183], v[40:43]\n"
    "v_mfma_f32_16x16x32_bf16 v[44:47], v[234:237], v[180:183], v[44:47]\n"
    "s_nop 4\n"
    "v_pk_add_f32 v[144:145], v[144:145], v[188:189]\n"
    "v_pk_add_f32 v[146:147], v[146:147], v[190:191]\n"
    "v_pk_add_f32 v[148:149], v[148:149], v[192:193]\n"
    "v_pk_add_f32 v[150:151], v[150:151], v[194:195]\n"
    "v_pk_mul_f32 v[240:241], v[144:145], s[46:47] op_sel_hi:[1,0]\n"
    "v_pk_mul_f32 v[242:243], v[146:147], s[46:47] op_sel_hi:[1,0]\n"
    "v_pk_mul_f32 v[244:245], v[148:149], s[46:47] op_sel_hi:[1,0]\n"
    "v_pk_mul_f32 v[246:247], v[150:151], s[46:47] op_sel_hi:[1,0]\n"
    "v_exp_f32 v240, -|v240|\n"
    "v_fma_f32 v248, -v144, v144, |v144|\n"
    "v_exp_f32 v241, -|v241|\n"
    "v_fma_f32 v249, -v145, v145, |v145|\n"
    "v_exp_f32 v242, -|v242|\n"
    "v_fma_f32 v250, -v146, v146, |v146|\n"
    "v_exp_f32 v243, -|v243|\n"
    "v_fma_f32 v251, -v147, v147, |v147|\n"
    "v_exp_f32 v244, -|v244|\n"
    "v_fma_f32 v252, -v148, v148, |v148|\n"
    "v_exp_f32 v245, -|v245|\n"
    "v_fma_f32 v253, -v149, v149, |v149|\n"
    "v_exp_f32 v246, -|v246|\n"
    "v_fma_f32 v254, -v150, v150, |v150|\n"
    "v_exp_f32 v247, -|v247|\n"
    "v_fma_f32 v255, -v151, v151, |v151|\n"
    "v_pk_fma_f32 v[168:169], v[240:241], 0.5, 0.5 op_sel_hi:[1,0,0] neg_lo:[1,0,0] neg_hi:[1,0,0]\n"
    "v_pk_fma_f32 v[170:171], v[242:243], 0.5, 0.5 op_sel_hi:[1,0,0] neg_lo:[1,0,0] neg_hi:[1,0,0]\n"
    "v_pk_fma_f32 v[172:173], v[244:245], 0.5, 0.5 op_sel_hi:[1,0,0] neg_lo:[1,0,0] neg_hi:[1,0,0]\n"
    "v_pk_fma_f32 v[174:175], v[246:247], 0.5, 0.5 op_sel_hi:[1,0,0] neg_lo:[1,0,0] neg_hi:[1,0,0]\n"
    "v_pk_fma_f32 v[240:241], v[240:241], 0.5, 0.5 op_sel_hi:[1,0,0]\n"
    "v_pk_fma_f32 v[242:243], v[242:243], 0.5, 0.5 op_sel_hi:[1,0,0]\n"
    "v_pk_fma_f32 v[244:245], v[244:245], 0.5, 0.5 op_sel_hi:[1,0,0]\n"
    "v_pk_fma_f32 v[246:247], v[246:247], 0.5, 0.5 op_sel_hi:[1,0,0]\n"
    "v_med3_f32 v248, v168, v248, |v144|\n"
    "v_med3_f32 v249, v169, v249, |v145|\n"
    "v_med3_f32 v250, v170, v250, |v146|\n"
    "v_med3_f32 v251, v171, v251, |v147|\n"
    "v_med3_f32 v252, v172, v252, |v148|\n"
    "v_med3_f32 v253, v173, v253, |v149|\n"
    "v_med3_f32 v254, v174, v254, |v150|\n"
    "v_med3_f32 v255, v175, v255, |v151|\n"
    "v_bfi_b32 v248, s47, v248, v144\n"
    "v_bfi_b32 v249, s47, v249, v145\n"
    "v_bfi_b32 v250, s47, v250, v146\n"
    "v_bfi_b32 v251, s47, v251, v147\n"
    "v_bfi_b32 v252, s47, v252, v148\n"
    "v_bfi_b32 v253, s47, v253, v149\n"
    "v_bfi_b32 v254, s47, v254, v150\n"
    "v_bfi_b32 v255, s47, v255, v151\n"
    "s_waitcnt vmcnt(0)\n"
    "v_max3_u32 v188, v184, v185, v186\n"
    "v_max_u32 v188, v188, v187\n"
    "v_cmp_eq_u32 vcc, -1, v188\n"
    "s_cbranch_vccz LL6ok_%=\n"
    "s_mov_b32 s43, 0x100000\n"
    "LL6lp_%=:\n"
    "buffer_load_dwordx4 v[184:187], v22, s[16:19], s25 offen sc1\n"
    "s_waitcnt vmcnt(0)\n"
    "s_sub_u32 s43, s43, 1\n"
    "s_cbranch_scc1 LL6ok_%=\n"
    "v_max3_u32 v188, v184, v185, v186\n"
    "v_max_u32 v188, v188, v187\n"
    "v_cmp_eq_u32 vcc, -1, v188\n"
    "s_cbranch_vccnz LL6lp_%=\n"
    "LL6ok_%=:\n"
    "v_lshlrev_b32 v188, 16, v184\n"
    "v_and_b32 v189, s48, v184\n"
    "v_lshlrev_b32 v190, 16, v185\n"
    "v_and_b32 v191, s48, v185\n"
    "v_lshlrev_b32 v192, 16, v186\n"
    "v_and_b32 v193, s48, v186\n"
    "v_lshlrev_b32 v194, 16, v187\n"
    "v_and_b32 v195, s48, v187\n"
    "v_pk_add_f32 v[40:41], v[40:41], v[188:189]\n"
    "v_pk_add_f32 v[42:43], v[42:43], v[190:191]\n"
    "v_pk_add_f32 v[44:45], v[44:45], v[192:193]\n"
    "v_pk_add_f32 v[46:47], v[46:47], v[194:195]\n"
    "v_pk_mul_f32 v[200:201], v[40:41], s[44:45] op_sel:[0,1] op_sel_hi:[1,1]\n"
    "v_pk_mul_f32 v[202:203], v[42:43], s[44:45] op_sel:[0,1] op_sel_hi:[1,1]\n"
    "v_pk_mul_f32 v[204:205], v[44:45], s[44:45] op_sel:[0,1] op_sel_hi:[1,1]\n"
    "v_pk_mul_f32 v[206:207], v[46:47], s[44:45] op_sel:[0,1] op_sel_hi:[1,1]\n"
    "v_exp_f32 v200, v200\n"
    "v_exp_f32 v201, v201\n"
    "v_exp_f32 v202, v202\n"
    "v_exp_f32 v203, v203\n"
    "v_exp_f32 v204, v204\n"
    "v_exp_f32 v205, v205\n"
    "v_exp_f32 v206, v206\n"
    "v_exp_f32 v207, v207\n"
    "v_pk_add_f32 v[200:201], v[200:201], 1.0 op_sel_hi:[1,0]\n"
    "v_pk_add_f32 v[202:203], v[202:203], 1.0 op_sel_hi:[1,0]\n"
    "v_pk_add_f32 v[204:205], v[204:205], 1.0 op_sel_hi:[1,0]\n"
    "v_pk_add_f32 v[206:207], v[206:207], 1.0 op_sel_hi:[1,0]\n"
    "v_pk_mul_f32 v[240:241], v[240:241], v[200:201]\n"
    "v_pk_mul_f32 v[242:243], v[242:243], v[202:203]\n"
    "v_pk_mul_f32 v[244:245], v[244:245], v[204:205]\n"
    "v_pk_mul_f32 v[246:247], v[246:247], v[206:207]\n"
    "v_rcp_f32 v240, v240\n"
    "v_rcp_f32 v241, v241\n"
    "v_rcp_f32 v242, v242\n"
    "v_rcp_f32 v243, v243\n"
    "v_rcp_f32 v244, v244\n"
    "v_rcp_f32 v245, v245\n"
    "v_rcp_f32 v246, v246\n"
    "v_rcp_f32 v247, v247\n"
    "v_pk_mul_f32 v[248:249], v[248:249], v[240:241]\n"
    "v_pk_mul_f32 v[250:251], v[250:251], v[242:243]\n"
    "v_pk_mul_f32 v[252:253], v[252:253], v[244:245]\n"
    "v_pk_mul_f32 v[254:255], v[254:255], v[246:247]\n"
    "v_cvt_pk_bf16_f32 v240, v248, v249\n"
    "v_cvt_pk_bf16_f32 v241, v250, v251\n"
    "v_cvt_pk_bf16_f32 v242, v252, v253\n"
    "v_cvt_pk_bf16_f32 v243, v254, v255\n"
    "buffer_store_dwordx2 v[240:241], v25, s[20:23], 0 offen\n"
    "buffer_store_dwordx2 v[242:243], v25, s[20:23], 0 offen offset:2048\n"
    "s_endpgm\n"
    ".fill (LV0_%=+43008-.)/4, 4, 0xbf800000\n"
    "LV7_%=:\n"
    "ds_read_b128 a[192:195], v10 offset:62208\n"
    "v_mfma_f32_16x16x32_bf16 v[168:171], v[192:195], a[212:215], v[168:171]\n"
    "s_waitcnt lgkmcnt(2)\n"
    "ds_read_b128 a[196:199], v10 offset:62336\n"
    "v_mfma_f32_16x16x32_bf16 v[172:175], v[192:195], a[184:187], v[172:175]\n"
    "ds_read_b128 a[184:187], v10 offset:62464\n"
    "v_mfma_f32_16x16x32_bf16 v[96:99], a[138:141], a[236:239], v[96:99]\n"
    "ds_read_b128 a[200:203], v10 offset:62592\n"
    "v_mfma_f32_16x16x32_bf16 v[100:103], a[138:141], a[228:231], v[100:103]\n"
    "v_add_f32 v64, v64, v160\n"
    "v_add_f32 v65, v65, v161\n"
    "v_mfma_f32_16x16x32_bf16 v[104:107], a[138:141], a[204:207], v[104:107]\n"
    "v_add_f32 v66, v66, v162\n"
    "v_add_f32 v67, v67, v163\n"
    "v_mfma_f32_16x16x32_bf16 v[108:111], a[138:141], a[220:223], v[108:111]\n"
    "v_add_f32 v68, v68, v160\n"
    "v_add_f32 v69, v69, v161\n"
    "v_mfma_f32_16x16x32_bf16 v[112:115], a[138:141], a[176:179], v[112:115]\n"
    "ds_read_b128 a[176:179], v10 offset:62720\n"
    "v_add_f32 v70, v70, v162\n"
    "v_mfma_f32_16x16x32_bf16 v[116:119], a[138:141], a[232:235], v[116:119]\n"
    "v_add_f32 v71, v71, v163\n"
    "v_add_f32 v72, v72, v164\n"
    "v_mfma_f32_16x16x32_bf16 v[120:123], a[138:141], a[180:183], v[120:123]\n"
    "s_waitcnt lgkmcnt(5)\n"
    "v_add_f32 v73, v73, v165\n"
    "v_add_f32 v74, v74, v166\n"
    "v_mfma_f32_16x16x32_bf16 v[124:127], a[138:141], a[188:191], v[124:127]\n"
    "s_waitcnt lgkmcnt(4)\n"
    "v_add_f32 v75, v75, v167\n"
    "v_add_f32 v76, v76, v164\n"
    "v_mfma_f32_16x16x32_bf16 v[128:131], a[0:3], a[192:195], v[64:67]\n"
    "s_waitcnt lgkmcnt(3)\n"
    "ds_read_b128 a[180:183], v10 offset:62848\n"
    "v_add_f32 v77, v77, v165\n"
    "v_mfma_f32_16x16x32_bf16 v[132:135], a[0:3], a[196:199], v[68:71]\n"
    "s_waitcnt lgkmcnt(3)\n"
    "v_add_f32 v78, v78, v166\n"
    "v_add_f32 v79, v79, v167\n"
    "v_mfma_f32_16x16x32_bf16 v[136:139], a[0:3], a[184:187], v[72:75]\n"
    "s_waitcnt lgkmcnt(2)\n"
    "v_add_f32 v80, v80, v168\n"
    "v_add_f32 v81, v81, v169\n"
    "v_mfma_f32_16x16x32_bf16 v[140:143], a[0:3], a[200:203], v[76:79]\n"
    "v_add_f32 v82, v82, v170\n"
    "v_add_f32 v83, v83, v171\n"
    "v_mfma_f32_16x16x32_bf16 v[128:131], a[4:7], a[196:199], v[128:131]\n"
    "ds_read_b128 a[188:191], v10 offset:62976\n"
    "v_add_f32 v84, v84, v168\n"
    "v_mfma_f32_16x16x32_bf16 v[132:135], a[4:7], a[184:187], v[132:135]\n"
    "v_add_f32 v85, v85, v169\n"
    "v_add_f32 v86, v86, v170\n"
    "v_mfma_f32_16x16x32_bf16 v[136:139], a[4:7], a[200:203], v[136:139]\n"
    "s_waitcnt lgkmcnt(2)\n"
    "v_add_f32 v87, v87, v171\n"
    "v_add_f32 v88, v88, v172\n"
    "v_mfma_f32_16x16x32_bf16 v[140:143], a[4:7], a[176:179], v[140:143]\n"
    "v_add_f32 v89, v89, v173\n"
    "v_add_f32 v90, v90, v174\n"
    "v_mfma_f32_16x16x32_bf16 v[128:131], a[8:11], a[184:187], v[128:131]\n"
    "ds_read_b128 a[184:187], v10 offset:63104\n"
    "v_add_f32 v91, v91, v175\n"
    "v_mfma_f32_16x16x32_bf16 v[132:135], a[8:11], a[200:203], v[132:135]\n"
    "v_add_f32 v92, v92, v172\n"
    "v_add_f32 v93, v93, v173\n"
    "v_mfma_f32_16x16x32_bf16 v[136:139], a[8:11], a[176:179], v[136:139]\n"
    "s_waitcnt lgkmcnt(2)\n"
    "v_add_f32 v94, v94, v174\n"
    "v_add_f32 v95, v95, v175\n"
    "v_mfma_f32_16x16x32_bf16 v[140:143], a[8:11], a[180:183], v[140:143]\n"
    "v_add_f32 v120, v120, v88\n"
    "v_add_f32 v121, v121, v89\n"
    "v_mfma_f32_16x16x32_bf16 v[128:131], a[12:15], a[200:203], v[128:131]\n"
    "ds_read_b128 a[192:195], v11 offset:62208\n"
    "v_add_f32 v122, v122, v90\n"
    "v_mfma_f32_16x16x32_bf16 v[132:135], a[12:15], a[176:179], v[132:135]\n"
    "v_add_f32 v123, v123, v91\n"
    "v_add_f32 v124, v124, v92\n"
    "v_mfma_f32_16x16x32_bf16 v[136:139], a[12:15], a[180:183], v[136:139]\n"
    "s_waitcnt lgkmcnt(2)\n"
    "v_add_f32 v125, v125, v93\n"
    "v_add_f32 v126, v126, v94\n"
    "v_mfma_f32_16x16x32_bf16 v[140:143], a[12:15], a[188:191], v[140:143]\n"
    "v_add_f32 v127, v127, v95\n"
    "v_cvt_pk_bf16_f32 v200, v120, v121\n"
    "v_mfma_f32_16x16x32_bf16 v[128:131], a[16:19], a[176:179], v[128:131]\n"
    "ds_read_b128 a[176:179], v11 offset:62336\n"
    "v_cvt_pk_bf16_f32 v201, v122, v123\n"
    "v_mfma_f32_16x16x32_bf16 v[132:135], a[16:19], a[180:183], v[132:135]\n"
    "v_cvt_pk_bf16_f32 v202, v124, v125\n"
    "v_cvt_pk_bf16_f32 v203, v126, v127\n"
    "v_mfma_f32_16x16x32_bf16 v[136:139], a[16:19], a[188:191], v[136:139]\n"
    "s_waitcnt lgkmcnt(2)\n"
    "s_xor_b32 s36, s39, 131072\n"
    "v_min_u32 v200, -2, v200\n"
    "v_mfma_f32_16x16x32_bf16 v[140:143], a[16:19], a[184:187], v[140:143]\n"
    "v_min_u32 v201, -2, v201\n"
    "v_min_u32 v202, -2, v202\n"
    "v_mfma_f32_16x16x32_bf16 v[128:131], a[20:23], a[180:183], v[128:131]\n"
    "ds_read_b128 a[180:183], v11 offset:62464\n"
    "v_min_u32 v203, -2, v203\n"
    "v_mfma_f32_16x16x32_bf16 v[132:135], a[20:23], a[188:191], v[132:135]\n"
    "v_add_f32 v112, v112, v80\n"
    "v_add_f32 v113, v113, v81\n"
    "v_mfma_f32_16x16x32_bf16 v[136:139], a[20:23], a[184:187], v[136:139]\n"
    "s_waitcnt lgkmcnt(2)\n"
    "v_add_f32 v114, v114, v82\n"
    "v_add_f32 v115, v115, v83\n"
    "v_mfma_f32_16x16x32_bf16 v[140:143], a[20:23], a[192:195], v[140:143]\n"
    "v_add_f32 v116, v116, v84\n"
    "v_add_f32 v117, v117, v85\n"
    "v_mfma_f32_16x16x32_bf16 v[128:131], a[24:27], a[188:191], v[128:131]\n"
    "ds_read_b128 a[188:191], v11 offset:62592\n"
    "v_add_f32 v118, v118, v86\n"
    "v_mfma_f32_16x16x32_bf16 v[132:135], a[24:27], a[184:187], v[132:135]\n"
    "v_add_f32 v119, v119, v87\n"
    "v_cvt_pk_bf16_f32 v204, v112, v113\n"
    "v_mfma_f32_16x16x32_bf16 v[136:139], a[24:27], a[192:195], v[136:139]\n"
    "s_waitcnt lgkmcnt(2)\n"
    "v_cvt_pk_bf16_f32 v205, v114, v115\n"
    "v_cvt_pk_bf16_f32 v206, v116, v117\n"
    "v_mfma_f32_16x16x32_bf16 v[140:143], a[24:27], a[176:179], v[140:143]\n"
    "v_cvt_pk_bf16_f32 v207, v118, v119\n"
    "v_mfma_f32_16x16x32_bf16 v[128:131], a[28:31], a[184:187], v[128:131]\n"
    "s_xor_b32 s36, s39, 163840\n"
    "ds_read_b128 a[184:187], v11 offset:62720\n"
    "v_min_u32 v204, -2, v204\n"
    "v_mfma_f32_16x16x32_bf16 v[132:135], a[28:31], a[192:195], v[132:135]\n"
    "v_min_u32 v205, -2, v205\n"
    "v_min_u32 v206, -2, v206\n"
    "v_mfma_f32_16x16x32_bf16 v[136:139], a[28:31], a[176:179], v[136:139]\n"
    "s_waitcnt lgkmcnt(2)\n"
    "v_min_u32 v207, -2, v207\n"
    "v_mfma_f32_16x16x32_bf16 v[140:143], a[28:31], a[180:183], v[140:143]\n"
    "s_waitcnt vmcnt(0)\n"
    "v_cmp_ne_u64 vcc, v[28:29], v[26:27]\n"
    "s_cbranch_vccz LF7ok_%=\n"
    "s_mov_b32 s43, 0x100000\n"
    "LF7lp_%=:\n"
    "buffer_load_dwordx2 v[28:29], v23, s[16:19], s40 offen offset:0 sc1\n"
    "s_waitcnt vmcnt(0)\n"
    "s_sub_u32 s43, s43, 1\n"
    "s_cbranch_scc1 LF7ok_%=\n"
    "v_cmp_ne_u64 vcc, v[28:29], v[26:27]\n"
    "s_cbranch_vccnz LF7lp_%=\n"
    "LF7ok_%=:\n"
    "v_mfma_f32_16x16x32_bf16 v[128:131], a[32:35], a[192:195], v[128:131]\n"
    "s_xor_b32 s36, s39, 131072\n"
    "ds_read_b128 a[192:195], v11 offset:62848\n"
    "v_mfma_f32_16x16x32_bf16 v[132:135], a[32:35], a[176:179], v[132:135]\n"
    "buffer_store_dwordx4 v[200:203], v22, s[16:19], s36 offen sc1\n"
    "v_mfma_f32_16x16x32_bf16 v[136:139], a[32:35], a[180:183], v[136:139]\n"
    "s_waitcnt lgkmcnt(2)\n"
    "s_xor_b32 s36, s39, 163840\n"
    "v_mfma_f32_16x16x32_bf16 v[140:143], a[32:35], a[188:191], v[140:143]\n"
    "v_mfma_f32_16x16x32_bf16 v[128:131], a[36:39], a[176:179], v[128:131]\n"
    "ds_read_b128 a[176:179], v11 offset:62976\n"
    "v_mfma_f32_16x16x32_bf16 v[132:135], a[36:39], a[180:183], v[132:135]\n"
    "buffer_store_dwordx4 v[204:207], v22, s[16:19], s36 offen sc1\n"
    "v_mfma_f32_16x16x32_bf16 v[136:139], a[36:39], a[188:191], v[136:139]\n"
    "s_waitcnt lgkmcnt(2)\n"
    "v_add_f32 v104, v104, v72\n"
    "v_add_f32 v105, v105, v73\n"
    "v_mfma_f32_16x16x32_bf16 v[140:143], a[36:39], a[184:187], v[140:143]\n"
    "v_add_f32 v106, v106, v74\n"
    "v_add_f32 v107, v107, v75\n"
    "v_mfma_f32_16x16x32_bf16 v[128:131], a[40:43], a[180:183], v[128:131]\n"
    "ds_read_b128 a[180:183], v11 offset:63104\n"
    "v_add_f32 v108, v108, v76\n"
    "v_mfma_f32_16x16x32_bf16 v[132:135], a[40:43], a[188:191], v[132:135]\n"
    "v_add_f32 v109, v109, v77\n"
    "v_add_f32 v110, v110, v78\n"
    "v_mfma_f32_16x16x32_bf16 v[136:139], a[40:43], a[184:187], v[136:139]\n"
    "s_waitcnt lgkmcnt(2)\n"
    "v_add_f32 v111, v111, v79\n"
    "v_cvt_pk_bf16_f32 v208, v104, v105\n"
    "v_mfma_f32_16x16x32_bf16 v[140:143], a[40:43], a[192:195], v[140:143]\n"
    "v_cvt_pk_bf16_f32 v209, v106, v107\n"
    "v_cvt_pk_bf16_f32 v210, v108, v109\n"
    "v_mfma_f32_16x16x32_bf16 v[128:131], a[44:47], a[188:191], v[128:131]\n"
    "ds_read_b128 a[188:191], v12 offset:62208\n"
    "v_cvt_pk_bf16_f32 v211, v110, v111\n"
    "v_mfma_f32_16x16x32_bf16 v[132:135], a[44:47], a[184:187], v[132:135]\n"
    "s_xor_b32 s36, s39, 196608\n"
    "v_min_u32 v208, -2, v208\n"
    "v_min_u32 v209, -2, v209\n"
    "v_mfma_f32_16x16x32_bf16 v[136:139], a[44:47], a[192:195], v[136:139]\n"
    "s_waitcnt lgkmcnt(2)\n"
    "v_min_u32 v210, -2, v210\n"
    "v_min_u32 v211, -2, v211\n"
    "v_mfma_f32_16x16x32_bf16 v[140:143], a[44:47], a[176:179], v[140:143]\n"
    "buffer_store_dwordx4 v[208:211], v22, s[16:19], s36 offen sc1\n"
    "v_mfma_f32_16x16x32_bf16 v[128:131], a[48:51], a[184:187], v[128:131]\n"
    "ds_read_b128 a[184:187], v12 offset:62336\n"
    "v_add_f32 v96, v96, v64\n"
    "v_mfma_f32_16x16x32_bf16 v[132:135], a[48:51], a[192:195], v[132:135]\n"
    "v_add_f32 v97, v97, v65\n"
    "v_add_f32 v98, v98, v66\n"
    "v_mfma_f32_16x16x32_bf16 v[136:139], a[48:51], a[176:179], v[136:139]\n"
    "s_waitcnt lgkmcnt(2)\n"
    "v_add_f32 v99, v99, v67\n"
    "v_add_f32 v100, v100, v68\n"
    "v_mfma_f32_16x16x32_bf16 v[140:143], a[48:51], a[180:183], v[140:143]\n"
    "v_add_f32 v101, v101, v69\n"
    "v_add_f32 v102, v102, v70\n"
    "v_mfma_f32_16x16x32_bf16 v[128:131], a[52:55], a[192:195], v[128:131]\n"
    "ds_read_b128 a[192:195], v12 offset:62464\n"
    "v_add_f32 v103, v103, v71\n"
    "v_mfma_f32_16x16x32_bf16 v[132:135], a[52:55], a[176:179], v[132:135]\n"
    "ds_read_b128 a[196:199], v13 offset:62208\n"
    "v_cvt_pk_bf16_f32 v212, v96, v97\n"
    "v_mfma_f32_16x16x32_bf16 v[136:139], a[52:55], a[180:183], v[136:139]\n"
    "s_waitcnt lgkmcnt(3)\n"
    "ds_read_b128 a[200:203], v13 offset:62336\n"
    "v_cvt_pk_bf16_f32 v213, v98, v99\n"
    "v_mfma_f32_16x16x32_bf16 v[140:143], a[52:55], a[188:191], v[140:143]\n"
    "ds_read_b128 a[204:207], v13 offset:62464\n"
    "v_cvt_pk_bf16_f32 v214, v100, v101\n"
    "v_mfma_f32_16x16x32_bf16 v[128:131], a[56:59], a[176:179], v[128:131]\n"
    "ds_read_b128 a[176:179], v13 offset:62592\n"
    "v_cvt_pk_bf16_f32 v215, v102, v103\n"
    "v_mfma_f32_16x16x32_bf16 v[132:135], a[56:59], a[180:183], v[132:135]\n"
    "s_xor_b32 s36, s39, 229376\n"
    "v_min_u32 v212, -2, v212\n"
    "v_min_u32 v213, -2, v213\n"
    "v_mfma_f32_16x16x32_bf16 v[136:139], a[56:59], a[188:191], v[136:139]\n"
    "s_waitcnt lgkmcnt(5)\n"
    "v_min_u32 v214, -2, v214\n"
    "v_min_u32 v215, -2, v215\n"
    "v_mfma_f32_16x16x32_bf16 v[140:143], a[56:59], a[184:187], v[140:143]\n"
    "buffer_store_dwordx4 v[212:215], v22, s[16:19], s36 offen sc1\n"
    "v_mfma_f32_16x16x32_bf16 v[128:131], a[60:63], a[180:183], v[128:131]\n"
    "ds_read_b128 a[180:183], v13 offset:62720\n"
    "v_mfma_f32_16x16x32_bf16 v[132:135], a[60:63], a[188:191], v[132:135]\n"
    "v_mfma_f32_16x16x32_bf16 v[136:139], a[60:63], a[184:187], v[136:139]\n"
    "s_waitcnt lgkmcnt(5)\n"
    "v_mfma_f32_16x16x32_bf16 v[140:143], a[60:63], a[192:195], v[140:143]\n"
    "s_waitcnt lgkmcnt(4)\n"
    "v_mfma_f32_16x16x32_bf16 v[128:131], a[64:67], a[196:199], v[128:131]\n"
    "s_waitcnt lgkmcnt(3)\n"
    "ds_read_b128 a[184:187], v13 offset:62848\n"
    "v_mfma_f32_16x16x32_bf16 v[132:135], a[64:67], a[200:203], v[132:135]\n"
    "s_waitcnt lgkmcnt(3)\n"
    "v_mfma_f32_16x16x32_bf16 v[136:139], a[64:67], a[204:207], v[136:139]\n"
    "s_waitcnt lgkmcnt(2)\n"
    "v_mfma_f32_16x16x32_bf16 v[140:143], a[64:67], a[176:179], v[140:143]\n"
    "v_mfma_f32_16x16x32_bf16 v[128:131], a[68:71], a[200:203], v[128:131]\n"
    "ds_read_b128 a[188:191], v13 offset:62976\n"
    "v_mfma_f32_16x16x32_bf16 v[132:135], a[68:71], a[204:207], v[132:135]\n"
    "v_mfma_f32_16x16x32_bf16 v[136:139], a[68:71], a[176:179], v[136:139]\n"
    "s_waitcnt lgkmcnt(2)\n"
    "v_mfma_f32_16x16x32_bf16 v[140:143], a[68:71], a[180:183], v[140:143]\n"
    "v_mfma_f32_16x16x32_bf16 v[128:131], a[72:75], a[204:207], v[128:131]\n"
    "ds_read_b128 a[192:195], v13 offset:63104\n"
    "v_mfma_f32_16x16x32_bf16 v[132:135], a[72:75], a[176:179], v[132:135]\n"
    "v_mfma_f32_16x16x32_bf16 v[136:139], a[72:75], a[180:183], v[136:139]\n"
    "s_waitcnt lgkmcnt(2)\n"
    "v_mfma_f32_16x16x32_bf16 v[140:143], a[72:75], a[184:187], v[140:143]\n"
    "v_mfma_f32_16x16x32_bf16 v[128:131], a[76:79], a[176:179], v[128:131]\n"
    "ds_read_b128 a[176:179], v14 offset:62208\n"
    "v_mfma_f32_16x16x32_bf16 v[132:135], a[76:79], a[180:183], v[132:135]\n"
    "v_mfma_f32_16x16x32_bf16 v[136:139], a[76:79], a[184:187], v[136:139]\n"
    "s_waitcnt lgkmcnt(2)\n"
    "v_mfma_f32_16x16x32_bf16 v[140:143], a[76:79], a[188:191], v[140:143]\n"
    "v_mfma_f32_16x16x32_bf16 v[128:131], a[80:83], a[180:183], v[128:131]\n"
    "ds_read_b128 a[180:183], v14 offset:62336\n"
    "v_mfma_f32_16x16x32_bf16 v[132:135], a[80:83], a[184:187], v[132:135]\n"
    "v_mfma_f32_16x16x32_bf16 v[136:139], a[80:83], a[188:191], v[136:139]\n"
    "s_waitcnt lgkmcnt(2)\n"
    "v_mfma_f32_16x16x32_bf16 v[140:143], a[80:83], a[192:195], v[140:143]\n"
    "v_mfma_f32_16x16x32_bf16 v[128:131], a[84:87], a[184:187], v[128:131]\n"
    "ds_read_b128 a[184:187], v14 offset:62464\n"
    "v_mfma_f32_16x16x32_bf16 v[132:135], a[84:87], a[188:191], v[132:135]\n"
    "v_mfma_f32_16x16x32_bf16 v[136:139], a[84:87], a[192:195], v[136:139]\n"
    "s_waitcnt lgkmcnt(2)\n"
    "v_mfma_f32_16x16x32_bf16 v[140:143], a[84:87], a[176:179], v[140:143]\n"
    "v_mfma_f32_16x16x32_bf16 v[128:131], a[88:91], a[188:191], v[128:131]\n"
    "ds_read_b128 a[188:191], v14 offset:62592\n"
    "v_mfma_f32_16x16x32_bf16 v[132:135], a[88:91], a[192:195], v[132:135]\n"
    "v_mfma_f32_16x16x32_bf16 v[136:139], a[88:91], a[176:179], v[136:139]\n"
    "s_waitcnt lgkmcnt(2)\n"
    "v_mfma_f32_16x16x32_bf16 v[140:143], a[88:91], a[180:183], v[140:143]\n"
    "v_mfma_f32_16x16x32_bf16 v[128:131], a[92:95], a[192:195], v[128:131]\n"
    "ds_read_b128 a[192:195], v14 offset:62720\n"
    "v_mfma_f32_16x16x32_bf16 v[132:135], a[92:95], a[176:179], v[132:135]\n"
    "v_mfma_f32_16x16x32_bf16 v[136:139], a[92:95], a[180:183], v[136:139]\n"
    "s_waitcnt lgkmcnt(2)\n"
    "v_mfma_f32_16x16x32_bf16 v[140:143], a[92:95], a[184:187], v[140:143]\n"
    "v_mfma_f32_16x16x32_bf16 v[128:131], a[96:99], a[176:179], v[128:131]\n"
    "ds_read_b128 a[176:179], v14 offset:62848\n"
    "v_mfma_f32_16x16x32_bf16 v[132:135], a[96:99], a[180:183], v[132:135]\n"
    "v_mfma_f32_16x16x32_bf16 v[136:139], a[96:99], a[184:187], v[136:139]\n"
    "s_waitcnt lgkmcnt(2)\n"
    "v_mfma_f32_16x16x32_bf16 v[140:143], a[96:99], a[188:191], v[140:143]\n"
    "v_mfma_f32_16x16x32_bf16 v[128:131], a[100:103], a[180:183], v[128:131]\n"
    "ds_read_b128 a[180:183], v14 offset:62976\n"
    "v_mfma_f32_16x16x32_bf16 v[132:135], a[100:103], a[184:187], v[132:135]\n"
    "v_mfma_f32_16x16x32_bf16 v[136:139], a[100:103], a[188:191], v[136:139]\n"
    "s_waitcnt lgkmcnt(2)\n"
    "v_mfma_f32_16x16x32_bf16 v[140:143], a[100:103], a[192:195], v[140:143]\n"
    "v_mfma_f32_16x16x32_bf16 v[128:131], a[104:107], a[184:187], v[128:131]\n"
    "ds_read_b128 a[184:187], v14 offset:63104\n"
    "v_mfma_f32_16x16x32_bf16 v[132:135], a[104:107], a[188:191], v[132:135]\n"
    "v_mfma_f32_16x16x32_bf16 v[136:139], a[104:107], a[192:195], v[136:139]\n"
    "s_waitcnt lgkmcnt(2)\n"
    "v_mfma_f32_16x16x32_bf16 v[140:143], a[104:107], a[176:179], v[140:143]\n"
    "v_mfma_f32_16x16x32_bf16 v[128:131], a[108:111], a[188:191], v[128:131]\n"
    "ds_read_b128 a[188:191], v15 offset:62208\n"
    "v_mfma_f32_16x16x32_bf16 v[132:135], a[108:111], a[192:195], v[132:135]\n"
    "v_mfma_f32_16x16x32_bf16 v[136:139], a[108:111], a[176:179], v[136:139]\n"
    "s_waitcnt lgkmcnt(2)\n"
    "v_mfma_f32_16x16x32_bf16 v[140:143], a[108:111], a[180:183], v[140:143]\n"
    "v_mfma_f32_16x16x32_bf16 v[128:131], a[112:115], a[192:195], v[128:131]\n"
    "ds_read_b128 a[192:195], v15 offset:62336\n"
    "v_mfma_f32_16x16x32_bf16 v[132:135], a[112:115], a[176:179], v[132:135]\n"
    "v_mfma_f32_16x16x32_bf16 v[136:139], a[112:115], a[180:183], v[136:139]\n"
    "s_waitcnt lgkmcnt(2)\n"
    "buffer_load_dwordx4 v[160:163], v22, s[16:19], s28 offen sc1\n"
    "v_mfma_f32_16x16x32_bf16 v[140:143], a[112:115], a[184:187], v[140:143]\n"
    "v_mfma_f32_16x16x32_bf16 v[128:131], a[116:119], a[176:179], v[128:131]\n"
    "ds_read_b128 a[176:179], v15 offset:62464\n"
    "v_mfma_f32_16x16x32_bf16 v[132:135], a[116:119], a[180:183], v[132:135]\n"
    "ds_read_b128 a[196:199], v10 offset:62720\n"
    "v_mfma_f32_16x16x32_bf16 v[136:139], a[116:119], a[184:187], v[136:139]\n"
    "s_waitcnt lgkmcnt(3)\n"
    "ds_read_b128 a[200:203], v10 offset:62848\n"
    "v_mfma_f32_16x16x32_bf16 v[140:143], a[116:119], a[188:191], v[140:143]\n"
    "buffer_load_dwordx4 v[164:167], v22, s[16:19], s30 offen sc1\n"
    "v_mfma_f32_16x16x32_bf16 v[128:131], a[120:123], a[180:183], v[128:131]\n"
    "ds_read_b128 a[180:183], v10 offset:62976\n"
    "v_mfma_f32_16x16x32_bf16 v[132:135], a[120:123], a[184:187], v[132:135]\n"
    "v_mfma_f32_16x16x32_bf16 v[136:139], a[120:123], a[188:191], v[136:139]\n"
    "s_waitcnt lgkmcnt(4)\n"
    "ds_read_b128 a[204:207], v10 offset:63104\n"
    "v_mfma_f32_16x16x32_bf16 v[140:143], a[120:123], a[192:195], v[140:143]\n"
    "buffer_load_dwordx4 v[168:171], v22, s[16:19], s29 offen sc1\n"
    "v_mfma_f32_16x16x32_bf16 v[128:131], a[124:127], a[184:187], v[128:131]\n"
    "ds_read_b128 a[184:187], v11 offset:62208\n"
    "v_mfma_f32_16x16x32_bf16 v[132:135], a[124:127], a[188:191], v[132:135]\n"
    "v_mfma_f32_16x16x32_bf16 v[136:139], a[124:127], a[192:195], v[136:139]\n"
    "s_waitcnt lgkmcnt(5)\n"
    "ds_read_b128 a[188:191], v11 offset:62336\n"
    "v_mfma_f32_16x16x32_bf16 v[140:143], a[124:127], a[176:179], v[140:143]\n"
    "s_waitcnt lgkmcnt(5)\n"
    "buffer_load_dwordx4 v[172:175], v22, s[16:19], s31 offen sc1\n"
    "v_mfma_f32_16x16x32_bf16 v[144:147], a[0:3], a[196:199], v[80:83]\n"
    "s_waitcnt lgkmcnt(4)\n"
    "ds_read_b128 a[176:179], v11 offset:62464\n"
    "v_mfma_f32_16x16x32_bf16 v[148:151], a[0:3], a[200:203], v[84:87]\n"
    "v_mfma_f32_16x16x32_bf16 v[144:147], a[4:7], a[200:203], v[144:147]\n"
    "s_waitcnt lgkmcnt(4)\n"
    "ds_read_b128 a[192:195], v11 offset:62592\n"
    "v_mfma_f32_16x16x32_bf16 v[148:151], a[4:7], a[180:183], v[148:151]\n"
    "v_cvt_pk_bf16_f32 v216, v136, v137\n"
    "v_cvt_pk_bf16_f32 v217, v138, v139\n"
    "v_mfma_f32_16x16x32_bf16 v[144:147], a[8:11], a[180:183], v[144:147]\n"
    "s_waitcnt lgkmcnt(4)\n"
    "ds_read_b128 a[180:183], v11 offset:62720\n"
    "v_cvt_pk_bf16_f32 v218, v140, v141\n"
    "v_mfma_f32_16x16x32_bf16 v[148:151], a[8:11], a[204:207], v[148:151]\n"
    "v_cvt_pk_bf16_f32 v219, v142, v143\n"
    "v_mfma_f32_16x16x32_bf16 v[144:147], a[12:15], a[204:207], v[144:147]\n"
    "s_waitcnt lgkmcnt(4)\n"
    "s_xor_b32 s24, s39, 65536\n"
    "ds_read_b128 a[196:199], v11 offset:62848\n"
    "v_mfma_f32_16x16x32_bf16 v[148:151], a[12:15], a[184:187], v[148:151]\n"
    "v_min_u32 v216, -2, v216\n"
    "v_min_u32 v217, -2, v217\n"
    "v_mfma_f32_16x16x32_bf16 v[144:147], a[16:19], a[184:187], v[144:147]\n"
    "s_waitcnt lgkmcnt(4)\n"
    "ds_read_b128 a[184:187], v11 offset:62976\n"
    "v_min_u32 v218, -2, v218\n"
    "v_mfma_f32_16x16x32_bf16 v[148:151], a[16:19], a[188:191], v[148:151]\n"
    "v_min_u32 v219, -2, v219\n"
    "v_mfma_f32_16x16x32_bf16 v[144:147], a[20:23], a[188:191], v[144:147]\n"
    "s_waitcnt lgkmcnt(4)\n"
    "ds_read_b128 a[188:191], v11 offset:63104\n"
    "v_mfma_f32_16x16x32_bf16 v[148:151], a[20:23], a[176:179], v[148:151]\n"
    "buffer_store_dwordx4 v[216:219], v22, s[16:19], s24 offen sc1\n"
    "v_mfma_f32_16x16x32_bf16 v[144:147], a[24:27], a[176:179], v[144:147]\n"
    "s_waitcnt lgkmcnt(4)\n"
    "ds_read_b128 a[176:179], v12 offset:62208\n"
    "v_cvt_pk_bf16_f32 v220, v128, v129\n"
    "v_mfma_f32_16x16x32_bf16 v[148:151], a[24:27], a[192:195], v[148:151]\n"
    "v_cvt_pk_bf16_f32 v221, v130, v131\n"
    "v_cvt_pk_bf16_f32 v222, v132, v133\n"
    "v_mfma_f32_16x16x32_bf16 v[144:147], a[28:31], a[192:195], v[144:147]\n"
    "s_waitcnt lgkmcnt(4)\n"
    "ds_read_b128 a[192:195], v12 offset:62336\n"
    "v_cvt_pk_bf16_f32 v223, v134, v135\n"
    "v_mfma_f32_16x16x32_bf16 v[148:151], a[28:31], a[180:183], v[148:151]\n"
    "s_xor_b32 s24, s39, 98304\n"
    "v_min_u32 v220, -2, v220\n"
    "v_min_u32 v221, -2, v221\n"
    "v_mfma_f32_16x16x32_bf16 v[144:147], a[32:35], a[180:183], v[144:147]\n"
    "s_waitcnt lgkmcnt(4)\n"
    "ds_read_b128 a[180:183], v12 offset:62464\n"
    "v_min_u32 v222, -2, v222\n"
    "v_mfma_f32_16x16x32_bf16 v[148:151], a[32:35], a[196:199], v[148:151]\n"
    "v_min_u32 v223, -2, v223\n"
    "v_mfma_f32_16x16x32_bf16 v[144:147], a[36:39], a[196:199], v[144:147]\n"
    "s_waitcnt lgkmcnt(4)\n"
    "ds_read_b128 a[196:199], v12 offset:62592\n"
    "v_mfma_f32_16x16x32_bf16 v[148:151], a[36:39], a[184:187], v[148:151]\n"
    "buffer_store_dwordx4 v[220:223], v22, s[16:19], s24 offen sc1\n"
    "v_mfma_f32_16x16x32_bf16 v[144:147], a[40:43], a[184:187], v[144:147]\n"
    "s_waitcnt lgkmcnt(4)\n"
    "ds_read_b128 a[184:187], v12 offset:62720\n"
    "v_mfma_f32_16x16x32_bf16 v[148:151], a[40:43], a[188:191], v[148:151]\n"
    "ds_read_b128 a[200:203], v13 offset:62720\n"
    "v_mfma_f32_16x16x32_bf16 v[144:147], a[44:47], a[188:191], v[144:147]\n"
    "s_waitcnt lgkmcnt(5)\n"
    "ds_read_b128 a[188:191], v13 offset:62848\n"
    "v_mfma_f32_16x16x32_bf16 v[148:151], a[44:47], a[176:179], v[148:151]\n"
    "v_mfma_f32_16x16x32_bf16 v[144:147], a[48:51], a[176:179], v[144:147]\n"
    "s_waitcnt lgkmcnt(5)\n"
    "ds_read_b128 a[176:179], v13 offset:62976\n"
    "v_mfma_f32_16x16x32_bf16 v[148:151], a[48:51], a[192:195], v[148:151]\n"
    "v_mfma_f32_16x16x32_bf16 v[144:147], a[52:55], a[192:195], v[144:147]\n"
    "s_waitcnt lgkmcnt(5)\n"
    "ds_read_b128 a[192:195], v13 offset:63104\n"
    "v_mfma_f32_16x16x32_bf16 v[148:151], a[52:55], a[180:183], v[148:151]\n"
    "v_mfma_f32_16x16x32_bf16 v[144:147], a[56:59], a[180:183], v[144:147]\n"
    "s_waitcnt lgkmcnt(5)\n"
    "ds_read_b128 a[180:183], v14 offset:62208\n"
    "v_mfma_f32_16x16x32_bf16 v[148:151], a[56:59], a[196:199], v[148:151]\n"
    "v_mfma_f32_16x16x32_bf16 v[144:147], a[60:63], a[196:199], v[144:147]\n"
    "s_waitcnt lgkmcnt(5)\n"
    "ds_read_b128 a[196:199], v14 offset:62336\n"
    "v_mfma_f32_16x16x32_bf16 v[148:151], a[60:63], a[184:187], v[148:151]\n"
    "s_waitcnt lgkmcnt(5)\n"
    "v_mfma_f32_16x16x32_bf16 v[144:147], a[64:67], a[200:203], v[144:147]\n"
    "s_waitcnt lgkmcnt(4)\n"
    "ds_read_b128 a[184:187], v14 offset:62464\n"
    "v_mfma_f32_16x16x32_bf16 v[148:151], a[64:67], a[188:191], v[148:151]\n"
    "v_mfma_f32_16x16x32_bf16 v[144:147], a[68:71], a[188:191], v[144:147]\n"
    "s_waitcnt lgkmcnt(4)\n"
    "ds_read_b128 a[188:191], v14 offset:62592\n"
    "v_mfma_f32_16x16x32_bf16 v[148:151], a[68:71], a[176:179], v[148:151]\n"
    "v_mfma_f32_16x16x32_bf16 v[144:147], a[72:75], a[176:179], v[144:147]\n"
    "s_waitcnt lgkmcnt(4)\n"
    "ds_read_b128 a[176:179], v14 offset:62720\n"
    "v_mfma_f32_16x16x32_bf16 v[148:151], a[72:75], a[192:195], v[148:151]\n"
    "v_mfma_f32_16x16x32_bf16 v[144:147], a[76:79], a[192:195], v[144:147]\n"
    "s_waitcnt lgkmcnt(4)\n"
    "ds_read_b128 a[192:195], v14 offset:62848\n"
    "v_mfma_f32_16x16x32_bf16 v[148:151], a[76:79], a[180:183], v[148:151]\n"
    "v_mfma_f32_16x16x32_bf16 v[144:147], a[80:83], a[180:183], v[144:147]\n"
    "s_waitcnt lgkmcnt(4)\n"
    "ds_read_b128 a[180:183], v14 offset:62976\n"
    "v_mfma_f32_16x16x32_bf16 v[148:151], a[80:83], a[196:199], v[148:151]\n"
    "v_mfma_f32_16x16x32_bf16 v[144:147], a[84:87], a[196:199], v[144:147]\n"
    "s_waitcnt lgkmcnt(4)\n"
    "ds_read_b128 a[196:199], v14 offset:63104\n"
    "v_mfma_f32_16x16x32_bf16 v[148:151], a[84:87], a[184:187], v[148:151]\n"
    "v_mfma_f32_16x16x32_bf16 v[144:147], a[88:91], a[184:187], v[144:147]\n"
    "s_waitcnt lgkmcnt(4)\n"
    "ds_read_b128 a[184:187], v15 offset:62208\n"
    "v_mfma_f32_16x16x32_bf16 v[148:151], a[88:91], a[188:191], v[148:151]\n"
    "v_mfma_f32_16x16x32_bf16 v[144:147], a[92:95], a[188:191], v[144:147]\n"
    "s_waitcnt lgkmcnt(4)\n"
    "ds_read_b128 a[188:191], v15 offset:62336\n"
    "v_mfma_f32_16x16x32_bf16 v[148:151], a[92:95], a[176:179], v[148:151]\n"
    "v_mfma_f32_16x16x32_bf16 v[144:147], a[96:99], a[176:179], v[144:147]\n"
    "s_waitcnt lgkmcnt(4)\n"
    "ds_read_b128 a[176:179], v15 offset:62464\n"
    "v_mfma_f32_16x16x32_bf16 v[148:151], a[96:99], a[192:195], v[148:151]\n"
    "v_mfma_f32_16x16x32_bf16 v[144:147], a[100:103], a[192:195], v[144:147]\n"
    "s_waitcnt lgkmcnt(4)\n"
    "ds_read_b128 a[192:195], v15 offset:62592\n"
    "v_mfma_f32_16x16x32_bf16 v[148:151], a[100:103], a[180:183], v[148:151]\n"
    "v_mfma_f32_16x16x32_bf16 v[144:147], a[104:107], a[180:183], v[144:147]\n"
    "s_waitcnt lgkmcnt(4)\n"
    "ds_read_b128 a[180:183], v15 offset:62720\n"
    "v_mfma_f32_16x16x32_bf16 v[148:151], a[104:107], a[196:199], v[148:151]\n"
    "ds_read_b128 a[200:203], v10 offset:62976\n"
    "v_mfma_f32_16x16x32_bf16 v[144:147], a[108:111], a[196:199], v[144:147]\n"
    "s_waitcnt lgkmcnt(5)\n"
    "ds_read_b128 a[196:199], v10 offset:63104\n"
    "v_mfma_f32_16x16x32_bf16 v[148:151], a[108:111], a[184:187], v[148:151]\n"
    "v_mfma_f32_16x16x32_bf16 v[144:147], a[112:115], a[184:187], v[144:147]\n"
    "s_waitcnt lgkmcnt(5)\n"
    "ds_read_b128 a[184:187], v11 offset:62208\n"
    "v_mfma_f32_16x16x32_bf16 v[148:151], a[112:115], a[188:191], v[148:151]\n"
    "v_mfma_f32_16x16x32_bf16 v[144:147], a[116:119], a[188:191], v[144:147]\n"
    "s_waitcnt lgkmcnt(5)\n"
    "ds_read_b128 a[188:191], v11 offset:62336\n"
    "v_mfma_f32_16x16x32_bf16 v[148:151], a[116:119], a[176:179], v[148:151]\n"
    "v_mfma_f32_16x16x32_bf16 v[144:147], a[120:123], a[176:179], v[144:147]\n"
    "s_waitcnt lgkmcnt(5)\n"
    "ds_read_b128 a[176:179], v11 offset:62464\n"
    "v_mfma_f32_16x16x32_bf16 v[148:151], a[120:123], a[192:195], v[148:151]\n"
    "v_mfma_f32_16x16x32_bf16 v[144:147], a[124:127], a[192:195], v[144:147]\n"
    "s_waitcnt lgkmcnt(5)\n"
    "ds_read_b128 a[192:195], v11 offset:62592\n"
    "v_mfma_f32_16x16x32_bf16 v[148:151], a[124:127], a[180:183], v[148:151]\n"
    "s_waitcnt lgkmcnt(5)\n"
    "v_mfma_f32_16x16x32_bf16 v[152:155], a[0:3], a[200:203], v[88:91]\n"
    "s_waitcnt lgkmcnt(4)\n"
    "ds_read_b128 a[180:183], v11 offset:62720\n"
    "v_mfma_f32_16x16x32_bf16 v[156:159], a[0:3], a[196:199], v[92:95]\n"
    "v_mfma_f32_16x16x32_bf16 v[152:155], a[4:7], a[196:199], v[152:155]\n"
    "s_waitcnt lgkmcnt(4)\n"
    "ds_read_b128 a[196:199], v11 offset:62848\n"
    "v_mfma_f32_16x16x32_bf16 v[156:159], a[4:7], a[184:187], v[156:159]\n"
    "v_cvt_pk_bf16_f32 v224, v144, v145\n"
    "v_cvt_pk_bf16_f32 v225, v146, v147\n"
    "v_mfma_f32_16x16x32_bf16 v[152:155], a[8:11], a[184:187], v[152:155]\n"
    "s_waitcnt lgkmcnt(4)\n"
    "ds_read_b128 a[184:187], v11 offset:62976\n"
    "v_cvt_pk_bf16_f32 v226, v148, v149\n"
    "v_mfma_f32_16x16x32_bf16 v[156:159], a[8:11], a[188:191], v[156:159]\n"
    "v_cvt_pk_bf16_f32 v227, v150, v151\n"
    "v_mfma_f32_16x16x32_bf16 v[152:155], a[12:15], a[188:191], v[152:155]\n"
    "s_waitcnt lgkmcnt(4)\n"
    "s_xor_b32 s24, s39, 32768\n"
    "ds_read_b128 a[188:191], v11 offset:63104\n"
    "v_mfma_f32_16x16x32_bf16 v[156:159], a[12:15], a[176:179], v[156:159]\n"
    "v_min_u32 v224, -2, v224\n"
    "v_min_u32 v225, -2, v225\n"
    "v_mfma_f32_16x16x32_bf16 v[152:155], a[16:19], a[176:179], v[152:155]\n"
    "s_waitcnt lgkmcnt(4)\n"
    "ds_read_b128 a[176:179], v12 offset:62208\n"
    "v_min_u32 v226, -2, v226\n"
    "v_mfma_f32_16x16x32_bf16 v[156:159], a[16:19], a[192:195], v[156:159]\n"
    "v_min_u32 v227, -2, v227\n"
    "v_mfma_f32_16x16x32_bf16 v[152:155], a[20:23], a[192:195], v[152:155]\n"
    "s_waitcnt lgkmcnt(4)\n"
    "ds_read_b128 a[192:195], v12 offset:62336\n"
    "v_mfma_f32_16x16x32_bf16 v[156:159], a[20:23], a[180:183], v[156:159]\n"
    "buffer_store_dwordx4 v[224:227], v22, s[16:19], s24 offen sc1\n"
    "v_mfma_f32_16x16x32_bf16 v[152:155], a[24:27], a[180:183], v[152:155]\n"
    "s_waitcnt lgkmcnt(4)\n"
    "ds_read_b128 a[180:183], v12 offset:62464\n"
    "v_mfma_f32_16x16x32_bf16 v[156:159], a[24:27], a[196:199], v[156:159]\n"
    "v_mfma_f32_16x16x32_bf16 v[152:155], a[28:31], a[196:199], v[152:155]\n"
    "s_waitcnt lgkmcnt(4)\n"
    "ds_read_b128 a[196:199], v12 offset:62592\n"
    "v_mfma_f32_16x16x32_bf16 v[156:159], a[28:31], a[184:187], v[156:159]\n"
    "v_mfma_f32_16x16x32_bf16 v[152:155], a[32:35], a[184:187], v[152:155]\n"
    "s_waitcnt lgkmcnt(4)\n"
    "ds_read_b128 a[184:187], v12 offset:62720\n"
    "v_mfma_f32_16x16x32_bf16 v[156:159], a[32:35], a[188:191], v[156:159]\n"
    "v_mfma_f32_16x16x32_bf16 v[152:155], a[36:39], a[188:191], v[152:155]\n"
    "s_waitcnt lgkmcnt(4)\n"
    "ds_read_b128 a[188:191], v12 offset:62848\n"
    "v_mfma_f32_16x16x32_bf16 v[156:159], a[36:39], a[176:179], v[156:159]\n"
    "v_mfma_f32_16x16x32_bf16 v[152:155], a[40:43], a[176:179], v[152:155]\n"
    "s_waitcnt lgkmcnt(4)\n"
    "ds_read_b128 a[176:179], v12 offset:62976\n"
    "v_mfma_f32_16x16x32_bf16 v[156:159], a[40:43], a[192:195], v[156:159]\n"
    "ds_read_b128 a[200:203], v13 offset:62976\n"
    "v_mfma_f32_16x16x32_bf16 v[152:155], a[44:47], a[192:195], v[152:155]\n"
    "s_waitcnt lgkmcnt(5)\n"
    "ds_read_b128 a[192:195], v13 offset:63104\n"
    "v_mfma_f32_16x16x32_bf16 v[156:159], a[44:47], a[180:183], v[156:159]\n"
    "v_mfma_f32_16x16x32_bf16 v[152:155], a[48:51], a[180:183], v[152:155]\n"
    "s_waitcnt lgkmcnt(5)\n"
    "s_waitcnt vmcnt(3)\n"
    "ds_read_b128 a[180:183], v14 offset:62208\n"
    "v_mfma_f32_16x16x32_bf16 v[156:159], a[48:51], a[196:199], v[156:159]\n"
    "v_max3_u32 v240, v160, v161, v162\n"
    "v_max3_u32 v240, v240, v163, v164\n"
    "v_mfma_f32_16x16x32_bf16 v[152:155], a[52:55], a[196:199], v[152:155]\n"
    "s_waitcnt lgkmcnt(5)\n"
    "ds_read_b128 a[196:199], v14 offset:62336\n"
    "v_max3_u32 v240, v240, v165, v166\n"
    "v_mfma_f32_16x16x32_bf16 v[156:159], a[52:55], a[184:187], v[156:159]\n"
    "buffer_load_dwordx4 v[176:179], v22, s[16:19], s26 offen sc1\n"
    "v_mfma_f32_16x16x32_bf16 v[152:155], a[56:59], a[184:187], v[152:155]\n"
    "s_waitcnt lgkmcnt(5)\n"
    "ds_read_b128 a[184:187], v14 offset:62464\n"
    "v_max3_u32 v240, v240, v167, v168\n"
    "v_mfma_f32_16x16x32_bf16 v[156:159], a[56:59], a[188:191], v[156:159]\n"
    "v_max3_u32 v240, v240, v169, v170\n"
    "v_max3_u32 v240, v240, v171, v172\n"
    "v_mfma_f32_16x16x32_bf16 v[152:155], a[60:63], a[188:191], v[152:155]\n"
    "s_waitcnt lgkmcnt(5)\n"
    "ds_read_b128 a[188:191], v14 offset:62592\n"
    "v_max3_u32 v240, v240, v173, v174\n"
    "v_mfma_f32_16x16x32_bf16 v[156:159], a[60:63], a[176:179], v[156:159]\n"
    "s_waitcnt lgkmcnt(5)\n"
    "buffer_load_dwordx4 v[180:183], v22, s[16:19], s27 offen sc1\n"
    "v_mfma_f32_16x16x32_bf16 v[152:155], a[64:67], a[200:203], v[152:155]\n"
    "s_waitcnt lgkmcnt(4)\n"
    "ds_read_b128 a[176:179], v14 offset:62720\n"
    "v_max_u32 v240, v240, v175\n"
    "v_mfma_f32_16x16x32_bf16 v[156:159], a[64:67], a[192:195], v[156:159]\n"
    "v_cmp_eq_u32 vcc, -1, v240\n"
    "s_cbranch_vccz LE7ok_%=\n"
    "s_mov_b32 s43, 0x100000\n"
    "LE7lp_%=:\n"
    "buffer_load_dwordx4 v[160:163], v22, s[16:19], s28 offen sc1\n"
    "buffer_load_dwordx4 v[164:167], v22, s[16:19], s30 offen sc1\n"
    "buffer_load_dwordx4 v[168:171], v22, s[16:19], s29 offen sc1\n"
    "buffer_load_dwordx4 v[172:175], v22, s[16:19], s31 offen sc1\n"
    "s_waitcnt vmcnt(0)\n"
    "s_sub_u32 s43, s43, 1\n"
    "s_cbranch_scc1 LE7ok_%=\n"
    "v_max3_u32 v240, v160, v161, v162\n"
    "v_max3_u32 v240, v240, v163, v164\n"
    "v_max3_u32 v240, v240, v165, v166\n"
    "v_max3_u32 v240, v240, v167, v168\n"
    "v_max3_u32 v240, v240, v169, v170\n"
    "v_max3_u32 v240, v240, v171, v172\n"
    "v_max3_u32 v240, v240, v173, v174\n"
    "v_max_u32 v240, v240, v175\n"
    "v_cmp_eq_u32 vcc, -1, v240\n"
    "s_cbranch_vccnz LE7lp_%=\n"
    "LE7ok_%=:\n"
    "v_mfma_f32_16x16x32_bf16 v[152:155], a[68:71], a[192:195], v[152:155]\n"
    "s_waitcnt lgkmcnt(4)\n"
    "ds_read_b128 a[192:195], v14 offset:62848\n"
    "v_lshlrev_b32 v188, 16, v160\n"
    "v_mfma_f32_16x16x32_bf16 v[156:159], a[68:71], a[180:183], v[156:159]\n"
    "v_and_b32 v189, s48, v160\n"
    "v_lshlrev_b32 v190, 16, v161\n"
    "v_mfma_f32_16x16x32_bf16 v[152:155], a[72:75], a[180:183], v[152:155]\n"
    "s_waitcnt lgkmcnt(4)\n"
    "ds_read_b128 a[180:183], v14 offset:62976\n"
    "v_and_b32 v191, s48, v161\n"
    "v_mfma_f32_16x16x32_bf16 v[156:159], a[72:75], a[196:199], v[156:159]\n"
    "v_lshlrev_b32 v192, 16, v162\n"
    "v_and_b32 v193, s48, v162\n"
    "v_mfma_f32_16x16x32_bf16 v[152:155], a[76:79], a[196:199], v[152:155]\n"
    "s_waitcnt lgkmcnt(4)\n"
    "ds_read_b128 a[196:199], v14 offset:63104\n"
    "v_lshlrev_b32 v194, 16, v163\n"
    "v_mfma_f32_16x16x32_bf16 v[156:159], a[76:79], a[184:187], v[156:159]\n"
    "v_and_b32 v195, s48, v163\n"
    "v_lshlrev_b32 v240, 16, v164\n"
    "v_mfma_f32_16x16x32_bf16 v[152:155], a[80:83], a[184:187], v[152:155]\n"
    "s_waitcnt lgkmcnt(4)\n"
    "ds_read_b128 a[184:187], v15 offset:62208\n"
    "v_and_b32 v241, s48, v164\n"
    "v_mfma_f32_16x16x32_bf16 v[156:159], a[80:83], a[188:191], v[156:159]\n"
    "v_lshlrev_b32 v242, 16, v165\n"
    "v_and_b32 v243, s48, v165\n"
    "v_mfma_f32_16x16x32_bf16 v[152:155], a[84:87], a[188:191], v[152:155]\n"
    "s_waitcnt lgkmcnt(4)\n"
    "ds_read_b128 a[188:191], v15 offset:62336\n"
    "v_lshlrev_b32 v244, 16, v166\n"
    "v_mfma_f32_16x16x32_bf16 v[156:159], a[84:87], a[176:179], v[156:159]\n"
    "v_and_b32 v245, s48, v166\n"
    "v_lshlrev_b32 v246, 16, v167\n"
    "v_mfma_f32_16x16x32_bf16 v[152:155], a[88:91], a[176:179], v[152:155]\n"
    "s_waitcnt lgkmcnt(4)\n"
    "ds_read_b128 a[176:179], v15 offset:62464\n"
    "v_and_b32 v247, s48, v167\n"
    "v_mfma_f32_16x16x32_bf16 v[156:159], a[88:91], a[192:195], v[156:159]\n"
    "v_add_f32 v188, v188, v240\n"
    "v_add_f32 v189, v189, v241\n"
    "v_mfma_f32_16x16x32_bf16 v[152:155], a[92:95], a[192:195], v[152:155]\n"
    "s_waitcnt lgkmcnt(4)\n"
    "ds_read_b128 a[192:195], v15 offset:62592\n"
    "v_add_f32 v190, v190, v242\n"
    "v_mfma_f32_16x16x32_bf16 v[156:159], a[92:95], a[180:183], v[156:159]\n"
    "v_add_f32 v191, v191, v243\n"
    "v_add_f32 v192, v192, v244\n"
    "v_mfma_f32_16x16x32_bf16 v[152:155], a[96:99], a[180:183], v[152:155]\n"
    "s_waitcnt lgkmcnt(4)\n"
    "ds_read_b128 a[180:183], v15 offset:62720\n"
    "v_add_f32 v193, v193, v245\n"
    "v_mfma_f32_16x16x32_bf16 v[156:159], a[96:99], a[196:199], v[156:159]\n"
    "v_add_f32 v194, v194, v246\n"
    "v_add_f32 v195, v195, v247\n"
    "v_mfma_f32_16x16x32_bf16 v[152:155], a[100:103], a[196:199], v[152:155]\n"
    "s_waitcnt lgkmcnt(4)\n"
    "ds_read_b128 a[196:199], v15 offset:62848\n"
    "v_lshlrev_b32 v40, 16, v168\n"
    "v_mfma_f32_16x16x32_bf16 v[156:159], a[100:103], a[184:187], v[156:159]\n"
    "v_and_b32 v41, s48, v168\n"
    "v_lshlrev_b32 v42, 16, v169\n"
    "v_mfma_f32_16x16x32_bf16 v[152:155], a[104:107], a[184:187], v[152:155]\n"
    "s_waitcnt lgkmcnt(4)\n"
    "ds_read_b128 a[184:187], v15 offset:62976\n"
    "v_and_b32 v43, s48, v169\n"
    "v_mfma_f32_16x16x32_bf16 v[156:159], a[104:107], a[188:191], v[156:159]\n"
    "v_lshlrev_b32 v44, 16, v170\n"
    "v_and_b32 v45, s48, v170\n"
    "v_mfma_f32_16x16x32_bf16 v[152:155], a[108:111], a[188:191], v[152:155]\n"
    "s_waitcnt lgkmcnt(4)\n"
    "buffer_load_dwordx4 v[184:187], v22, s[16:19], s25 offen sc1\n"
    "v_mfma_f32_16x16x32_bf16 v[156:159], a[108:111], a[176:179], v[156:159]\n"
    "v_lshlrev_b32 v46, 16, v171\n"
    "v_and_b32 v47, s48, v171\n"
    "v_mfma_f32_16x16x32_bf16 v[152:155], a[112:115], a[176:179], v[152:155]\n"
    "s_waitcnt lgkmcnt(3)\n"
    "v_lshlrev_b32 v240, 16, v172\n"
    "v_and_b32 v241, s48, v172\n"
    "v_mfma_f32_16x16x32_bf16 v[156:159], a[112:115], a[192:195], v[156:159]\n"
    "v_lshlrev_b32 v242, 16, v173\n"
    "v_and_b32 v243, s48, v173\n"
    "v_mfma_f32_16x16x32_bf16 v[152:155], a[116:119], a[192:195], v[152:155]\n"
    "s_waitcnt lgkmcnt(2)\n"
    "v_lshlrev_b32 v244, 16, v174\n"
    "v_and_b32 v245, s48, v174\n"
    "v_mfma_f32_16x16x32_bf16 v[156:159], a[116:119], a[180:183], v[156:159]\n"
    "v_lshlrev_b32 v246, 16, v175\n"
    "v_and_b32 v247, s48, v175\n"
    "v_mfma_f32_16x16x32_bf16 v[152:155], a[120:123], a[180:183], v[152:155]\n"
    "s_waitcnt lgkmcnt(1)\n"
    "v_add_f32 v40, v40, v240\n"
    "v_add_f32 v41, v41, v241\n"
    "v_mfma_f32_16x16x32_bf16 v[156:159], a[120:123], a[196:199], v[156:159]\n"
    "v_add_f32 v42, v42, v242\n"
    "v_add_f32 v43, v43, v243\n"
    "v_mfma_f32_16x16x32_bf16 v[152:155], a[124:127], a[196:199], v[152:155]\n"
    "s_waitcnt lgkmcnt(0)\n"
    "v_add_f32 v44, v44, v244\n"
    "v_add_f32 v45, v45, v245\n"
    "v_mfma_f32_16x16x32_bf16 v[156:159], a[124:127], a[184:187], v[156:159]\n"
    "v_add_f32 v46, v46, v246\n"
    "v_add_f32 v47, v47, v247\n"
    "s_waitcnt vmcnt(1)\n"
    "v_max3_u32 v240, v176, v177, v178\n"
    "v_max3_u32 v240, v240, v179, v180\n"
    "v_max3_u32 v240, v240, v181, v182\n"
    "v_max_u32 v240, v240, v183\n"
    "v_cmp_eq_u32 vcc, -1, v240\n"
    "s_cbranch_vccz LM7ok_%=\n"
    "s_mov_b32 s43, 0x100000\n"
    "LM7lp_%=:\n"
    "buffer_load_dwordx4 v[176:179], v22, s[16:19], s26 offen sc1\n"
    "buffer_load_dwordx4 v[180:183], v22, s[16:19], s27 offen sc1\n"
    "s_waitcnt vmcnt(0)\n"
    "s_sub_u32 s43, s43, 1\n"
    "s_cbranch_scc1 LM7ok_%=\n"
    "v_max3_u32 v240, v176, v177, v178\n"
    "v_max3_u32 v240, v240, v179, v180\n"
    "v_max3_u32 v240, v240, v181, v182\n"
    "v_max_u32 v240, v240, v183\n"
    "v_cmp_eq_u32 vcc, -1, v240\n"
    "s_cbranch_vccnz LM7lp_%=\n"
    "LM7ok_%=:\n"
    "v_mfma_f32_16x16x32_bf16 v[152:155], v[236:239], v[176:179], v[152:155]\n"
    "v_mfma_f32_16x16x32_bf16 v[156:159], v[234:237], v[176:179], v[156:159]\n"
    "v_mfma_f32_16x16x32_bf16 v[40:43], v[236:239], v[180:183], v[40:43]\n"
    "v_mfma_f32_16x16x32_bf16 v[44:47], v[234:237], v[180:183], v[44:47]\n"
    "s_nop 4\n"
    "v_pk_add_f32 v[152:153], v[152:153], v[188:189]\n"
    "v_pk_add_f32 v[154:155], v[154:155], v[190:191]\n"
    "v_pk_add_f32 v[156:157], v[156:157], v[192:193]\n"
    "v_pk_add_f32 v[158:159], v[158:159], v[194:195]\n"
    "v_pk_mul_f32 v[200:201], v[152:153], s[44:45] op_sel:[0,1] op_sel_hi:[1,1]\n"
    "v_pk_mul_f32 v[202:203], v[154:155], s[44:45] op_sel:[0,1] op_sel_hi:[1,1]\n"
    "v_pk_mul_f32 v[204:205], v[156:157], s[44:45] op_sel:[0,1] op_sel_hi:[1,1]\n"
    "v_pk_mul_f32 v[206:207], v[158:159], s[44:45] op_sel:[0,1] op_sel_hi:[1,1]\n"
    "v_exp_f32 v200, v200\n"
    "v_exp_f32 v201, v201\n"
    "v_exp_f32 v202, v202\n"
    "v_exp_f32 v203, v203\n"
    "v_exp_f32 v204, v204\n"
    "v_exp_f32 v205, v205\n"
    "v_exp_f32 v206, v206\n"
    "v_exp_f32 v207, v207\n"
    "v_pk_add_f32 v[200:201], v[200:201], 1.0 op_sel_hi:[1,0]\n"
    "v_pk_add_f32 v[202:203], v[202:203], 1.0 op_sel_hi:[1,0]\n"
    "v_pk_add_f32 v[204:205], v[204:205], 1.0 op_sel_hi:[1,0]\n"
    "v_pk_add_f32 v[206:207], v[206:207], 1.0 op_sel_hi:[1,0]\n"
    "s_waitcnt vmcnt(0)\n"
    "v_max3_u32 v188, v184, v185, v186\n"
    "v_max_u32 v188, v188, v187\n"
    "v_cmp_eq_u32 vcc, -1, v188\n"
    "s_cbranch_vccz LL7ok_%=\n"
    "s_mov_b32 s43, 0x100000\n"
    "LL7lp_%=:\n"
    "buffer_load_dwordx4 v[184:187], v22, s[16:19], s25 offen sc1\n"
    "s_waitcnt vmcnt(0)\n"
    "s_sub_u32 s43, s43, 1\n"
    "s_cbranch_scc1 LL7ok_%=\n"
    "v_max3_u32 v188, v184, v185, v186\n"
    "v_max_u32 v188, v188, v187\n"
    "v_cmp_eq_u32 vcc, -1, v188\n"
    "s_cbranch_vccnz LL7lp_%=\n"
    "LL7ok_%=:\n"
    "v_lshlrev_b32 v188, 16, v184\n"
    "v_and_b32 v189, s48, v184\n"
    "v_lshlrev_b32 v190, 16, v185\n"
    "v_and_b32 v191, s48, v185\n"
    "v_lshlrev_b32 v192, 16, v186\n"
    "v_and_b32 v193, s48, v186\n"
    "v_lshlrev_b32 v194, 16, v187\n"
    "v_and_b32 v195, s48, v187\n"
    "v_pk_add_f32 v[40:41], v[40:41], v[188:189]\n"
    "v_pk_add_f32 v[42:43], v[42:43], v[190:191]\n"
    "v_pk_add_f32 v[44:45], v[44:45], v[192:193]\n"
    "v_pk_add_f32 v[46:47], v[46:47], v[194:195]\n"
    "v_pk_mul_f32 v[240:241], v[40:41], s[46:47] op_sel_hi:[1,0]\n"
    "v_pk_mul_f32 v[242:243], v[42:43], s[46:47] op_sel_hi:[1,0]\n"
    "v_pk_mul_f32 v[244:245], v[44:45], s[46:47] op_sel_hi:[1,0]\n"
    "v_pk_mul_f32 v[246:247], v[46:47], s[46:47] op_sel_hi:[1,0]\n"
    "v_exp_f32 v240, -|v240|\n"
    "v_fma_f32 v248, -v40, v40, |v40|\n"
    "v_exp_f32 v241, -|v241|\n"
    "v_fma_f32 v249, -v41, v41, |v41|\n"
    "v_exp_f32 v242, -|v242|\n"
    "v_fma_f32 v250, -v42, v42, |v42|\n"
    "v_exp_f32 v243, -|v243|\n"
    "v_fma_f32 v251, -v43, v43, |v43|\n"
    "v_exp_f32 v244, -|v244|\n"
    "v_fma_f32 v252, -v44, v44, |v44|\n"
    "v_exp_f32 v245, -|v245|\n"
    "v_fma_f32 v253, -v45, v45, |v45|\n"
    "v_exp_f32 v246, -|v246|\n"
    "v_fma_f32 v254, -v46, v46, |v46|\n"
    "v_exp_f32 v247, -|v247|\n"
    "v_fma_f32 v255, -v47, v47, |v47|\n"
    "v_pk_fma_f32 v[168:169], v[240:241], 0.5, 0.5 op_sel_hi:[1,0,0] neg_lo:[1,0,0] neg_hi:[1,0,0]\n"
    "v_pk_fma_f32 v[170:171], v[242:243], 0.5, 0.5 op_sel_hi:[1,0,0] neg_lo:[1,0,0] neg_hi:[1,0,0]\n"
    "v_pk_fma_f32 v[172:173], v[244:245], 0.5, 0.5 op_sel_hi:[1,0,0] neg_lo:[1,0,0] neg_hi:[1,0,0]\n"
    "v_pk_fma_f32 v[174:175], v[246:247], 0.5, 0.5 op_sel_hi:[1,0,0] neg_lo:[1,0,0] neg_hi:[1,0,0]\n"
    "v_pk_fma_f32 v[240:241], v[240:241], 0.5, 0.5 op_sel_hi:[1,0,0]\n"
    "v_pk_fma_f32 v[242:243], v[242:243], 0.5, 0.5 op_sel_hi:[1,0,0]\n"
    "v_pk_fma_f32 v[244:245], v[244:245], 0.5, 0.5 op_sel_hi:[1,0,0]\n"
    "v_pk_fma_f32 v[246:247], v[246:247], 0.5, 0.5 op_sel_hi:[1,0,0]\n"
    "v_med3_f32 v248, v168, v248, |v40|\n"
    "v_med3_f32 v249, v169, v249, |v41|\n"
    "v_med3_f32 v250, v170, v250, |v42|\n"
    "v_med3_f32 v251, v171, v251, |v43|\n"
    "v_med3_f32 v252, v172, v252, |v44|\n"
    "v_med3_f32 v253, v173, v253, |v45|\n"
    "v_med3_f32 v254, v174, v254, |v46|\n"
    "v_med3_f32 v255, v175, v255, |v47|\n"
    "v_bfi_b32 v248, s47, v248, v40\n"
    "v_bfi_b32 v249, s47, v249, v41\n"
    "v_bfi_b32 v250, s47, v250, v42\n"
    "v_bfi_b32 v251, s47, v251, v43\n"
    "v_bfi_b32 v252, s47, v252, v44\n"
    "v_bfi_b32 v253, s47, v253, v45\n"
    "v_bfi_b32 v254, s47, v254, v46\n"
    "v_bfi_b32 v255, s47, v255, v47\n"
    "v_pk_mul_f32 v[240:241], v[240:241], v[200:201]\n"
    "v_pk_mul_f32 v[242:243], v[242:243], v[202:203]\n"
    "v_pk_mul_f32 v[244:245], v[244:245], v[204:205]\n"
    "v_pk_mul_f32 v[246:247], v[246:247], v[206:207]\n"
    "v_rcp_f32 v240, v240\n"
    "v_rcp_f32 v241, v241\n"
    "v_rcp_f32 v242, v242\n"
    "v_rcp_f32 v243, v243\n"
    "v_rcp_f32 v244, v244\n"
    "v_rcp_f32 v245, v245\n"
    "v_rcp_f32 v246, v246\n"
    "v_rcp_f32 v247, v247\n"
    "v_pk_mul_f32 v[248:249], v[248:249], v[240:241]\n"
    "v_pk_mul_f32 v[250:251], v[250:251], v[242:243]\n"
    "v_pk_mul_f32 v[252:253], v[252:253], v[244:245]\n"
    "v_pk_mul_f32 v[254:255], v[254:255], v[246:247]\n"
    "v_cvt_pk_bf16_f32 v240, v248, v249\n"
    "v_cvt_pk_bf16_f32 v241, v250, v251\n"
    "v_cvt_pk_bf16_f32 v242, v252, v253\n"
    "v_cvt_pk_bf16_f32 v243, v254, v255\n"
    "buffer_store_dwordx2 v[240:241], v25, s[20:23], 0 offen\n"
    "buffer_store_dwordx2 v[242:243], v25, s[20:23], 0 offen offset:2048\n"
    "s_endpgm\n"
    ".fill (LV0_%=+49152-.)/4, 4, 0xbf800000\n"
    "LEND_%=:\n"
    :
    : [ka] "s"(__builtin_amdgcn_kernarg_segment_ptr()), [bid] "s"(bid), [tid] "v"(tid)
    : "memory", "scc", "vcc", "s3", "s4", "s5", "s6", "s7", "s8", "s9", "s10", "s11", "s12", "s13", "s14", "s15", "s16", "s17", "s18", "s19", "s20", "s21", "s22", "s23", "s24", "s25", "s26", "s27", "s28", "s29", "s30", "s31", "s33", "s34", "s35", "s36", "s37", "s38", "s39", "s40", "s41", "s42", "s43", "s44", "s45", "s46", "s47", "s48", "s49", "s50", "s51", "s52", "s53", "s54", "s55", "s56", "s57", "s58", "s59", "s60", "s61", "s62", "s63", "s64", "s65", "s66", "s67", "s68", "s69", "s70", "s71", "s72", "s73", "s74", "s75", "v1", "v2", "v3", "v4", "v5", "v6", "v7", "v8", "v9", "v10", "v11", "v12", "v13", "v14", "v15", "v16", "v17", "v18", "v19", "v20", "v21", "v22", "v23", "v24", "v25", "v26", "v27", "v28", "v29", "v30", "v31", "v32", "v33", "v34", "v35", "v36", "v37", "v38", "v39", "v40", "v41", "v42", "v43", "v44", "v45", "v46", "v47", "v48", "v49", "v50", "v51", "v52", "v53", "v54", "v55", "v56", "v57", "v58", "v59", "v60", "v61", "v62", "v63", "v64", "v65", "v66", "v67", "v68", "v69", "v70", "v71", "v72", "v73", "v74", "v75", "v76", "v77", "v78", "v79", "v80", "v81", "v82", "v83", "v84", "v85", "v86", "v87", "v88", "v89", "v90", "v91", "v92", "v93", "v94", "v95", "v96", "v97", "v98", "v99", "v100", "v101", "v102", "v103", "v104", "v105", "v106", "v107", "v108", "v109", "v110", "v111", "v112", "v113", "v114", "v115", "v116", "v117", "v118", "v119", "v120", "v121", "v122", "v123", "v124", "v125", "v126", "v127", "v128", "v129", "v130", "v131", "v132", "v133", "v134", "v135", "v136", "v137", "v138", "v139", "v140", "v141", "v142", "v143", "v144", "v145", "v146", "v147", "v148", "v149", "v150", "v151", "v152", "v153", "v154", "v155", "v156", "v157", "v158", "v159", "v160", "v161", "v162", "v163", "v164", "v165", "v166", "v167", "v168", "v169", "v170", "v171", "v172", "v173", "v174", "v175", "v176", "v177", "v178", "v179", "v180", "v181", "v182", "v183", "v184", "v185", "v186", "v187", "v188", "v189", "v190", "v191", "v192", "v193", "v194", "v195", "v196", "v197", "v198", "v199", "v200", "v201", "v202", "v203", "v204", "v205", "v206", "v207", "v208", "v209", "v210", "v211", "v212", "v213", "v214", "v215", "v216", "v217", "v218", "v219", "v220", "v221", "v222", "v223", "v224", "v225", "v226", "v227", "v228", "v229", "v230", "v231", "v232", "v233", "v234", "v235", "v236", "v237", "v238", "v239", "v240", "v241", "v242", "v243", "v244", "v245", "v246", "v247", "v248", "v249", "v250", "v251", "v252", "v253", "v254", "v255", "a0", "a1", "a2", "a3", "a4", "a5", "a6", "a7", "a8", "a9", "a10", "a11", "a12", "a13", "a14", "a15", "a16", "a17", "a18", "a19", "a20", "a21", "a22", "a23", "a24", "a25", "a26", "a27", "a28", "a29", "a30", "a31", "a32", "a33", "a34", "a35", "a36", "a37", "a38", "a39", "a40", "a41", "a42", "a43", "a44", "a45", "a46", "a47", "a48", "a49", "a50", "a51", "a52", "a53", "a54", "a55", "a56", "a57", "a58", "a59", "a60", "a61", "a62", "a63", "a64", "a65", "a66", "a67", "a68", "a69", "a70", "a71", "a72", "a73", "a74", "a75", "a76", "a77", "a78", "a79", "a80", "a81", "a82", "a83", "a84", "a85", "a86", "a87", "a88", "a89", "a90", "a91", "a92", "a93", "a94", "a95", "a96", "a97", "a98", "a99", "a100", "a101", "a102", "a103", "a104", "a105", "a106", "a107", "a108", "a109", "a110", "a111", "a112", "a113", "a114", "a115", "a116", "a117", "a118", "a119", "a120", "a121", "a122", "a123", "a124", "a125", "a126", "a127", "a128", "a129", "a130", "a131", "a132", "a133", "a134", "a135", "a136", "a137", "a138", "a139", "a140", "a141", "a142", "a143", "a144", "a145", "a146", "a147", "a148", "a149", "a150", "a151", "a152", "a153", "a154", "a155", "a156", "a157", "a158", "a159", "a160", "a161", "a162", "a163", "a164", "a165", "a166", "a167", "a168", "a169", "a170", "a171", "a172", "a173", "a174", "a175", "a176", "a177", "a178", "a179", "a180", "a181", "a182", "a183", "a184", "a185", "a186", "a187", "a188", "a189", "a190", "a191", "a192", "a193", "a194", "a195", "a196", "a197", "a198", "a199", "a200", "a201", "a202", "a203", "a204", "a205", "a206", "a207", "a208", "a209", "a210", "a211", "a212", "a213", "a214", "a215", "a216", "a217", "a218", "a219", "a220", "a221", "a222", "a223", "a224", "a225", "a226", "a227", "a228", "a229", "a230", "a231", "a232", "a233", "a234", "a235", "a236", "a237", "a238", "a239", "a240", "a241", "a242", "a243", "a244", "a245", "a246", "a247", "a248", "a249", "a250", "a251", "a252", "a253", "a254", "a255");
}

extern "C" void kernel_launch(void* const* d_in, const int* in_sizes, int n_in, void* d_out, int out_size, void* d_ws, size_t ws_size,
                              hipStream_t stream) {
  (void)in_sizes; (void)n_in; (void)out_size; (void)ws_size;
  (void)hipFuncSetAttribute((const void*)conv_gate_kernel, hipFuncAttributeMaxDynamicSharedMemorySize, LDS_BYTES);
  conv_gate_kernel<<<256, 256, LDS_BYTES, stream>>>(d_in[0], d_in[1], d_out, d_ws);
}
```
